```python
import torch

def _softmax(x, axis=-1):
    return torch.softmax(x, dim=axis)
def _gelu(x):
    return torch.nn.functional.gelu(x, approximate="tanh")
def _silu(x):
    return torch.nn.functional.silu(x)
def _sigmoid(x):
    return torch.sigmoid(x)
def _layernorm(x, axis=-1, eps=1e-5):
    m = x.mean(dim=axis, keepdim=True)
    v = x.var(dim=axis, keepdim=True, unbiased=False)
    return (x - m) / torch.sqrt(v + eps)
def _scatter_add(tgt, idx, src, axis=0):
    return tgt.clone().index_add_(axis, idx, src)
def _segment_sum(src, seg, n):
    out = torch.zeros((n,) + src.shape[1:], dtype=src.dtype)
    return out.index_add_(0, seg, src)
def _pad(x, pw):
    flat = tuple(v for pair in reversed(pw) for v in pair)
    return torch.nn.functional.pad(x, flat)

kernel_name = 'recurrent_scan__mha_block__glu_gated_glu__prompt'

def setup_inputs():

    _w = (lambda a: (lambda t: t.to(torch.bfloat16) if t.is_floating_point() else t)(torch.as_tensor(a)))
    def _A_setup():
        B, S, D, H = 1, 2048, 448, 16
        return {
            "x": _w((lambda *s: torch.randn(*s, dtype=torch.bfloat16) * 0.05)(B, S, D)),
            "wqkv": _w((lambda *s: torch.randn(*s, dtype=torch.bfloat16) * 0.05)(D, 3 * D)),
            "wo": _w((lambda *s: torch.randn(*s, dtype=torch.bfloat16) * 0.05)(D, D)),
        }
    _A_in = _A_setup()

    def _B_setup():
        B, S, D, F = 1, 2048, 448, 1792
        return {
            "x": _w((lambda *s: torch.randn(*s, dtype=torch.bfloat16) * 0.05)(B, S, D)),
            "w_gate": _w((lambda *s: torch.randn(*s, dtype=torch.bfloat16) * 0.05)(D, F)),
            "w_up": _w((lambda *s: torch.randn(*s, dtype=torch.bfloat16) * 0.05)(D, F)),
            "w_down": _w((lambda *s: torch.randn(*s, dtype=torch.bfloat16) * 0.05)(F, D)),
        }
    _B_in = _B_setup()

    return {
        "A_x": _A_in["x"],
        "A_wqkv": _A_in["wqkv"],
        "A_wo": _A_in["wo"],
        "B_x": _B_in["x"],
        "B_w_gate": _B_in["w_gate"],
        "B_w_up": _B_in["w_up"],
        "B_w_down": _B_in["w_down"],
    }

def reference(A_x, A_wqkv, A_wo, B_x, B_w_gate, B_w_up, B_w_down):
    def _A_ref(x, wqkv, wo):
        B, S, D = x.shape
        H = 16
        Dh = D // H
        qkv = (lambda x, *d: x.permute(*d))((x @ wqkv).reshape(B, S, 3, H, Dh), 2, 0, 3, 1, 4)
        q, k, v = qkv[0], qkv[1], qkv[2]
        scores = torch.einsum("bhqd,bhkd->bhqk", q, k) * (Dh ** -0.5)
        p = _softmax(scores, axis=-1)
        o = torch.einsum("bhqk,bhkd->bhqd", p, v)
        return (lambda x, *d: x.permute(*d))(o, 0, 2, 1, 3).reshape(B, S, D) @ wo

    def _B_ref(x, w_gate, w_up, w_down):
        g = torch.einsum("bsd,df->bsf", x, w_gate)
        u = torch.einsum("bsd,df->bsf", x, w_up)
        h = _sigmoid(g) * u
        return torch.einsum("bsf,fd->bsd", h, w_down)

    x = A_x
    xn = _layernorm(x, axis=-1)
    s = _A_ref(xn, A_wqkv, A_wo)
    g = _silu(_B_ref(xn, B_w_gate, B_w_up, B_w_down))
    return x + s * g
```

```cpp
#include <hip/hip_runtime.h>
#include <hip/hip_bf16.h>
typedef __hip_bfloat16 bf16;
namespace nv {
constexpr int S = 2048, D = 448, H = 16, DH = 28, F = 1792;
__device__ __forceinline__ float b2f(bf16 v) { return __bfloat162float(v); }
__device__ __forceinline__ float rb(float v) { return __bfloat162float(__float2bfloat16(v)); }
__global__ void ln_k(const bf16* x, float* xn) {
  int r = blockIdx.x; __shared__ float red[64];
  float s = 0; for (int i = threadIdx.x; i < D; i += 64) s += b2f(x[r * D + i]);
  for (int o = 32; o; o >>= 1) s += __shfl_xor(s, o);
  float m = s / D; float v = 0; for (int i = threadIdx.x; i < D; i += 64) { float d = b2f(x[r * D + i]) - m; v += d * d; }
  for (int o = 32; o; o >>= 1) v += __shfl_xor(v, o);
  v /= D; float mb = rb(m), sd = rb(sqrtf(rb(rb(v) + 1e-5f)));
  for (int i = threadIdx.x; i < D; i += 64) xn[r * D + i] = rb(rb(b2f(x[r * D + i]) - mb) / sd);
}
__global__ void gemm_k(const float* A, const bf16* W, float* C, int K, int N) {
  int n = blockIdx.x * 64 + threadIdx.x, m0 = blockIdx.y * 8;
  float acc[8] = {};
  for (int k = 0; k < K; ++k) { float w = b2f(W[k * N + n]); for (int i = 0; i < 8; ++i) acc[i] += A[(m0 + i) * K + k] * w; }
  for (int i = 0; i < 8; ++i) C[(m0 + i) * N + n] = rb(acc[i]);
}
__global__ void attn_k(const float* qkv, float* o) {
  int q = blockIdx.x * 64 + threadIdx.x, h = blockIdx.y;
  float qv[DH], acc[DH] = {}; for (int d = 0; d < DH; ++d) qv[d] = qkv[q * 1344 + h * DH + d];
  float m = -1e30f, l = 0; const float sc = 0.1889822365f;
  for (int k = 0; k < S; ++k) { float s = 0; for (int d = 0; d < DH; ++d) s += qv[d] * qkv[k * 1344 + 448 + h * DH + d]; s *= sc;
    float mn = fmaxf(m, s), a = __expf(m - mn), p = __expf(s - mn); l = l * a + p; for (int d = 0; d < DH; ++d) acc[d] = acc[d] * a + p * qkv[k * 1344 + 896 + h * DH + d]; m = mn; }
  for (int d = 0; d < DH; ++d) o[q * D + h * DH + d] = rb(acc[d] / l);
}
__global__ void glu_k(const float* g, const float* u, float* h, int n) { int i = blockIdx.x * 256 + threadIdx.x; if (i < n) h[i] = rb(rb(1.f / (1.f + __expf(-g[i]))) * u[i]); }
__global__ void fin_k(const bf16* x, const float* s, const float* ff, bf16* out, int n) { int i = blockIdx.x * 256 + threadIdx.x; if (i < n) { float f = ff[i]; float g = rb(f / (1.f + __expf(-f))); out[i] = __float2bfloat16(b2f(x[i]) + rb(s[i] * g)); } }
static void run(void* const* d_in, void* d_out, void* d_ws, hipStream_t stream) {
  const bf16* x = (const bf16*)d_in[0]; const bf16* wqkv = (const bf16*)d_in[1]; const bf16* wo = (const bf16*)d_in[2];
  const bf16* wg = (const bf16*)d_in[4]; const bf16* wu = (const bf16*)d_in[5]; const bf16* wd = (const bf16*)d_in[6];
  float* w = (float*)d_ws; float* xn = w; float* qkv = xn + S * D; float* o = qkv + S * 1344; float* s = o + S * D; float* g = s + S * D; float* u = g + S * F; float* h = u + S * F; float* ff = h + S * F;
  ln_k<<<S, 64, 0, stream>>>(x, xn);
  gemm_k<<<dim3(1344 / 64, S / 8), 64, 0, stream>>>(xn, wqkv, qkv, D, 1344);
  attn_k<<<dim3(S / 64, H), 64, 0, stream>>>(qkv, o);
  gemm_k<<<dim3(D / 64, S / 8), 64, 0, stream>>>(o, wo, s, D, D);
  gemm_k<<<dim3(F / 64, S / 8), 64, 0, stream>>>(xn, wg, g, D, F);
  gemm_k<<<dim3(F / 64, S / 8), 64, 0, stream>>>(xn, wu, u, D, F);
  glu_k<<<S * F / 256, 256, 0, stream>>>(g, u, h, S * F);
  gemm_k<<<dim3(D / 64, S / 8), 64, 0, stream>>>(h, wd, ff, F, D);
  fin_k<<<S * D / 256, 256, 0, stream>>>(x, s, ff, (bf16*)d_out, S * D);
}
}
extern "C" void kernel_launch(void* const* d_in, const int* in_sizes, int n_in, void* d_out, int out_size, void* d_ws, size_t ws_size, hipStream_t stream) {
  nv::run(d_in, d_out, d_ws, stream);
}
```

```cpp
#include <hip/hip_runtime.h>
#include <hip/hip_bf16.h>
#include <stdint.h>

typedef short bf16x8 __attribute__((ext_vector_type(8)));
typedef short s16x4 __attribute__((ext_vector_type(4)));
typedef float f32x16 __attribute__((ext_vector_type(16)));
typedef float f32x4 __attribute__((ext_vector_type(4)));
typedef float f32x2 __attribute__((ext_vector_type(2)));
typedef unsigned u32x4 __attribute__((ext_vector_type(4)));
typedef unsigned u32x2 __attribute__((ext_vector_type(2)));
typedef __bf16 bf2 __attribute__((ext_vector_type(2)));
#define LAS __attribute__((address_space(3)))
typedef LAS char* lptr;
#define DI __device__ __forceinline__
#define MFMA(a, b, c) __builtin_amdgcn_mfma_f32_32x32x16_bf16((a), (b), (c), 0, 0, 0)
#define PIN(x) asm volatile("" : "+v"(x))
#define SBAR() __builtin_amdgcn_sched_barrier(0)
#define WAIT_BAR(N) asm volatile("s_waitcnt vmcnt(" #N ") lgkmcnt(0)\n\ts_barrier" ::: "memory")
#define LBAR() asm volatile("s_waitcnt lgkmcnt(0)\n\ts_barrier" ::: "memory")

constexpr int S_ = 2048, D_ = 448, F_ = 1792;
constexpr size_t WS_Q = 0;
constexpr size_t WS_KV = WS_Q + (2u << 20);
constexpr size_t WS_H = WS_KV + (4u << 20);
constexpr size_t WS_O = WS_H + (size_t)64 * 112 * 1024;
constexpr size_t WS_FF = WS_O + (2u << 20);
constexpr size_t WS_END = WS_FF + (size_t)2 * 64 * 14 * 4096;

#define TS(k, i) do { } while (0)

DI unsigned pk(float a, float b) { f32x2 v = {a, b}; return __builtin_bit_cast(unsigned, __builtin_convertvector(v, bf2)); }
DI float rbf(float a) { return __uint_as_float(pk(a, a) & 0xffff0000u); }
DI float dot2(unsigned a, unsigned b, float c) { return __builtin_amdgcn_fdot2_f32_bf16(__builtin_bit_cast(bf2, a), __builtin_bit_cast(bf2, b), c, false); }
DI u32x2 trd(lptr p) { return __builtin_bit_cast(u32x2, __builtin_amdgcn_ds_read_tr16_b64_v4i16((LAS s16x4*)p)); }
DI bf16x8 frag(u32x2 a, u32x2 b) { u32x4 v = {a[0], a[1], b[0], b[1]}; return __builtin_bit_cast(bf16x8, v); }
DI bf16x8 frag4(unsigned a, unsigned b, unsigned c, unsigned d) { u32x4 v = {a, b, c, d}; return __builtin_bit_cast(bf16x8, v); }
DI u32x2 ld64(lptr p) { return *(LAS u32x2*)p; }
DI u32x4 ld128(lptr p) { return *(LAS u32x4*)p; }
DI void glds16(const void* sbase, unsigned voff, unsigned lds_dst) {
  asm volatile("s_mov_b32 m0, %2\n\ts_nop 0\n\tglobal_load_lds_dwordx4 %0, %1" :: "v"(voff), "s"(sbase), "s"(lds_dst) : "memory");
}
DI float swap32(float v) {
  auto r = __builtin_amdgcn_permlane32_swap(__float_as_uint(v), __float_as_uint(v), false, false);
  return __uint_as_float((threadIdx.x & 32) ? r[0] : r[1]);
}

constexpr int SLOT = 28672, PART = 14336;
constexpr int K1_CS = 5 * SLOT;
constexpr int K1_ST = K1_CS + 2048;
constexpr int K1_RS = K1_ST + 4096;
constexpr int K1_LDS = K1_RS + 512;
constexpr unsigned ONES2 = 0x3f803f80u;

template <bool VB>
DI void k1_body(const char* x, const char* wqkv, const char* wg, const char* wu, char* ws, lptr lds, int mg, int ng) {
  const int tid = threadIdx.x, lane = tid & 63, wid = __builtin_amdgcn_readfirstlane(tid >> 6), wm = wid & 3, wn = wid >> 2;
  const int r = lane & 31, h = lane >> 5, q = (lane & 15) >> 2, p = lane & 3, grp = (lane >> 4) & 1;
  const unsigned lds0 = (unsigned)(uintptr_t)lds;
  const int rr = lane >> 3, cc = lane & 7;
  const unsigned voff_q = (unsigned)((28 * wm + rr) * 2688 + ((cc ^ (((rr >> 1) & 1) << 2)) << 4));
  const unsigned voff_g = VB ? voff_q : (unsigned)((28 * wm + rr) * 3584 + ((cc ^ (((rr >> 1) & 1) << 1) ^ (wn << 2)) << 4));
  constexpr int ST01 = VB ? 2688 : 3584;
  const char* src0; const char* src1; const char* src2;
  if (VB) { const int l0 = (ng == 14) ? 14 : 18; src0 = wqkv + (l0 + wn) * 128; src1 = wqkv + (16 + wn) * 128; src2 = wqkv + 20 * 128 + wn * 112 * 2688; }
  else { const char* w = wn ? wu : wg; src0 = w + ng * 256; src1 = src0 + 128; src2 = wqkv + ng * 128 + wn * 112 * 2688; }
  const unsigned wdst = lds0 + wn * PART + wm * 3584;
#define K1_DMA(i) do { constexpr int sl_ = ((i) + 4) % 5; constexpr int st_ = (i) < 8 ? ST01 : 2688;                         \
    const char* sb_ = (i) < 4 ? src0 + (i) * 112 * ST01 : (i) < 8 ? src1 + ((i) - 4) * 112 * ST01 : src2 + ((i) - 8) * 224 * 2688; \
    const unsigned vo_ = (i) < 8 ? voff_g : voff_q;                                                                           \
    glds16(sb_, vo_, wdst + sl_ * SLOT); glds16(sb_ + 8 * st_, vo_, wdst + sl_ * SLOT + 1024); glds16(sb_ + 16 * st_, vo_, wdst + sl_ * SLOT + 2048); \
    if (lane < 32) glds16(sb_ + 24 * st_, vo_, wdst + sl_ * SLOT + 3072); } while (0)
  TS(0, 0);
  K1_DMA(0);
  {
    const char* xs = x + (size_t)(mg * 128 + wid * 16) * 896;
    const unsigned vo0 = (unsigned)(rr * 896 + ((cc ^ rr) << 4));
    const unsigned vo1 = (unsigned)((8 + (rr ^ 1)) * 896 + ((cc ^ (rr ^ 1)) << 4));
#pragma unroll
    for (int ln = 0; ln < 7; ++ln) { glds16(xs + ln * 128, vo0, lds0 + (ln * 16 + wid * 2) * 1024); glds16(xs + ln * 128, vo1, lds0 + (ln * 16 + wid * 2 + 1) * 1024); }
  }
  WAIT_BAR(0);
  TS(0, 1);
  u32x2 xa[28], xb[28];
  {
    const int j = r & 7, rg = 4 * wm + (r >> 3), i = j ^ (rg & 1), t0 = wm >> 1;
    const lptr xb0 = lds + rg * 1024 + i * 128;
#pragma unroll
    for (int ks = 0; ks < 28; ++ks) {
      const lptr a = xb0 + (ks >> 2) * 16384 + (((((ks & 3) << 1) + h) ^ j) << 4);
      xa[ks] = *(volatile LAS u32x2*)(a + t0 * 8); xb[ks] = *(volatile LAS u32x2*)(a + (t0 ^ 1) * 8);
    }
  }
  LBAR();
  K1_DMA(1); K1_DMA(2); K1_DMA(3); K1_DMA(4);
  {
    float s1a = 0.f, s1b = 0.f, s2a = 0.f, s2b = 0.f;
#define K1_STAT(k0) _Pragma("unroll") for (int ks = (k0); ks < (k0) + 14; ++ks) {                                             \
      s1a = dot2(xa[ks][0], ONES2, s1a); s2a = dot2(xa[ks][0], xa[ks][0], s2a); s1b = dot2(xa[ks][1], ONES2, s1b); s2b = dot2(xa[ks][1], xa[ks][1], s2b); \
      s1a = dot2(xb[ks][0], ONES2, s1a); s2a = dot2(xb[ks][0], xb[ks][0], s2a); s1b = dot2(xb[ks][1], ONES2, s1b); s2b = dot2(xb[ks][1], xb[ks][1], s2b); }
    if (wn == 0) { K1_STAT(0) } else { K1_STAT(14) }
    f32x2 st = {s1a + s1b, s2a + s2b};
    *(LAS f32x2*)(lds + K1_ST + (wm * 32 + r) * 32 + (wn * 2 + h) * 8) = st;
  }
  const int t0 = wm >> 1, lt0 = 2 * wn + (wm & 1), lt1 = 2 * wn + ((wm & 1) ^ 1);
  const int rowb = (8 * h + 4 * t0 + q) * 128 + p * 8;
  int b40, b41;
  if (VB) { b40 = wn * PART + rowb + (((2 * (lt0 & 1) + grp) ^ ((q >> 1) << 1)) << 5); b41 = wn * PART + rowb + (((2 * (lt1 & 1) + grp) ^ ((q >> 1) << 1)) << 5); }
  else { b40 = grp * PART + rowb + ((lt0 ^ (q >> 1) ^ (grp << 1)) << 5); b41 = grp * PART + rowb + ((lt1 ^ (q >> 1) ^ (grp << 1)) << 5); }
  const int b2 = rowb + (((2 * wn + grp) ^ ((q >> 1) << 1)) << 5);
  f32x16 A0 = {}, A1 = {}, B0 = {}, B1 = {};
  float cs = 0.f, rstd = 0.f, sig = 0.f; unsigned mm = 0;
#define K1_MM(acc, wf, ks) acc = VB ? MFMA(frag(xa[ks], xb[ks]), wf, acc) : MFMA(wf, frag(xa[ks], xb[ks]), acc)
#define K1_SUB4(i, C0, C1) do { const lptr sl_ = lds + (((i) + 4) % 5) * SLOT;                                                 \
    _Pragma("unroll") for (int k = 0; k < 7; ++k) { const int ks = ((i) & 3) * 7 + k;                                          \
      const u32x2 w0 = trd(sl_ + b40 + k * 2048), w1 = trd(sl_ + (b40 ^ 512) + k * 2048);                                     \
      const u32x2 w2 = trd(sl_ + b41 + k * 2048), w3 = trd(sl_ + (b41 ^ 512) + k * 2048);                                     \
      K1_MM(C0, frag(w0, w1), ks); K1_MM(C1, frag(w2, w3), ks);                                                               \
      cs = dot2(w0[0], ONES2, cs); cs = dot2(w0[1], ONES2, cs); PIN(cs); } } while (0)
#define K1_SUB2(i, C0) do { const lptr sl_ = lds + (((i) + 4) % 5) * SLOT;                                                     \
    _Pragma("unroll") for (int k = 0; k < 14; ++k) { const int ks = ((i) & 1) * 14 + k;                                        \
      const u32x2 w0 = trd(sl_ + b2 + k * 2048), w1 = trd(sl_ + (b2 ^ 512) + k * 2048);                                       \
      K1_MM(C0, frag(w0, w1), ks);                                                                                            \
      cs = dot2(w0[0], ONES2, cs); cs = dot2(w0[1], ONES2, cs); PIN(cs); } } while (0)
#define K1_CSW(T) do { const float hi_ = rbf(cs); *(LAS unsigned*)(lds + K1_CS + (T) * 512 + lane * 8 + t0 * 4) = pk(hi_, cs - hi_); cs = 0.f; } while (0)
#define K1_FIX(acc, T) do { const u32x2 c_ = ld64(lds + K1_CS + (T) * 512 + lane * 8);                                         \
    acc = VB ? MFMA(frag4(mm, mm, 0, 0), frag4(c_[0], c_[1], 0, 0), acc) : MFMA(frag4(c_[0], c_[1], 0, 0), frag4(mm, mm, 0, 0), acc); } while (0)
  const int mt = mg * 4 + wm;
  auto epi_gu = [&](f32x16& acc, int kstep) {
    const float kg = -1.4426950408889634f * rstd; float hv[8];
#pragma unroll
    for (int i = 0; i < 8; ++i) { const float e = __builtin_amdgcn_exp2f(acc[i] * kg); hv[i] = acc[i + 8] * __builtin_amdgcn_rcpf(__builtin_fmaf(e, sig, sig)); }
    u32x4 o = {pk(hv[0], hv[1]), pk(hv[2], hv[3]), pk(hv[4], hv[5]), pk(hv[6], hv[7])};
    *(u32x4*)(ws + WS_H + ((size_t)mt * 112 + kstep) * 1024 + lane * 16) = o;
  };
  auto epi_v = [&](f32x16& acc, int col0) {
    const int c = col0 + r, hd = (c * 2341) >> 16, d = c - hd * 28;
    char* dst = ws + WS_KV + ((size_t)hd * 64 + mt) * 4096 + 2048 + (h * 32 + d) * 16;
    unsigned o[8];
#pragma unroll
    for (int g = 0; g < 4; ++g) { const f32x4 rs = *(LAS f32x4*)(lds + K1_RS + (wm * 32 + 8 * g + 4 * h) * 4);
      o[2 * g] = pk(acc[4 * g] * rs[0], acc[4 * g + 1] * rs[1]); o[2 * g + 1] = pk(acc[4 * g + 2] * rs[2], acc[4 * g + 3] * rs[3]); }
    *(u32x4*)dst = (u32x4){o[0], o[1], o[2], o[3]}; *(u32x4*)(dst + 1024) = (u32x4){o[4], o[5], o[6], o[7]};
    if (d >= 24) { const unsigned v = d == 24 ? ONES2 : 0u; *(u32x4*)(dst + 64) = (u32x4){v, v, v, v}; *(u32x4*)(dst + 1024 + 64) = (u32x4){v, v, v, v}; }
  };
  auto epi_qk = [&](f32x16& acc, int col0) {
    const bool isq = col0 < 448; const float sc = isq ? rstd * (0.18898223650461362f * 1.4426950408889634f) : rstd;
    char* base = ws + (isq ? WS_Q : WS_KV) + r * 8; const int sh = isq ? 11 : 12;
#pragma unroll
    for (int g = 0; g < 4; ++g) { const int c4 = ((isq ? col0 : col0 - 448) >> 2) + 2 * g + h, hd = (c4 * 9363) >> 16, qd = c4 - hd * 7;
      char* dst = base + ((size_t)(hd * 64 + mt) << sh) + qd * 256;
      *(u32x2*)dst = (u32x2){pk(acc[4 * g] * sc, acc[4 * g + 1] * sc), pk(acc[4 * g + 2] * sc, acc[4 * g + 3] * sc)};
      if (!isq && qd == 6) *(u32x2*)(dst + 256) = (u32x2){ONES2, 0x00003f80u}; }
  };
#define K1_EPI4(ph, C0, C1) do { K1_FIX(C0, lt0); K1_FIX(C1, lt1);                                                             \
    if (VB) { const int c0_ = ((ph) ? 2 : (ng == 14 ? 0 : 4)) * 64; epi_v(C0, c0_ + lt0 * 32); epi_v(C1, c0_ + lt1 * 32); }    \
    else { epi_gu(C0, ng * 8 + (ph) * 4 + lt0); epi_gu(C1, ng * 8 + (ph) * 4 + lt1); } } while (0)

  K1_SUB4(0, A0, A1); WAIT_BAR(12); K1_DMA(5);
  TS(0, 2);
  {
    const f32x4 a = *(LAS f32x4*)(lds + K1_ST + (wm * 32 + r) * 32), b = *(LAS f32x4*)(lds + K1_ST + (wm * 32 + r) * 32 + 16);
    const float mean = ((a[0] + a[2]) + (b[0] + b[2])) * (1.f / 448.f);
    const float var = __builtin_fmaxf(((a[1] + a[3]) + (b[1] + b[3])) * (1.f / 448.f) - mean * mean, 0.f);
    sig = rbf(__builtin_amdgcn_sqrtf(rbf(rbf(var) + 1e-5f))); rstd = __builtin_amdgcn_rcpf(sig); mm = pk(-mean, -mean);
    if (VB) *(LAS float*)(lds + K1_RS + (wm * 32 + r) * 4) = rstd;
  }
  K1_SUB4(1, A0, A1); WAIT_BAR(12); K1_DMA(6);
  K1_SUB4(2, A0, A1); WAIT_BAR(12); K1_DMA(7);
  K1_SUB4(3, A0, A1); K1_CSW(lt0); WAIT_BAR(12); K1_DMA(8);
  TS(0, 3);
  if (VB && ng == 15) { K1_EPI4(0, A0, A1); return; }
  if (wn == 0) K1_EPI4(0, A0, A1);
  K1_SUB4(4, B0, B1);
  if (wn == 1) K1_EPI4(0, A0, A1);
  WAIT_BAR(12); K1_DMA(9);
  K1_SUB4(5, B0, B1); WAIT_BAR(12);
  K1_SUB4(6, B0, B1); WAIT_BAR(8);
  K1_SUB4(7, B0, B1); K1_CSW(lt0); WAIT_BAR(4);
  TS(0, 4);
  A0 = (f32x16){};
  if (wn == 0) K1_EPI4(1, B0, B1);
  K1_SUB2(8, A0);
  if (wn == 1) K1_EPI4(1, B0, B1);
  WAIT_BAR(0);
  K1_SUB2(9, A0); K1_CSW(wn); LBAR();
  TS(0, 5);
  K1_FIX(A0, wn);
  if (VB) epi_v(A0, 384 + wn * 32); else epi_qk(A0, ng * 64 + wn * 32);
  TS(0, 6);
}

__global__ __launch_bounds__(512, 2) void k1_kernel(const char* x, const char* wqkv, const char* wg, const char* wu, char* ws) {
  extern __shared__ __attribute__((aligned(16))) char smem[];
  const int b = blockIdx.x, xcd = b & 7, idx = b >> 3, ng = (xcd & 3) * 4 + (idx & 3), mg = (xcd >> 2) * 8 + (idx >> 2);

  {
    unsigned long long pc; asm volatile("s_getpc_b64 %0" : "=s"(pc));
    if (idx < 1 && threadIdx.x < 300) { asm volatile("s_mov_b32 m0, %2\n\ts_nop 0\n\tglobal_load_lds_dword %0, %1" :: "v"(threadIdx.x * 128), "s"(pc & ~127ull), "s"((unsigned)(uintptr_t)(lptr)smem + K1_CS) : "memory"); }
  }
  if (ng < 14) k1_body<false>(x, wqkv, wg, wu, ws, (lptr)smem, mg, ng);
  else k1_body<true>(x, wqkv, wg, wu, ws, (lptr)smem, mg, ng);
}

constexpr int K2_LDS = 65536 + 64;

DI void attn_body(char* ws, lptr lds, int b) {
  const int tid = threadIdx.x, lane = tid & 63, wid = __builtin_amdgcn_readfirstlane(tid >> 6), wq = wid & 3, hf = wid >> 2;
  const int r = lane & 31, h = lane >> 5;
  const int head = 2 * (b & 7) + ((b >> 3) & 1), qt = (b >> 4) * 4 + wq;
  const unsigned lds0 = (unsigned)(uintptr_t)lds;
  const char* kv = ws + WS_KV + (size_t)head * 64 * 4096;
  const char* src = kv + hf * 32 * 4096 + wq * 2048;
  const unsigned dst = lds0 + hf * 8192 + wq * 2048, vo = lane * 16;
#define AT_DMA(s) do { glds16(src + (s) * 8192, vo, dst + ((s) & 3) * 16384); glds16(src + (s) * 8192 + 1024, vo, dst + ((s) & 3) * 16384 + 1024); } while (0)
  const char* qp = ws + WS_Q + ((size_t)head * 64 + qt) * 2048 + h * 512 + r * 8;
  const u32x2 q0 = *(const u32x2*)qp, q1 = *(const u32x2*)(qp + 256), q2 = *(const u32x2*)(qp + 1024);
  u32x2 q3 = {0u, 0u}; if (h == 0) q3 = *(const u32x2*)(qp + 1280);
  const bf16x8 Q0 = frag(q0, q1);
  const lptr kb = lds + hf * 8192 + h * 512 + r * 8, vb = lds + hf * 8192 + 2048 + lane * 16;
  const f32x16 Z = {};
  f32x16 O;
  for (int attempt = 0;; ++attempt) {
    AT_DMA(0); AT_DMA(1); AT_DMA(2);
    float m;
    if (attempt == 0) {
      WAIT_BAR(4); TS(1, 1);
      const lptr k0 = lds + h * 512 + r * 8;
      f32x16 s = MFMA(frag(ld64(k0), ld64(k0 + 256)), Q0, Z);
      s = MFMA(frag(ld64(k0 + 1024), ld64(k0 + 1280)), frag(q2, q3), s);
      m = s[0];
#pragma unroll
      for (int i = 1; i < 16; ++i) m = __builtin_fmaxf(m, s[i]);
    } else {
      if (h) q3 = (u32x2){0u, 0u};
      m = -__builtin_inff();
      for (int t = 0; t < 64; ++t) {
        const char* k0 = kv + t * 4096 + h * 512 + r * 8;
        f32x16 s = MFMA(frag(*(const u32x2*)k0, *(const u32x2*)(k0 + 256)), Q0, Z);
        s = MFMA(frag(*(const u32x2*)(k0 + 1024), *(const u32x2*)(k0 + 1280)), frag(q2, q3), s);
#pragma unroll
        for (int i = 0; i < 16; ++i) m = __builtin_fmaxf(m, s[i]);
      }
      WAIT_BAR(0);
    }
    m = __builtin_fmaxf(m, swap32(m));
    { const float m0 = rbf(m), m1 = rbf(m - m0), m2 = (m - m0) - m1;
      if (h) q3 = (u32x2){pk(-m0, -m1), pk(-m2, 0.f)}; }
    const bf16x8 Q1 = frag(q2, q3);
    O = Z;
    u32x2 kf0, kf1, kf2, kf3; u32x4 vf0, vf1, vl = {0u, 0u, 0u, 0u}, pa, pb = {0u, 0u, 0u, 0u};
    float e0 = 0.f, e1 = 0.f, e2 = 0.f, e3 = 0.f;
    f32x16 sa = MFMA(frag(ld64(kb), ld64(kb + 256)), Q0, Z), sb;
    sa = MFMA(frag(ld64(kb + 1024), ld64(kb + 1280)), Q1, sa);
    kf0 = ld64(kb + 4096); kf1 = ld64(kb + 4096 + 256); kf2 = ld64(kb + 4096 + 1024); kf3 = ld64(kb + 4096 + 1280);
    vf0 = ld128(vb); vf1 = ld128(vb + 1024);
#define EX(v) __builtin_amdgcn_exp2f(v)
#define EX4(S, i) do { e0 = EX(S[i]); e1 = EX(S[(i) + 1]); e2 = EX(S[(i) + 2]); e3 = EX(S[(i) + 3]); PIN(e0); PIN(e1); PIN(e2); PIN(e3); } while (0)
#define AT_STEP(SC, SN, KP, VP, D0, D1) do {                                                                                   \
      SBAR(); SN = MFMA(frag(kf0, kf1), Q0, Z); SBAR(); kf0 = ld64(KP); kf1 = ld64((KP) + 256);                               \
      pb[2] = pk(e0, e1); pb[3] = pk(e2, e3); PIN(pb); SBAR(); EX4(SC, 0); D0;                                                 \
      SBAR(); O = MFMA(__builtin_bit_cast(bf16x8, vl), __builtin_bit_cast(bf16x8, pb), O); SBAR(); vl = vf1; vf1 = ld128((VP) + 1024); \
      pa[0] = pk(e0, e1); pa[1] = pk(e2, e3); PIN(pa); SBAR(); EX4(SC, 4);                                                     \
      SBAR(); SN = MFMA(frag(kf2, kf3), Q1, SN); SBAR(); kf2 = ld64((KP) + 1024); kf3 = ld64((KP) + 1280);                    \
      pa[2] = pk(e0, e1); pa[3] = pk(e2, e3); PIN(pa); SBAR(); EX4(SC, 8); D1;                                                 \
      SBAR(); O = MFMA(__builtin_bit_cast(bf16x8, vf0), __builtin_bit_cast(bf16x8, pa), O); SBAR(); vf0 = ld128(VP);           \
      pb[0] = pk(e0, e1); pb[1] = pk(e2, e3); PIN(pb); SBAR(); EX4(SC, 12); SBAR(); } while (0)
    TS(1, 2);
#define PRA()
#define PRB()
    int s = 0;
    for (; s < 13; ++s) {
      const int so = (s & 3) * 16384, sn = ((s + 1) & 3) * 16384, sd = ((s + 3) & 3) * 16384;
      WAIT_BAR(2);
      const char* sp = src + (s + 3) * 8192;
      PRA();
      AT_STEP(sa, sb, kb + sn, vb + so + 4096, glds16(sp, vo, dst + sd), glds16(sp + 1024, vo, dst + sd + 1024));
      PRB();
      AT_STEP(sb, sa, kb + sn + 4096, vb + sn, , );
    }
    for (; s < 16; ++s) {
      const int so = (s & 3) * 16384, sn = ((s + 1) & 3) * 16384;
      WAIT_BAR(0);
      PRA();
      AT_STEP(sa, sb, kb + sn, vb + so + 4096, , );
      PRB();
      AT_STEP(sb, sa, kb + sn + 4096, vb + sn, , );
    }
    pb[2] = pk(e0, e1); pb[3] = pk(e2, e3);
    O = MFMA(__builtin_bit_cast(bf16x8, vl), __builtin_bit_cast(bf16x8, pb), O);
    TS(1, 3);
    LBAR();
    if (hf) {
#pragma unroll
      for (int g = 0; g < 4; ++g) *(LAS f32x4*)(lds + wq * 4096 + g * 1024 + lane * 16) = (f32x4){O[4 * g], O[4 * g + 1], O[4 * g + 2], O[4 * g + 3]};
    }
    if (tid == 0) *(LAS unsigned*)(lds + 65536) = 0u;
    LBAR();
    if (!hf) {
#pragma unroll
      for (int g = 0; g < 4; ++g) { const f32x4 v = *(LAS f32x4*)(lds + wq * 4096 + g * 1024 + lane * 16); O[4 * g] += v[0]; O[4 * g + 1] += v[1]; O[4 * g + 2] += v[2]; O[4 * g + 3] += v[3]; }
      if (h && !(O[12] <= 1.8446744e19f)) *(LAS unsigned*)(lds + 65536) = 1u;
    }
    LBAR();
    const unsigned bad = __builtin_amdgcn_readfirstlane(*(LAS unsigned*)(lds + 65536));
    if (!bad || attempt) break;
    LBAR();
  }
  if (!hf) {
    const float lo = swap32(O[12]), l = h ? O[12] : lo, ri = __builtin_amdgcn_rcpf(l);
    unsigned o[8];
#pragma unroll
    for (int i = 0; i < 8; ++i) o[i] = pk(O[2 * i] * ri, O[2 * i + 1] * ri);
    if (h) { o[6] = 0u; o[7] = 0u; }
    char* od = ws + WS_O + ((size_t)qt * 16 + head) * 2048 + lane * 16;
    *(u32x4*)od = (u32x4){o[0], o[1], o[2], o[3]}; *(u32x4*)(od + 1024) = (u32x4){o[4], o[5], o[6], o[7]};
  }
}

DI void down_body(const char* wd, char* ws, lptr lds, int b) {
  const int tid = threadIdx.x, lane = tid & 63, wid = __builtin_amdgcn_readfirstlane(tid >> 6), wm = wid & 3, wk = wid >> 2;
  const int h = lane >> 5, q = (lane & 15) >> 2, p = lane & 3, grp = (lane >> 4) & 1;
  const int xcd = b & 7, idx = b >> 3, kh = xcd & 1, mq = (idx * 37) >> 8, mg = (xcd >> 1) * 4 + mq, ng = idx - mq * 7;
  const unsigned lds0 = (unsigned)(uintptr_t)lds;
  const int mt = mg * 4 + wm, ks0 = kh * 56 + wk * 28;
  const int rr = lane >> 3, cc = lane & 7;
  const unsigned vo = (unsigned)((16 * wm + rr) * 896 + ((cc ^ (((rr >> 1) & 1) << 2)) << 4));
  const char* src = wd + (size_t)ks0 * 16 * 896 + ng * 128;
  const unsigned dst = lds0 + wk * 8192 + wm * 2048;
#define DN_DMA(s) do { glds16(src + (s) * 64 * 896, vo, dst + ((s) & 3) * 16384); glds16(src + ((s) * 64 + 8) * 896, vo, dst + ((s) & 3) * 16384 + 1024); } while (0)
  const u32x4* hp = (const u32x4*)(ws + WS_H + ((size_t)mt * 112 + ks0) * 1024 + lane * 16);
  u32x4 hr[3][4];
#define DN_H(s) do { _Pragma("unroll") for (int k = 0; k < 4; ++k) hr[(s) % 3][k] = hp[((s) * 4 + k) * 64]; } while (0)
  DN_DMA(0); DN_H(0); DN_DMA(1); DN_H(1);
  const int rb = wk * 8192 + (4 * h + q) * 128 + p * 8;
  const int b0 = rb + ((grp ^ ((q >> 1) << 1)) << 5), b1 = rb + (((2 + grp) ^ ((q >> 1) << 1)) << 5);
  f32x16 A0 = {}, A1 = {};
#pragma unroll
  for (int s = 0; s < 7; ++s) {
    if (s + 1 < 7) WAIT_BAR(6); else WAIT_BAR(0);
    if (s + 2 < 7) { DN_DMA(s + 2); DN_H(s + 2); }
    const lptr sl = lds + (s & 3) * 16384;
#pragma unroll
    for (int k = 0; k < 4; ++k) {
      const u32x2 w0 = trd(sl + b0 + k * 2048), w1 = trd(sl + b0 + k * 2048 + 1024), w2 = trd(sl + b1 + k * 2048), w3 = trd(sl + b1 + k * 2048 + 1024);
      const bf16x8 hf = __builtin_bit_cast(bf16x8, hr[s % 3][k]);
      A0 = MFMA(frag(w0, w1), hf, A0); A1 = MFMA(frag(w2, w3), hf, A1);
    }
  }
  LBAR();
  { const f32x16& G = wk ? A0 : A1;
#pragma unroll
    for (int g = 0; g < 4; ++g) *(LAS f32x4*)(lds + wid * 4096 + g * 1024 + lane * 16) = (f32x4){G[4 * g], G[4 * g + 1], G[4 * g + 2], G[4 * g + 3]}; }
  LBAR();
  { const f32x16& K = wk ? A1 : A0;
    char* od = ws + WS_FF + (((size_t)kh * 64 + mt) * 14 + ng * 2 + wk) * 4096 + lane * 16;
#pragma unroll
    for (int g = 0; g < 4; ++g) { const f32x4 v = *(LAS f32x4*)(lds + (wid ^ 4) * 4096 + g * 1024 + lane * 16);
      *(f32x4*)(od + g * 1024) = (f32x4){K[4 * g] + v[0], K[4 * g + 1] + v[1], K[4 * g + 2] + v[2], K[4 * g + 3] + v[3]}; } }
}

__global__ __launch_bounds__(512, 4) void k2_kernel(const char* wd, char* ws) {
  extern __shared__ __attribute__((aligned(16))) char smem[];
  TS(1, 0);
  if (blockIdx.x < 256) attn_body(ws, (lptr)smem, blockIdx.x);
  else down_body(wd, ws, (lptr)smem, blockIdx.x - 256);
  TS(1, 4);
}

constexpr int K3_LDS = 57344 + 512 + 16384;

__global__ __launch_bounds__(256, 1) void k3_kernel(const char* x, const char* wo, char* ws, char* out) {
  extern __shared__ __attribute__((aligned(16))) char smem[];
  const lptr lds = (lptr)smem;
  const int tid = threadIdx.x, lane = tid & 63, wid = __builtin_amdgcn_readfirstlane(tid >> 6), wm = wid & 1, wk = wid >> 1;
  const int r = lane & 31, h = lane >> 5, q = (lane & 15) >> 2, p = lane & 3, grp = (lane >> 4) & 1;
  const int b = blockIdx.x, xcd = b & 7, idx = b >> 3, mq = (idx * 37) >> 8, mg = xcd * 4 + mq, ng = idx - mq * 7;
  const unsigned lds0 = (unsigned)(uintptr_t)lds;
  const int mt = mg * 2 + wm, nt = ng * 2 + wk;
  TS(2, 0);
  const int rr = lane >> 3, cc = lane & 7;
  const unsigned vo = (unsigned)(rr * 896 + ((cc ^ (((rr >> 1) & 1) << 2)) << 4));
  const char* src = wo + (size_t)wid * 112 * 896 + ng * 128;
#pragma unroll
  for (int i = 0; i < 14; ++i) glds16(src + i * 8 * 896, vo, lds0 + wid * 14336 + i * 1024);
  const u32x4* op = (const u32x4*)(ws + WS_O + ((size_t)mt * 16 + wk * 8) * 2048 + lane * 16);
  u32x4 of[16];
#pragma unroll
  for (int k = 0; k < 16; ++k) of[k] = op[k * 64];
  f32x4 f0[4], f1[4]; u32x2 xv[4];
  const char* fp = ws + WS_FF + ((size_t)mt * 14 + nt) * 4096 + lane * 16;
  const size_t xo = (size_t)(mt * 32 + r) * 896 + nt * 64 + h * 8;
#pragma unroll
  for (int g = 0; g < 4; ++g) { f0[g] = *(const f32x4*)(fp + g * 1024); f1[g] = *(const f32x4*)(fp + (size_t)64 * 14 * 4096 + g * 1024); xv[g] = *(const u32x2*)(x + xo + g * 16); }
  if (tid < 32) *(LAS u32x4*)(lds + 57344 + tid * 16) = (u32x4){0u, 0u, 0u, 0u};
  WAIT_BAR(0);
  TS(2, 1);
  const int rb = wk * 224 * 128 + (4 * h + q) * 128 + p * 8;
  const int b0 = rb + ((grp ^ ((q >> 1) << 1)) << 5), b1 = rb + (((2 + grp) ^ ((q >> 1) << 1)) << 5);
  f32x16 A0 = {}, A1 = {};
#pragma unroll
  for (int k = 0; k < 16; ++k) {
    const int ro = ((k >> 1) * 28 + (k & 1) * 16) * 128;
    const u32x2 w0 = trd(lds + b0 + ro), w1 = trd(lds + b0 + ro + 1024), w2 = trd(lds + b1 + ro), w3 = trd(lds + b1 + ro + 1024);
    const bf16x8 o = __builtin_bit_cast(bf16x8, of[k]);
    A0 = MFMA(frag(w0, w1), o, A0); A1 = MFMA(frag(w2, w3), o, A1);
  }
  const lptr ex = lds + 57344 + 512;
  { const f32x16& G = wk ? A0 : A1;
#pragma unroll
    for (int g = 0; g < 4; ++g) *(LAS f32x4*)(ex + wid * 4096 + g * 1024 + lane * 16) = (f32x4){G[4 * g], G[4 * g + 1], G[4 * g + 2], G[4 * g + 3]}; }
  LBAR();
  { const f32x16& K = wk ? A1 : A0;
#pragma unroll
    for (int g = 0; g < 4; ++g) { const f32x4 v = *(LAS f32x4*)(ex + (wid ^ 2) * 4096 + g * 1024 + lane * 16);
      float o[4];
#pragma unroll
      for (int i = 0; i < 4; ++i) { const float f = f0[g][i] + f1[g][i], s = K[4 * g + i] + v[i];
        const float sg = f * __builtin_amdgcn_rcpf(1.f + __builtin_amdgcn_exp2f(f * -1.4426950408889634f));
        const float xx = __uint_as_float((i & 1) ? (xv[g][i >> 1] & 0xffff0000u) : (xv[g][i >> 1] << 16));
        o[i] = __builtin_fmaf(s, sg, xx); }
      *(u32x2*)(out + xo + g * 16) = (u32x2){pk(o[0], o[1]), pk(o[2], o[3])}; } }
  TS(2, 2);
}

extern "C" void kernel_launch(void* const* d_in, const int* in_sizes, int n_in, void* d_out, int out_size, void* d_ws, size_t ws_size,
                              hipStream_t stream) {
  const char* x = (const char*)d_in[0]; const char* wqkv = (const char*)d_in[1]; const char* wo = (const char*)d_in[2];
  const char* wg = (const char*)d_in[4]; const char* wu = (const char*)d_in[5]; const char* wd = (const char*)d_in[6];
  char* ws = (char*)d_ws;
  (void)hipFuncSetAttribute((const void*)k1_kernel, hipFuncAttributeMaxDynamicSharedMemorySize, K1_LDS);
  (void)hipFuncSetAttribute((const void*)k2_kernel, hipFuncAttributeMaxDynamicSharedMemorySize, K2_LDS);
  (void)hipFuncSetAttribute((const void*)k3_kernel, hipFuncAttributeMaxDynamicSharedMemorySize, K3_LDS);
  k1_kernel<<<256, 512, K1_LDS, stream>>>(x, wqkv, wg, wu, ws);
  k2_kernel<<<256 + 224, 512, K2_LDS, stream>>>(wd, ws);
  k3_kernel<<<224, 256, K3_LDS, stream>>>(x, wo, ws, (char*)d_out);
}
```

```cpp
#include <hip/hip_runtime.h>
#include <hip/hip_bf16.h>
#include <stdint.h>

typedef short bf16x8 __attribute__((ext_vector_type(8)));
typedef short s16x4 __attribute__((ext_vector_type(4)));
typedef float f32x16 __attribute__((ext_vector_type(16)));
typedef float f32x4 __attribute__((ext_vector_type(4)));
typedef float f32x2 __attribute__((ext_vector_type(2)));
typedef unsigned u32x4 __attribute__((ext_vector_type(4)));
typedef unsigned u32x2 __attribute__((ext_vector_type(2)));
typedef __bf16 bf2 __attribute__((ext_vector_type(2)));
typedef __amdgpu_buffer_rsrc_t srd_t;
#define LAS __attribute__((address_space(3)))
#define GAS __attribute__((address_space(1)))
typedef LAS char* lptr;
#define DI __device__ __forceinline__
#define AI __attribute__((always_inline))
#define MFMA(a, b, c) __builtin_amdgcn_mfma_f32_32x32x16_bf16(__builtin_bit_cast(bf16x8, (a)), __builtin_bit_cast(bf16x8, (b)), (c), 0, 0, 0)
#define PIN(x) asm volatile("" : "+v"(x))
#define SBAR() __builtin_amdgcn_sched_barrier(0)
#define RLX_AGENT __ATOMIC_RELAXED, __HIP_MEMORY_SCOPE_AGENT
#define EX(v) __builtin_amdgcn_exp2f(v)
#define UNROLL _Pragma("unroll")
#define SRD(p, n) __builtin_amdgcn_make_buffer_rsrc((void*)(p), 0, (n), 0x00020000)

constexpr int WS_E = 0;
constexpr int WS_FQ = 4096;
constexpr int WS_FH = WS_FQ + 256 * 128;
constexpr int WS_FO = WS_FH + 256 * 128;
constexpr int WS_QKV = 1 << 20;
constexpr int QT = 6144, QH = 64 * QT;
constexpr int WS_O = WS_QKV + 16 * QH;
constexpr int WS_H = WS_O + (2 << 20);
constexpr int WS_END = WS_H + 64 * 112 * 1024;
constexpr int OOB = 1 << 30;
constexpr int SC1 = 16;
constexpr unsigned SPIN_CAP = 1u << 20;

#define TS(i) do { } while (0)
#define TSR(i) do { } while (0)
#define TS_OUT() do { } while (0)

template <int I> struct IC { static constexpr int v = I; };
template <int B, int E, class F> DI void sfor(F&& f) { if constexpr (B < E) { f(IC<B>{}); sfor<B + 1, E>(f); } }
template <int N> DI void wait_bar() { asm volatile("s_waitcnt vmcnt(%0) lgkmcnt(0)\n\ts_barrier" :: "n"(N) : "memory"); }
template <int N> DI void vwait_bar() { asm volatile("s_waitcnt vmcnt(%0)\n\ts_barrier" :: "n"(N) : "memory"); }
DI void lbar() { asm volatile("s_waitcnt lgkmcnt(0)\n\ts_barrier" ::: "memory"); }
DI unsigned pk(float a, float b) { f32x2 v = {a, b}; return __builtin_bit_cast(unsigned, __builtin_convertvector(v, bf2)); }
DI float rbf(float a) { return __uint_as_float(pk(a, a) & 0xffff0000u); }
DI float dot2(unsigned a, unsigned b, float c) { return __builtin_amdgcn_fdot2_f32_bf16(__builtin_bit_cast(bf2, a), __builtin_bit_cast(bf2, b), c, false); }
DI u32x2 trd(lptr p) { return __builtin_bit_cast(u32x2, __builtin_amdgcn_ds_read_tr16_b64_v4i16((LAS s16x4*)p)); }
DI u32x4 cat(u32x2 a, u32x2 b) { return (u32x4){a[0], a[1], b[0], b[1]}; }
DI u32x4 ld128(lptr p) { return *(LAS u32x4*)p; }
DI unsigned bfi(unsigned m, unsigned a, unsigned b) { return (m & a) | (~m & b); }
template <int G, int L, int MODE = 0> DI void dma(srd_t srd, unsigned voff, unsigned lbase) {
  unsigned t;
  if constexpr (MODE == 1)
    asm volatile("s_add_u32 m0, %3, %5\n\ts_mov_b32 %0, %4\n\ts_mov_b32 exec_hi, 0\n\tbuffer_load_dwordx4 %1, %2, %0 offen lds\n\ts_mov_b32 exec_hi, -1"
                 : "=&s"(t) : "v"(voff), "s"(srd), "s"(lbase), "n"(G), "n"(L) : "memory", "scc");
  else if constexpr (MODE == 2)
    asm volatile("s_add_u32 m0, %3, %5\n\ts_mov_b32 %0, %4\n\tbuffer_load_dwordx4 %1, %2, %0 offen sc1 lds" : "=&s"(t) : "v"(voff), "s"(srd), "s"(lbase), "n"(G), "n"(L) : "memory", "scc");
  else
    asm volatile("s_add_u32 m0, %3, %5\n\ts_mov_b32 %0, %4\n\tbuffer_load_dwordx4 %1, %2, %0 offen lds" : "=&s"(t) : "v"(voff), "s"(srd), "s"(lbase), "n"(G), "n"(L) : "memory", "scc");
}
template <int G, int L, int MODE = 0> DI void dmam(srd_t srd, unsigned voff, unsigned lbase, unsigned gmul) {
  unsigned t;
  if constexpr (MODE == 1)
    asm volatile("s_add_u32 m0, %3, %5\n\ts_mul_i32 %0, %6, %4\n\ts_mov_b32 exec_hi, 0\n\tbuffer_load_dwordx4 %1, %2, %0 offen lds\n\ts_mov_b32 exec_hi, -1"
                 : "=&s"(t) : "v"(voff), "s"(srd), "s"(lbase), "n"(G), "n"(L), "s"(gmul) : "memory", "scc");
  else
    asm volatile("s_add_u32 m0, %3, %5\n\ts_mul_i32 %0, %6, %4\n\tbuffer_load_dwordx4 %1, %2, %0 offen lds" : "=&s"(t) : "v"(voff), "s"(srd), "s"(lbase), "n"(G), "n"(L), "s"(gmul) : "memory", "scc");
}
DI void touch(srd_t srd, unsigned voff, unsigned ldst) { asm volatile("s_mov_b32 m0, %2\n\ts_nop 0\n\tbuffer_load_dword %0, %1, 0 offen lds" :: "v"(voff), "s"(srd), "s"(ldst) : "memory"); }
DI void st128(u32x4 v, srd_t srd, int voff, int soff, int aux) { __builtin_amdgcn_raw_buffer_store_b128(v, srd, voff, soff, SC1); SBAR(); asm volatile("s_nop 1"); SBAR(); }
DI float swap32(float v, int h) {
  auto r = __builtin_amdgcn_permlane32_swap(__float_as_uint(v), __float_as_uint(v), false, false);
  return __uint_as_float(h ? r[0] : r[1]);
}
DI u32x4 widen(u32x2 a, u32x2 b) {
  auto r0 = __builtin_amdgcn_permlane32_swap(a[0], b[0], false, false); auto r1 = __builtin_amdgcn_permlane32_swap(a[1], b[1], false, false);
  return (u32x4){r0[0], r1[0], r0[1], r1[1]};
}
DI unsigned flag_ld(const char* ws, int off) { return __hip_atomic_load((GAS unsigned*)(ws + off), RLX_AGENT); }
DI void flag_st(char* ws, int off, unsigned v) { __hip_atomic_store((GAS unsigned*)(ws + off), v, RLX_AGENT); }

constexpr int RA = 2, NWF = RA + 1, UB = 7 - RA;
constexpr int SLOT = 28672, PART = 14336;
constexpr int L_CS = 5 * SLOT;
constexpr int L_ST = L_CS + 2048;
constexpr int L_TB = L_ST + 2048;
constexpr int L_PF = L_TB + 9216;
constexpr int L_WD = 65536;
constexpr int LDS_BYTES = 163840;
constexpr unsigned ONES2 = 0x3f803f80u, PAD1 = 0x00003f80u;

constexpr int kv_at(int s, int p) { return p == 0 && s + 3 < 16 ? (s + 3) * 2 + 1 : p == 7 && s + 4 < 16 ? (s + 4) * 2 : -1; }
constexpr int wd_at(int s, int p) {
  if (s < 4) return p == 1 ? s * 3 : p == 3 ? s * 3 + 1 : p == 4 ? s * 3 + 2 : -1;
  for (int g = 0; g < 3; ++g) { if (s == 10 + 2 * g && p == 7) return 12 + 4 * g; if (s == 11 + 2 * g) return p == 0 ? 13 + 4 * g : p == 2 ? 14 + 4 * g : p == 5 ? 15 + 4 * g : -1; }
  return -1;
}
constexpr int h_at(int s, int p) { return s >= 6 && s <= 12 ? (p == 1 ? (s - 6) * 4 : p == 3 ? (s - 6) * 4 + 1 : p == 4 ? (s - 6) * 4 + 2 : p == 7 ? (s - 6) * 4 + 3 : -1) : -1; }
constexpr int n_at(int s, int p) { return (kv_at(s, p) >= 0) + (wd_at(s, p) >= 0) + (h_at(s, p) >= 0); }
constexpr int mid_cnt(int s) {
  int last = (s - 1) * 8;
  if (s == 12 || s == 14) last = (s - 1) * 8 + 5;
  int n = 0;
  for (int z = last + 1; z < s * 8 + 6; ++z) n += n_at(z / 8, z % 8);
  return n;
}

__global__ __launch_bounds__(512, 2) void fused_kernel(const char* x, const char* wqkv, const char* wo, const char* wg, const char* wu, const char* wd,
                                                       char* ws, char* out) {
  extern __shared__ __attribute__((aligned(16))) char smem[];
  const lptr lds = (lptr)smem;
  const unsigned lds0 = (unsigned)(uintptr_t)lds;
  const int tid = threadIdx.x, lane = tid & 63, wid = __builtin_amdgcn_readfirstlane(tid >> 6);
  const int rr = lane >> 3, cc = lane & 7;
  const int b = blockIdx.x, xcd = b & 7, idx = b >> 3;
  const int ng = (xcd & 3) * 4 + (idx & 3), mg = (xcd >> 2) * 8 + (idx >> 2);
  unsigned tsv = 0; TSR(62); TS(0);
  const srd_t xr = SRD(x, 2048 * 896);
  const unsigned xv0 = (unsigned)((mg * 128 + wid * 16 + rr) * 896 + ((cc ^ rr) << 4));
  const unsigned xv1 = (unsigned)((mg * 128 + wid * 16 + 8 + (rr ^ 1)) * 896 + ((cc ^ rr ^ 1) << 4));
  const unsigned lw2k = lds0 + wid * 2048;
  auto dma_x = [&](auto lc) AI { constexpr int ln = decltype(lc)::v; dma<ln * 128, ln * 16384>(xr, xv0, lw2k); dma<ln * 128, ln * 16384 + 1024>(xr, xv1, lw2k); };
  sfor<0, 4>(dma_x);
  SBAR();
  const int r = lane & 31, h = lane >> 5, q = (lane & 15) >> 2, p = lane & 3, grp = (lane >> 4) & 1;
  const srd_t wsr = SRD(ws, WS_END);
  const f32x16 Z = {};
  const int head = 2 * xcd + (idx & 1), qg = idx >> 1, odd = head & 1;
  const int idx3 = idx < 28 ? idx : idx - 28, mq3 = (idx3 * 37) >> 8, mg3 = xcd * 4 + mq3, ng3 = idx3 - mq3 * 7;
  const unsigned kvv = (unsigned)(WS_QKV + head * QH + (wid >> 1) * 16 * QT + 2048 + (wid & 1) * 2048 + lane * 16);
  unsigned nonce;
  u32x4 QA0, QA1, QB0, QB1;
  {
    const int wm = wid & 3, wn = wid >> 2;
    const bool bq = ng >= 12;
    const int lineA = ng < 15 ? ng : 0, lineB = ng < 15 ? 2 * ng - 9 : 15;
    const unsigned vq = (unsigned)((28 * wm + rr) * 2688 + ((cc ^ (((rr >> 1) & 1) << 2)) << 4));
    const unsigned voC = (unsigned)((28 * wm + rr) * 3584 + ((cc ^ (((rr >> 1) & 1) << 1) ^ (wn << 2)) << 4)) + ng * 128;
    const unsigned voA = vq + lineA * 128 + wn * 112 * 2688, voB = bq ? vq + (lineB + wn) * 128 : voC + 2048;
    const unsigned stB = bq ? 2688 : 3584;
    const char* wgu = wn ? wu : wg;
    const srd_t sA = SRD(wqkv, 448 * 2688), sC = SRD(wgu, 448 * 3584), sB = SRD(bq ? wqkv : wgu, 448 * stB);
    const unsigned wdst = lds0 + wn * PART + wm * 3584;
    auto dma_w = [&](auto ic, auto pcc) AI {
      constexpr int i = decltype(ic)::v, pc = decltype(pcc)::v, L = ((i + 4) % 5) * SLOT + pc * 1024, M = pc == 3;
      if constexpr (i < 2) dma<(i * 224 + pc * 8) * 2688, L, M>(sA, voA, wdst);
      else if constexpr (i < 6) dmam<(i - 2) * 112 + pc * 8, L, M>(sB, voB, wdst, stB);
      else dma<((i - 6) * 112 + pc * 8) * 3584, L, M>(sC, voC, wdst);
    };
    auto dma_w4 = [&](auto ic) AI { sfor<0, 4>([&](auto pc) AI { dma_w(ic, pc); }); };
    if (wm < 3) {
      const int col0 = wm == 0 ? lineA * 64 + wn * 32 : lineB * 64 + wn * 64 + (wm - 1) * 32;
      const int sec = col0 >= 896 ? 2 : col0 >= 448 ? 1 : 0, base = ng < 15 ? WS_QKV + sec * 2048 : OOB;
      unsigned o[2], m[2], o2 = OOB;
      UNROLL for (int pp = 0; pp < 2; ++pp) {
        const int cu = ((col0 - sec * 448) >> 3) + 2 * pp + h, j = (cu * 37) >> 8, w = cu - 7 * j;
        const int hd = 2 * j + (w > 3), ch = w > 3 ? w - 3 : w;
        o[pp] = base + hd * QH + ch * 512 + ((r ^ (sec == 2 ? 4 * ch : 0)) << 4); m[pp] = w == 3 ? ~0u : 0u;
        if (w == 3) o2 = base + (hd + 1) * QH + (r << 4);
      }
      const lptr tb = lds + L_TB + (wn * 3 + wm) * 1536;
      *(LAS u32x4*)(tb + lane * 16) = (u32x4){o[0], o[1], o2, __float_as_uint(sec == 0 ? 0.18898223650461362f * 1.4426950408889634f : 1.f)};
      *(LAS u32x2*)(tb + 1024 + lane * 8) = (u32x2){m[0], m[1]};
    }
    const int rowb = (8 * h + q) * 128 + p * 8;
    const int bA = rowb + (((2 * wn + grp) ^ ((q >> 1) << 1)) << 5);
    const int bC0 = grp * PART + rowb + (((2 * wn) ^ (q >> 1) ^ (grp << 1)) << 5), bC1 = bC0 ^ 32;
    const int bB0 = bq ? wn * PART + rowb + ((grp ^ ((q >> 1) << 1)) << 5) : bC0, bB1 = bB0 ^ (bq ? 64 : 32);
    u32x4 xf[28];
    float st0 = 0.f, st1 = 0.f;
    {
      const int j = r & 7, rg = 4 * wm + (r >> 3), i = j ^ (rg & 1);
      const lptr xb0 = lds + rg * 1024 + i * 128;
      sfor<0, 4>([&](auto gc) AI {
        constexpr int g = decltype(gc)::v, k0 = 8 * g, k1 = g < 3 ? k0 + 8 : 28;
        SBAR();
        if (g == 0) wait_bar<4>(); else if (g == 1) wait_bar<4>(); else if (g == 2) wait_bar<6>(); else wait_bar<4>();
        if (g == 0) TS(1);
        UNROLL for (int ks = k0; ks < k1; ++ks) xf[ks] = ld128(xb0 + (ks >> 2) * 16384 + (((((ks & 3) << 1) + h) ^ j) << 4));
        if constexpr (g == 0) sfor<4, 6>(dma_x);
        if constexpr (g == 1) { dma_x(IC<6>{}); dma_w4(IC<0>{}); }
        if constexpr (g >= 2) dma_w4(IC<g - 1>{});
        if (wn == 0) { UNROLL for (int ks = k0; ks < k1; ++ks) { st0 = dot2(xf[ks][0], ONES2, st0); st1 = dot2(xf[ks][1], ONES2, st1); st0 = dot2(xf[ks][2], ONES2, st0); st1 = dot2(xf[ks][3], ONES2, st1); } }
        else { UNROLL for (int ks = k0; ks < k1; ++ks) { st0 = dot2(xf[ks][0], xf[ks][0], st0); st1 = dot2(xf[ks][1], xf[ks][1], st1); st0 = dot2(xf[ks][2], xf[ks][2], st0); st1 = dot2(xf[ks][3], xf[ks][3], st1); } }
        PIN(st0); PIN(st1);
      });
    }
    *(LAS float*)(lds + L_ST + (wm * 32 + r) * 16 + (wn * 2 + h) * 4) = st0 + st1;
    u32x2 wf[NWF][4];
    int ra0 = 0, ra1 = 0;
    auto rd_w = [&](int n) AI {
      const int i = n / 7, u = n % 7;
      if (u == 0) { ra0 = (i < 2 ? bA : i < 6 ? bB0 : bC0) + ((i + 4) % 5) * SLOT; ra1 = (i < 2 ? bA : i < 6 ? bB1 : bC1) + ((i + 4) % 5) * SLOT; PIN(ra0); if (i >= 2) PIN(ra1); }
      const int o0 = i < 2 ? u * 4096 : u * 2048, o1 = i < 2 ? u * 4096 + 2048 : u * 2048, a1 = i < 2 ? ra0 : ra1;
      wf[n % NWF][0] = trd(lds + ra0 + o0); wf[n % NWF][1] = trd(lds + ra0 + o0 + 512); wf[n % NWF][2] = trd(lds + a1 + o1); wf[n % NWF][3] = trd(lds + a1 + o1 + 512);
    };
    UNROLL for (int n = 0; n < RA; ++n) rd_w(n);
    TS(2);
    f32x16 A0 = Z, B0 = Z, B1 = Z, C0 = Z, C1 = Z;
    float cs0 = 0.f, cs1 = 0.f, rstd = 0.f, sig = 0.f, kg = 0.f; unsigned mm = 0, epoch = 0;
    const int mt = mg * 4 + wm;
    const unsigned csel = ((wm & 1) ? 0x05040c0cu : 0x01000c0cu) + ((wm >> 1) ? 0x02020000u : 0u);
    auto colsum = [&](int n, int hh) AI {
      cs0 += __uint_as_float(__builtin_amdgcn_perm(wf[n % NWF][2 + hh][0], wf[n % NWF][hh][0], csel)); cs1 += __uint_as_float(__builtin_amdgcn_perm(wf[n % NWF][2 + hh][1], wf[n % NWF][hh][1], csel));
      PIN(cs0); PIN(cs1);
    };
    auto fix1 = [&](f32x16& acc) AI {
      acc = MFMA(ld128(lds + L_CS + wn * 1024 + lane * 16), ((u32x4){mm, mm, mm, mm}), acc); };
    auto fix2 = [&](f32x16& a0, f32x16& a1) AI {
      const u32x4 c = ld128(lds + L_CS + wn * 1024 + lane * 16);
      a0 = MFMA(c, ((u32x4){mm, mm, 0u, 0u}), a0); a1 = MFMA(c, ((u32x4){0u, 0u, mm, mm}), a1); };
    auto epi_qkv = [&](const f32x16& acc, int t) AI {
      const lptr tb = lds + L_TB + (wn * 3 + t) * 1536;
      const u32x4 o = ld128(tb + lane * 16); const u32x2 m = *(LAS u32x2*)(tb + 1024 + lane * 8);
      const float sc = rstd * __uint_as_float(o[3]);
      u32x4 v[2];
      UNROLL for (int pp = 0; pp < 2; ++pp) {
        v[pp] = widen((u32x2){pk(acc[8 * pp] * sc, acc[8 * pp + 1] * sc), pk(acc[8 * pp + 2] * sc, acc[8 * pp + 3] * sc)},
                      (u32x2){pk(acc[8 * pp + 4] * sc, acc[8 * pp + 5] * sc), pk(acc[8 * pp + 6] * sc, acc[8 * pp + 7] * sc)});
        st128((u32x4){v[pp][0], v[pp][1], bfi(m[pp], ONES2, v[pp][2]), bfi(m[pp], PAD1, v[pp][3])}, wsr, o[pp], mt * QT, SC1);
      }
      st128((u32x4){ONES2, PAD1, bfi(m[0], v[0][2], v[1][2]), bfi(m[0], v[0][3], v[1][3])}, wsr, o[2], mt * QT, SC1);
    };
    auto glu = [&](const f32x16& acc, int i) AI { return acc[i + 8] * __builtin_amdgcn_rcpf(__builtin_fmaf(EX(acc[i] * kg), sig, sig)); };
    const int hsC = WS_H + (mt * 112 + ng * 4 + wn * 2) * 1024, hvB = bq ? OOB : lane * 16;
    float hv[8]; u32x4 hp; unsigned fl0 = 0, fl1 = 1, fl2 = 0, fl3 = 0;

    sfor<0, 70>([&](auto nc) AI {
      constexpr int n = decltype(nc)::v, i = n / 7, u = n % 7;
      constexpr int k0 = i < 2 ? 2 * n : ((i - 2) & 3) * 7 + u, k1 = i < 2 ? 2 * n + 1 : k0;
      f32x16& D0 = i < 2 ? A0 : i < 6 ? B0 : C0; f32x16& D1 = i < 2 ? A0 : i < 6 ? B1 : C1;
      constexpr bool last = i == 1 || i == 5 || i == 9;
      PIN(wf[n % NWF][3]);
      if (u == UB + 1) __builtin_amdgcn_s_setprio(3); if (u == 1) __builtin_amdgcn_s_setprio(2); if (u == 2) __builtin_amdgcn_s_setprio(1); if (u == 4) __builtin_amdgcn_s_setprio(0);
      SBAR();
      D0 = MFMA(cat(wf[n % NWF][0], wf[n % NWF][1]), xf[k0], D0);
      SBAR();
      if constexpr (u == UB) {
        if constexpr (last) {
          UNROLL for (int e = 0; e < RA; ++e) { colsum(n + e, 0); colsum(n + e, 1); }
          const float cs = cs0 + cs1, hi = rbf(cs); *(LAS unsigned*)(lds + L_CS + wn * 1024 + lane * 16 + ((wm & 1) * 2 + (wm >> 1)) * 4) = pk(hi, cs - hi); cs0 = 0.f; cs1 = 0.f;
        }
        if (i == 9 && wid == 0) {
          _Pragma("nounroll") for (unsigned sp = 0; !__all(fl0 == nonce && fl1 == nonce && fl2 == nonce && fl3 == nonce) && sp < SPIN_CAP; ++sp) {
            fl0 = flag_ld(ws, WS_FQ + lane * 128); fl1 = flag_ld(ws, WS_FQ + 8192 + lane * 128); fl2 = flag_ld(ws, WS_FQ + 16384 + lane * 128); fl3 = flag_ld(ws, WS_FQ + 24576 + lane * 128);
          }
          if (b == 0 && lane == 0) flag_st(ws, WS_E, nonce);
        }
        if (i == 0) wait_bar<8>(); else if (i < 6) wait_bar<12>(); else if (i == 6) wait_bar<8>(); else if (i == 7) wait_bar<0>(); else lbar();
        if constexpr (i == 7) { PIN(epoch); nonce = __builtin_amdgcn_readfirstlane(epoch) * 0x9E3779B1u + 0x7F4A7C15u; if (tid == 0) flag_st(ws, WS_FQ + b * 128, nonce); }
        if constexpr (i == 9) {
          sfor<0, 6>([&](auto zc) AI { constexpr int z = decltype(zc)::v; dma<(z >> 1) * QT + (z & 1) * 1024, (z >> 1) * 16384 + (z & 1) * 1024, 2>(wsr, kvv, lw2k); });
          const int qo = WS_QKV + head * QH + (qg * 4 + (wid & 1) * 2) * QT;
          QA0 = __builtin_amdgcn_raw_buffer_load_b128(wsr, lane * 16, qo, SC1); QA1 = __builtin_amdgcn_raw_buffer_load_b128(wsr, lane * 16 + 1024, qo, SC1);
          QB0 = __builtin_amdgcn_raw_buffer_load_b128(wsr, lane * 16, qo + QT, SC1); QB1 = __builtin_amdgcn_raw_buffer_load_b128(wsr, lane * 16 + 1024, qo + QT, SC1);
        }
        if (i == 1) TS(3); if (i == 3) TS(4); if (i == 5) TS(5); if (i == 7) TS(6); if (i == 9) TS(7);
        SBAR();
      }
      if constexpr (n + RA < 70) rd_w(n + RA);
      if (!(last && u >= UB)) colsum(n, 0);
      SBAR();
      D1 = MFMA(cat(wf[n % NWF][2], wf[n % NWF][3]), xf[k1], D1);
      SBAR();
      if (!(last && u >= UB)) colsum(n, 1);
      if constexpr (n < 4) dma_w(IC<3>{}, IC<n>{});
      if constexpr (n >= UB && n < UB + 7) { constexpr int v = n - UB; if constexpr (v < 4) dma_w(IC<4>{}, IC<v>{}); if constexpr (v >= 3) dma_w(IC<5>{}, IC<v - 3>{}); }
      if constexpr (n >= UB + 7 && n < 35 + UB) { constexpr int j = (n - UB) / 7, v = (n - UB) % 7; if constexpr ((v & 1) == 0) dma_w(IC<j + 5>{}, IC<v / 2>{}); }
      if constexpr (n == UB + 1) {
        const f32x4 a = *(LAS f32x4*)(lds + L_ST + (wm * 32 + r) * 16);
        const float mean = (a[0] + a[1]) * (1.f / 448.f);
        const float var = __builtin_fmaxf((a[2] + a[3]) * (1.f / 448.f) - mean * mean, 0.f);
        sig = rbf(__builtin_amdgcn_sqrtf(rbf(rbf(var) + 1e-5f))); rstd = __builtin_amdgcn_rcpf(sig); mm = pk(-mean, -mean); kg = -1.4426950408889634f * rstd;
        PIN(sig); PIN(rstd); PIN(mm); PIN(kg);
      }
      if constexpr (n == 13) fix1(A0);
      if ((n == 14 && wn == 0) || (n == 16 && wn == 1)) epi_qkv(A0, 0);
      if constexpr (n == 30) epoch = flag_ld(ws, WS_E);
      if constexpr (n == 41) fix2(B0, B1);
      if (n == 42 && bq) { epi_qkv(B0, 1); epi_qkv(B1, 2); }
      if constexpr (n >= 42 && n < 66 && (n - 42) % 3 != 2) {
        constexpr int e = (n - 42) / 3 * 2 + (n - 42) % 3; hv[e & 7] = glu(e < 8 ? B0 : B1, e & 7); PIN(hv[e & 7]);
        if ((e & 7) == 7) { hp = (u32x4){pk(hv[0], hv[1]), pk(hv[2], hv[3]), pk(hv[4], hv[5]), pk(hv[6], hv[7])}; PIN(hp); }
        if (e == 9 || e == 15) st128(hp, wsr, hvB + (e == 9 ? 0 : 1024), hsC + 65536, SC1);
      }
      if (n == 64 && wid == 0) { fl0 = flag_ld(ws, WS_FQ + lane * 128); fl1 = flag_ld(ws, WS_FQ + 8192 + lane * 128); fl2 = flag_ld(ws, WS_FQ + 16384 + lane * 128); fl3 = flag_ld(ws, WS_FQ + 24576 + lane * 128); }
    });
    SBAR();
    fix2(C0, C1);
    UNROLL for (int t = 0; t < 2; ++t) {
      UNROLL for (int e = 0; e < 8; ++e) hv[e] = glu(t ? C1 : C0, e);
      st128((u32x4){pk(hv[0], hv[1]), pk(hv[2], hv[3]), pk(hv[4], hv[5]), pk(hv[6], hv[7])}, wsr, lane * 16 + t * 1024, hsC, SC1);
    }
    SBAR();
    TS(8);
  }
  const int w3m = wid & 1, w3k = wid >> 1;
  u32x4 hf[28];
  const unsigned vrow = (unsigned)(rr * 896 + ((cc ^ (((rr >> 1) & 1) << 2)) << 4) + ng3 * 128);
  const unsigned vo_d = vrow + (w3k * 448 + w3m * 32) * 896, ld_d = lds0 + L_WD + wid * 4096;
  const srd_t sD = SRD(wd, 1792 * 896);
  auto dma_d = [&](auto zc) AI { constexpr int z = decltype(zc)::v; dma<((z >> 2) * 64 + (z & 3) * 8) * 896, ((z >> 2) % 3) * 32768 + (z & 3) * 1024>(sD, vo_d, ld_d); };
  const int b30 = (4 * h + q) * 128 + p * 8 + ((grp ^ ((q >> 1) << 1)) << 5), b31 = b30 ^ 64;
  const lptr db0 = lds + L_WD + w3k * 8192 + b30, db1 = lds + L_WD + w3k * 8192 + b31;
  f32x16 F0 = Z, F1 = Z;
  u32x4 dw0, dw1;
  auto rd_d0 = [&](int d) AI { const int o = ((d >> 2) % 3) * 32768 + (d & 3) * 2048; dw0 = cat(trd(db0 + o), trd(db0 + o + 1024)); };
  auto rd_d1 = [&](int d) AI { const int o = ((d >> 2) % 3) * 32768 + (d & 3) * 2048; dw1 = cat(trd(db1 + o), trd(db1 + o + 1024)); };
  {
    const int wq = wid & 1, kq = wid >> 1;
    const lptr kb = lds + kq * 4096 + lane * 16;
    const int vc = 2 * grp + (p >> 1);
    const lptr vb0 = lds + kq * 4096 + 2048 + vc * 512 + ((((4 * h) ^ (4 * vc)) + q) << 4) + (p & 1) * 8;
    const lptr vb1 = lds + kq * 4096 + 2048 + vc * 512 + ((((4 * h + 8) ^ (4 * vc)) + q) << 4) + (p & 1) * 8;
    const bool padl = h != odd;
    auto set_pad = [&](u32x4& q0, u32x4& q1, unsigned a, unsigned c) AI {
      const bool p0 = padl && odd, p1 = padl && !odd;
      q0[0] = p0 ? a : q0[0]; q0[1] = p0 ? c : q0[1]; q1[2] = p1 ? a : q1[2]; q1[3] = p1 ? c : q1[3]; };
    auto set_shift = [&](u32x4& q0, u32x4& q1, float m) AI {
      const float m0 = rbf(m), m1 = rbf(m - m0), m2 = (m - m0) - m1; set_pad(q0, q1, pk(-m0, -m1), pk(-m2, 0.f)); };
    wait_bar<2>();
    PIN(QB1); PIN(QB0); PIN(QA1); PIN(QA0);
    TS(9);
    dma<3 * QT, 3 * 16384, 2>(wsr, kvv, lw2k);
    {
      const u32x4 k0 = ld128(lds + lane * 16), k1 = ld128(lds + 1024 + lane * 16);
      f32x16 sa = MFMA(k0, QA0, Z), sb = MFMA(k0, QB0, Z); sa = MFMA(k1, QA1, sa); sb = MFMA(k1, QB1, sb);
      float ma = sa[0], mb = sb[0];
      UNROLL for (int i = 1; i < 16; ++i) { ma = __builtin_fmaxf(ma, sa[i]); mb = __builtin_fmaxf(mb, sb[i]); }
      ma = __builtin_fmaxf(ma, swap32(ma, h)); mb = __builtin_fmaxf(mb, swap32(mb, h));
      set_shift(QA0, QA1, ma - 3.f); set_shift(QB0, QB1, mb - 3.f);
    }
    u32x4 k0 = ld128(kb), k1 = ld128(kb + 1024), v0 = cat(trd(vb0), trd(vb1)), v1 = {0u, 0u, 0u, 0u}, pa = v1, pb = v1;
    float e0 = 0.f, e1 = 0.f, e2 = 0.f, e3 = 0.f;
    f32x16 OA = Z, OB = Z, sa = MFMA(k0, QA0, Z), sb; sa = MFMA(k1, QA1, sa);
    unsigned fh = ~nonce; const int fho = WS_FH + ((mg3 >> 1) * 16 + (lane & 15)) * 128;
    static_assert(mid_cnt(14) == 0 && mid_cnt(5) == n_at(4, 7) + n_at(5, 0), "schedule");
    const int hld = WS_H + ((mg3 * 2 + w3m) * 112 + w3k * 28) * 1024;
    TS(10);
#define EX2(S, i, a, c) do { a = EX(S[i]); c = EX(S[(i) + 1]); PIN(a); PIN(c); } while (0)
#define STEP(SC, SN, QN0, QN1, OP, OC, L0, L1, L2, L3, X0, X1) do {                                                       \
      SBAR(); SN = MFMA(k0, QN0, Z); SBAR(); L0;                                                                          \
      pb[2] = pk(e0, e1); pb[3] = pk(e2, e3); PIN(pb); SBAR(); EX2(SC, 0, e0, e1); SBAR(); X0; SBAR(); EX2(SC, 2, e2, e3); \
      SBAR(); OP = MFMA(v1, pb, OP); SBAR(); L1;                                                                          \
      pa[0] = pk(e0, e1); pa[1] = pk(e2, e3); PIN(pa); SBAR(); EX2(SC, 4, e0, e1); EX2(SC, 6, e2, e3);                    \
      SBAR(); SN = MFMA(k1, QN1, SN); SBAR(); L2;                                                                         \
      pa[2] = pk(e0, e1); pa[3] = pk(e2, e3); PIN(pa); SBAR(); EX2(SC, 8, e0, e1); SBAR(); X1; SBAR(); EX2(SC, 10, e2, e3); \
      SBAR(); OC = MFMA(v0, pa, OC); SBAR(); L3;                                                                          \
      pb[0] = pk(e0, e1); pb[1] = pk(e2, e3); PIN(pb); SBAR(); EX2(SC, 12, e0, e1); EX2(SC, 14, e2, e3); SBAR(); } while (0)
    sfor<0, 16>([&](auto sc_) AI {
      constexpr int s = decltype(sc_)::v, so = (s & 3) * 16384, sn = ((s + 1) & 3) * 16384;
      auto side = [&](auto pc) AI {
        constexpr int P = decltype(pc)::v, kv = kv_at(s, P), wz = wd_at(s, P), hz = h_at(s, P);
        if constexpr (kv >= 0) dma<(kv >> 1) * QT + (kv & 1) * 1024, ((kv >> 1) & 3) * 16384 + (kv & 1) * 1024, 2>(wsr, kvv, lw2k);
        if constexpr (wz >= 0) dma_d(IC<wz>{});
        if constexpr (hz >= 0) hf[hz] = __builtin_amdgcn_raw_buffer_load_b128(wsr, lane * 16 + (hz & 3) * 1024, hld + (hz >> 2) * 4096, SC1);
      };
      auto mid = [&]() AI {
        if (s == 5 && wid == 0) {
          asm volatile("s_waitcnt vmcnt(%1)" : "+v"(fh) : "n"(mid_cnt(5)) : "memory");
          _Pragma("nounroll") for (unsigned sp = 0; !__all(fh == nonce) && sp < SPIN_CAP; ++sp) { fh = flag_ld(ws, fho); PIN(fh); }
        }
        if constexpr (s == 0) vwait_bar<63>(); else if constexpr (s == 1) vwait_bar<0>(); else if constexpr (s < 15) vwait_bar<mid_cnt(s)>();
        if (s == 1 && tid == 0) flag_st(ws, WS_FH + (mg * 16 + ng) * 128, nonce);
        if (s == 4 && wid == 0) asm volatile("buffer_load_dword %0, %1, %2, 0 offen sc1" : "+v"(fh) : "v"(fho), "s"(wsr) : "memory");
        if constexpr (s == 14) { UNROLL for (int k = 27; k >= 13; --k) PIN(hf[k]); }
        __builtin_amdgcn_s_setprio(3);
      };
      auto dn0 = [&](int d) AI { if (d >= 0) { F0 = MFMA(dw0, hf[d], F0); PIN(F0); } if (d >= -1) rd_d0(d + 1); };
      auto dn1 = [&](int d) AI { if (d >= 0) { F1 = MFMA(dw1, hf[d], F1); PIN(F1); } if (d >= -1) rd_d1(d + 1); };
      STEP(sa, sb, QB0, QB1, OB, OA,
           k0 = ld128(kb + sn); side(IC<0>{}); __builtin_amdgcn_s_setprio(2),
           v1 = cat(trd(vb0 + so + 256), trd(vb1 + so + 256)); side(IC<1>{}),
           k1 = ld128(kb + sn + 1024); side(IC<2>{}); __builtin_amdgcn_s_setprio(1),
           side(IC<3>{}), dn0(2 * s - 16), dn1(2 * s - 16));
      STEP(sb, sa, QA0, QA1, OA, OB,
           __builtin_amdgcn_s_setprio(0); side(IC<4>{}),
           side(IC<5>{}),
           mid(),
           v0 = cat(trd(vb0 + sn), trd(vb1 + sn)); side(IC<7>{}), dn0(2 * s - 15), dn1(2 * s - 15));
    });
    pb[2] = pk(e0, e1); pb[3] = pk(e2, e3);
    OB = MFMA(v1, pb, OB);
    __builtin_amdgcn_s_setprio(0);
    TS(11);
    lbar();
    UNROLL for (int g = 0; g < 4; ++g) {
      *(LAS f32x4*)(lds + wid * 8192 + g * 1024 + lane * 16) = (f32x4){OA[4 * g], OA[4 * g + 1], OA[4 * g + 2], OA[4 * g + 3]};
      *(LAS f32x4*)(lds + wid * 8192 + 4096 + g * 1024 + lane * 16) = (f32x4){OB[4 * g], OB[4 * g + 1], OB[4 * g + 2], OB[4 * g + 3]};
    }
    sfor<24, 28>(dma_d);
    wait_bar<4>();
    const int ft = kq & 1, fs = kq >> 1, qt = qg * 4 + wq * 2 + ft;
    f32x4 o0 = {0.f, 0.f, 0.f, 0.f}, o1 = o0; float l = 0.f;
    UNROLL for (int k = 0; k < 4; ++k) {
      const lptr pp = lds + (k * 2 + wq) * 8192 + ft * 4096;
      o0 += *(LAS f32x4*)(pp + fs * 2048 + lane * 16); o1 += *(LAS f32x4*)(pp + fs * 2048 + 1024 + lane * 16);
      l += *(LAS float*)(pp + (odd ? 0 : 3072 + 512) + r * 16);
    }
    if (__builtin_expect(__any(!(l <= 1.8446744e19f)), 0)) {
      const int qo = WS_QKV + head * QH + qt * QT + lane * 16;
      u32x4 q0 = __builtin_amdgcn_raw_buffer_load_b128(wsr, qo, 0, SC1), q1 = __builtin_amdgcn_raw_buffer_load_b128(wsr, qo + 1024, 0, SC1);
      set_pad(q0, q1, 0u, 0u);
      f32x16 O = Z; float m = -__builtin_inff();
      _Pragma("nounroll") for (int t = 0; t < 64; ++t) {
        const int ko = WS_QKV + head * QH + t * QT + 2048;
        f32x16 s = MFMA(__builtin_amdgcn_raw_buffer_load_b128(wsr, ko + lane * 16, 0, SC1), q0, Z);
        s = MFMA(__builtin_amdgcn_raw_buffer_load_b128(wsr, ko + 1024 + lane * 16, 0, SC1), q1, s);
        float mn = m;
        UNROLL for (int i = 0; i < 16; ++i) mn = __builtin_fmaxf(mn, s[i]);
        mn = __builtin_fmaxf(mn, swap32(mn, h));
        const float f = EX(m - mn); m = mn;
        UNROLL for (int i = 0; i < 16; ++i) O[i] *= f;
        UNROLL for (int st = 0; st < 2; ++st) {
          unsigned vv[8];
          UNROLL for (int j = 0; j < 8; ++j) vv[j] = __builtin_amdgcn_raw_buffer_load_b16(wsr, ko + 2048 + (r >> 3) * 512 + (((16 * st + 8 * (j >> 2) + 4 * h + (j & 3)) ^ (4 * (r >> 3))) << 4) + (r & 7) * 2, 0, SC1) & 0xffffu;
          O = MFMA(((u32x4){vv[0] | vv[1] << 16, vv[2] | vv[3] << 16, vv[4] | vv[5] << 16, vv[6] | vv[7] << 16}),
                   ((u32x4){pk(EX(s[8 * st] - m), EX(s[8 * st + 1] - m)), pk(EX(s[8 * st + 2] - m), EX(s[8 * st + 3] - m)),
                            pk(EX(s[8 * st + 4] - m), EX(s[8 * st + 5] - m)), pk(EX(s[8 * st + 6] - m), EX(s[8 * st + 7] - m))}), O);
        }
      }
      const float lc = odd ? O[0] : O[12], lx = swap32(lc, h); l = padl ? lc : lx;
      o0 = fs ? (f32x4){O[8], O[9], O[10], O[11]} : (f32x4){O[0], O[1], O[2], O[3]};
      o1 = fs ? (f32x4){O[12], O[13], O[14], O[15]} : (f32x4){O[4], O[5], O[6], O[7]};
    }
    const float ri = __builtin_amdgcn_rcpf(l);
    const float r0 = padl && odd && fs == 0 ? 0.f : ri, r1 = padl && !odd && fs == 1 ? 0.f : ri;
    st128((u32x4){pk(o0[0] * r0, o0[1] * r0), pk(o0[2] * r0, o0[3] * r0), pk(o1[0] * r1, o1[1] * r1), pk(o1[2] * r1, o1[3] * r1)},
                                           wsr, lane * 16, WS_O + (qt * 16 + head) * 2048 + fs * 1024, SC1);
  }
  TS(12);
  if (idx >= 28) { wait_bar<0>(); if (tid == 0) flag_st(ws, WS_FO + (qg * 16 + head) * 128, nonce); return; }
  {
    lbar();
    {
      const srd_t sO = SRD(wo, 448 * 896); const unsigned vo = vrow + wid * 56 * 896, lo = lds0 + wid * 7168;
      sfor<0, 7>([&](auto ic) AI { constexpr int i = decltype(ic)::v; dma<i * 8 * 896, i * 1024>(sO, vo, lo); });
    }
    const int mt = mg3 * 2 + w3m, ft = w3k & 1, fs = w3k >> 1;
    const unsigned xo = (unsigned)((mt * 32 + r) * 896 + ng3 * 128 + ft * 64 + fs * 32);
    const u32x2 x0 = *(const u32x2*)(x + xo + h * 8), x1 = *(const u32x2*)(x + xo + 16 + h * 8);
    u32x4 ew0, ew1;
    auto down2 = [&](int d, bool more) AI {
      const int o = (((d + 1) >> 2) % 3) * 32768 + ((d + 1) & 3) * 2048;
      ew0 = cat(trd(db0 + o), trd(db0 + o + 1024)); ew1 = cat(trd(db1 + o), trd(db1 + o + 1024));
      SBAR(); F0 = MFMA(dw0, hf[d], F0); F1 = MFMA(dw1, hf[d], F1); PIN(F0); PIN(F1); SBAR();
      if (more) { rd_d0(d + 2); rd_d1(d + 2); }
      SBAR(); F0 = MFMA(ew0, hf[d + 1], F0); F1 = MFMA(ew1, hf[d + 1], F1); PIN(F0); PIN(F1); SBAR();
    };
    down2(16, true); down2(18, true); down2(20, true); down2(22, false);
    wait_bar<0>();
    if (tid == 0) flag_st(ws, WS_FO + (qg * 16 + head) * 128, nonce);
    TS(13);
    rd_d0(24); rd_d1(24); down2(24, true); down2(26, false);
    const lptr ex = lds + L_WD + 32768;
    UNROLL for (int g = 0; g < 4; ++g) {
      *(LAS f32x4*)(ex + wid * 8192 + g * 1024 + lane * 16) = (f32x4){F0[4 * g], F0[4 * g + 1], F0[4 * g + 2], F0[4 * g + 3]};
      *(LAS f32x4*)(ex + wid * 8192 + 4096 + g * 1024 + lane * 16) = (f32x4){F1[4 * g], F1[4 * g + 1], F1[4 * g + 2], F1[4 * g + 3]};
    }
    TS(14);
    if (wid == 0) {
      unsigned fo = 0;
      _Pragma("nounroll") for (unsigned sp = 0; sp < SPIN_CAP; ++sp) { fo = flag_ld(ws, WS_FO + ((mg3 >> 1) * 16 + (lane & 15)) * 128); if (__all(fo == nonce)) break; }
    }
    lbar();
    TS(15);
    u32x4 of[8];
    UNROLL for (int k = 0; k < 8; ++k) of[k] = __builtin_amdgcn_raw_buffer_load_b128(wsr, lane * 16 + (k & 3) * 1024, WS_O + (mt * 16 + w3k * 4) * 2048 + (k >> 2) * 4096, SC1);
    SBAR();
    f32x4 g0 = {0.f, 0.f, 0.f, 0.f}, g1 = g0;
    UNROLL for (int k = 0; k < 4; ++k) { const lptr pp = ex + (k * 2 + w3m) * 8192 + ft * 4096 + fs * 2048 + lane * 16; g0 += *(LAS f32x4*)pp; g1 += *(LAS f32x4*)(pp + 1024); }
    UNROLL for (int i = 0; i < 4; ++i) {
      g0[i] = g0[i] * __builtin_amdgcn_rcpf(1.f + EX(g0[i] * -1.4426950408889634f)); g1[i] = g1[i] * __builtin_amdgcn_rcpf(1.f + EX(g1[i] * -1.4426950408889634f)); }
    PIN(g0); PIN(g1);
    u32x4 wo0[8], wo1[8];
    UNROLL for (int k = 0; k < 8; ++k) {
      const lptr sl = lds + w3k * PART + ((k >> 2) * 56 + ((k >> 1) & 1) * 24 + (k & 1) * 16) * 128;
      wo0[k] = cat(trd(sl + b30), trd(sl + b30 + 1024)); wo1[k] = cat(trd(sl + b31), trd(sl + b31 + 1024));
    }
    lbar();
    f32x16 S0 = Z, S1 = Z;
    UNROLL for (int k = 0; k < 8; ++k) { S0 = MFMA(wo0[k], of[k], S0); S1 = MFMA(wo1[k], of[k], S1); }
    TS(16);
    UNROLL for (int g = 0; g < 4; ++g) {
      *(LAS f32x4*)(ex + wid * 8192 + g * 1024 + lane * 16) = (f32x4){S0[4 * g], S0[4 * g + 1], S0[4 * g + 2], S0[4 * g + 3]};
      *(LAS f32x4*)(ex + wid * 8192 + 4096 + g * 1024 + lane * 16) = (f32x4){S1[4 * g], S1[4 * g + 1], S1[4 * g + 2], S1[4 * g + 3]};
    }
    lbar();
    f32x4 s0 = {0.f, 0.f, 0.f, 0.f}, s1 = s0;
    UNROLL for (int k = 0; k < 4; ++k) { const lptr pp = ex + (k * 2 + w3m) * 8192 + ft * 4096 + fs * 2048 + lane * 16; s0 += *(LAS f32x4*)pp; s1 += *(LAS f32x4*)(pp + 1024); }
    auto lo = [](unsigned v) AI { return __uint_as_float(v << 16); }; auto hi = [](unsigned v) AI { return __uint_as_float(v & 0xffff0000u); };
    const u32x2 a = {pk(__builtin_fmaf(s0[0], g0[0], lo(x0[0])), __builtin_fmaf(s0[1], g0[1], hi(x0[0]))), pk(__builtin_fmaf(s0[2], g0[2], lo(x0[1])), __builtin_fmaf(s0[3], g0[3], hi(x0[1])))};
    const u32x2 c = {pk(__builtin_fmaf(s1[0], g1[0], lo(x1[0])), __builtin_fmaf(s1[1], g1[1], hi(x1[0]))), pk(__builtin_fmaf(s1[2], g1[2], lo(x1[1])), __builtin_fmaf(s1[3], g1[3], hi(x1[1])))};
    *(u32x4*)(out + xo + h * 16) = widen(a, c);
    TS(17); TS_OUT();
  }
}

extern "C" void kernel_launch(void* const* d_in, const int* in_sizes, int n_in, void* d_out, int out_size, void* d_ws, size_t ws_size,
                              hipStream_t stream) {
  (void)hipFuncSetAttribute((const void*)fused_kernel, hipFuncAttributeMaxDynamicSharedMemorySize, LDS_BYTES);
  fused_kernel<<<256, 512, LDS_BYTES, stream>>>((const char*)d_in[0], (const char*)d_in[1], (const char*)d_in[2], (const char*)d_in[4], (const char*)d_in[5],
                                                (const char*)d_in[6], (char*)d_ws, (char*)d_out);
}
```

```cpp
#define PF_CODE 7
#define PF_WD
#include <hip/hip_runtime.h>
#include <hip/hip_bf16.h>
#include <stdint.h>

typedef short bf16x8 __attribute__((ext_vector_type(8)));
typedef short s16x4 __attribute__((ext_vector_type(4)));
typedef float f32x16 __attribute__((ext_vector_type(16)));
typedef float f32x4 __attribute__((ext_vector_type(4)));
typedef float f32x2 __attribute__((ext_vector_type(2)));
typedef unsigned u32x4 __attribute__((ext_vector_type(4)));
typedef unsigned u32x2 __attribute__((ext_vector_type(2)));
typedef __bf16 bf2 __attribute__((ext_vector_type(2)));
typedef __amdgpu_buffer_rsrc_t srd_t;
#define LAS __attribute__((address_space(3)))
#define GAS __attribute__((address_space(1)))
typedef LAS char* lptr;
#define DI __device__ __forceinline__
#define AI __attribute__((always_inline))
#define MFMA(a, b, c) __builtin_amdgcn_mfma_f32_32x32x16_bf16(__builtin_bit_cast(bf16x8, (a)), __builtin_bit_cast(bf16x8, (b)), (c), 0, 0, 0)
#define PIN(x) asm volatile("" : "+v"(x))
#define SBAR() __builtin_amdgcn_sched_barrier(0)
#define RLX_AGENT __ATOMIC_RELAXED, __HIP_MEMORY_SCOPE_AGENT
#define EX(v) __builtin_amdgcn_exp2f(v)
#define UNROLL _Pragma("unroll")
#define SRD(p, n) __builtin_amdgcn_make_buffer_rsrc((void*)(p), 0, (n), 0x00020000)

constexpr int WS_E = 0;
constexpr int WS_FQ = 4096;
constexpr int WS_FH = WS_FQ + 256 * 128;
constexpr int WS_FO = WS_FH + 256 * 128;
constexpr int WS_QKV = 1 << 20;
constexpr int QT = 6144, QH = 64 * QT;
constexpr int WS_O = WS_QKV + 16 * QH;
constexpr int WS_H = WS_O + (2 << 20);
constexpr int WS_END = WS_H + 64 * 112 * 1024;
constexpr int OOB = 1 << 30;
constexpr int SC1 = 16;
constexpr unsigned SPIN_CAP = 1u << 20;

#define TS(i) do { } while (0)
#define TSR(i) do { } while (0)
#define TS_OUT() do { } while (0)

template <int I> struct IC { static constexpr int v = I; };
template <int B, int E, class F> DI void sfor(F&& f) { if constexpr (B < E) { f(IC<B>{}); sfor<B + 1, E>(f); } }
template <int N> DI void wait_bar() { asm volatile("s_waitcnt vmcnt(%0) lgkmcnt(0)\n\ts_barrier" :: "n"(N) : "memory"); }
template <int N> DI void vwait_bar() { asm volatile("s_waitcnt vmcnt(%0)\n\ts_barrier" :: "n"(N) : "memory"); }
DI void lbar() { asm volatile("s_waitcnt lgkmcnt(0)\n\ts_barrier" ::: "memory"); }
DI unsigned pk(float a, float b) { f32x2 v = {a, b}; return __builtin_bit_cast(unsigned, __builtin_convertvector(v, bf2)); }
DI float rbf(float a) { return __uint_as_float(pk(a, a) & 0xffff0000u); }
DI float dot2(unsigned a, unsigned b, float c) { return __builtin_amdgcn_fdot2_f32_bf16(__builtin_bit_cast(bf2, a), __builtin_bit_cast(bf2, b), c, false); }
DI u32x2 trd(lptr p) { return __builtin_bit_cast(u32x2, __builtin_amdgcn_ds_read_tr16_b64_v4i16((LAS s16x4*)p)); }
DI u32x4 cat(u32x2 a, u32x2 b) { return (u32x4){a[0], a[1], b[0], b[1]}; }
DI u32x4 ld128(lptr p) { return *(LAS u32x4*)p; }
DI unsigned bfi(unsigned m, unsigned a, unsigned b) { return (m & a) | (~m & b); }
template <int G, int L, int MODE = 0> DI void dma(srd_t srd, unsigned voff, unsigned lbase) {
  unsigned t;
  if constexpr (MODE == 1)
    asm volatile("s_add_u32 m0, %3, %5\n\ts_mov_b32 %0, %4\n\ts_mov_b32 exec_hi, 0\n\tbuffer_load_dwordx4 %1, %2, %0 offen lds\n\ts_mov_b32 exec_hi, -1"
                 : "=&s"(t) : "v"(voff), "s"(srd), "s"(lbase), "n"(G), "n"(L) : "memory", "scc");
  else if constexpr (MODE == 2)
    asm volatile("s_add_u32 m0, %3, %5\n\ts_mov_b32 %0, %4\n\tbuffer_load_dwordx4 %1, %2, %0 offen sc1 lds" : "=&s"(t) : "v"(voff), "s"(srd), "s"(lbase), "n"(G), "n"(L) : "memory", "scc");
  else
    asm volatile("s_add_u32 m0, %3, %5\n\ts_mov_b32 %0, %4\n\tbuffer_load_dwordx4 %1, %2, %0 offen lds" : "=&s"(t) : "v"(voff), "s"(srd), "s"(lbase), "n"(G), "n"(L) : "memory", "scc");
}
template <int G, int L, int MODE = 0> DI void dmam(srd_t srd, unsigned voff, unsigned lbase, unsigned gmul) {
  unsigned t;
  if constexpr (MODE == 1)
    asm volatile("s_add_u32 m0, %3, %5\n\ts_mul_i32 %0, %6, %4\n\ts_mov_b32 exec_hi, 0\n\tbuffer_load_dwordx4 %1, %2, %0 offen lds\n\ts_mov_b32 exec_hi, -1"
                 : "=&s"(t) : "v"(voff), "s"(srd), "s"(lbase), "n"(G), "n"(L), "s"(gmul) : "memory", "scc");
  else
    asm volatile("s_add_u32 m0, %3, %5\n\ts_mul_i32 %0, %6, %4\n\tbuffer_load_dwordx4 %1, %2, %0 offen lds" : "=&s"(t) : "v"(voff), "s"(srd), "s"(lbase), "n"(G), "n"(L), "s"(gmul) : "memory", "scc");
}
DI void touch(srd_t srd, unsigned voff, unsigned ldst) { asm volatile("s_mov_b32 m0, %2\n\ts_nop 0\n\tbuffer_load_dword %0, %1, 0 offen lds" :: "v"(voff), "s"(srd), "s"(ldst) : "memory"); }
DI void st128(u32x4 v, srd_t srd, int voff, int soff, int aux) { __builtin_amdgcn_raw_buffer_store_b128(v, srd, voff, soff, SC1); SBAR(); asm volatile("s_nop 1"); SBAR(); }
DI float swap32(float v, int h) {
  auto r = __builtin_amdgcn_permlane32_swap(__float_as_uint(v), __float_as_uint(v), false, false);
  return __uint_as_float(h ? r[0] : r[1]);
}
DI u32x4 widen(u32x2 a, u32x2 b) {
  auto r0 = __builtin_amdgcn_permlane32_swap(a[0], b[0], false, false); auto r1 = __builtin_amdgcn_permlane32_swap(a[1], b[1], false, false);
  return (u32x4){r0[0], r1[0], r0[1], r1[1]};
}
DI unsigned flag_ld(const char* ws, int off) { return __hip_atomic_load((GAS unsigned*)(ws + off), RLX_AGENT); }
DI void flag_st(char* ws, int off, unsigned v) { __hip_atomic_store((GAS unsigned*)(ws + off), v, RLX_AGENT); }

constexpr int RA = 2, NWF = RA + 1, UB = 7 - RA;
constexpr int SLOT = 28672, PART = 14336;
constexpr int L_CS = 5 * SLOT;
constexpr int L_ST = L_CS + 2048;
constexpr int L_TB = L_ST + 2048;
constexpr int L_PF = L_TB + 9216;
constexpr int L_WD = 65536;
constexpr int LDS_BYTES = 163840;
constexpr unsigned ONES2 = 0x3f803f80u, PAD1 = 0x00003f80u;

constexpr int kv_at(int s, int p) { return p == 0 && s + 3 < 16 ? (s + 3) * 2 + 1 : p == 7 && s + 4 < 16 ? (s + 4) * 2 : -1; }
constexpr int wd_at(int s, int p) {
  if (s < 4) return p == 1 ? s * 3 : p == 3 ? s * 3 + 1 : p == 4 ? s * 3 + 2 : -1;
  for (int g = 0; g < 3; ++g) { if (s == 10 + 2 * g && p == 7) return 12 + 4 * g; if (s == 11 + 2 * g) return p == 0 ? 13 + 4 * g : p == 2 ? 14 + 4 * g : p == 5 ? 15 + 4 * g : -1; }
  return -1;
}
constexpr int h_at(int s, int p) { return s >= 6 && s <= 12 ? (p == 1 ? (s - 6) * 4 : p == 3 ? (s - 6) * 4 + 1 : p == 4 ? (s - 6) * 4 + 2 : p == 7 ? (s - 6) * 4 + 3 : -1) : -1; }
constexpr int n_at(int s, int p) { return (kv_at(s, p) >= 0) + (wd_at(s, p) >= 0) + (h_at(s, p) >= 0); }
constexpr int mid_cnt(int s) {
  int last = (s - 1) * 8;
  if (s == 12 || s == 14) last = (s - 1) * 8 + 5;
  int n = 0;
  for (int z = last + 1; z < s * 8 + 6; ++z) n += n_at(z / 8, z % 8);
  return n;
}

__global__ __launch_bounds__(512, 2) void fused_kernel(const char* x, const char* wqkv, const char* wo, const char* wg, const char* wu, const char* wd,
                                                       char* ws, char* out) {
  extern __shared__ __attribute__((aligned(16))) char smem[];
  const lptr lds = (lptr)smem;
  const unsigned lds0 = (unsigned)(uintptr_t)lds;
  const int tid = threadIdx.x, lane = tid & 63, wid = __builtin_amdgcn_readfirstlane(tid >> 6);
  const int rr = lane >> 3, cc = lane & 7;
  const int b = blockIdx.x, xcd = b & 7, idx = b >> 3;
  const int ng = (xcd & 3) * 4 + (idx & 3), mg = (xcd >> 2) * 8 + (idx >> 2);
  unsigned tsv = 0; TSR(62); TS(0);
  const srd_t xr = SRD(x, 2048 * 896);
  const unsigned xv0 = (unsigned)((mg * 128 + wid * 16 + rr) * 896 + ((cc ^ rr) << 4));
  const unsigned xv1 = (unsigned)((mg * 128 + wid * 16 + 8 + (rr ^ 1)) * 896 + ((cc ^ rr ^ 1) << 4));
  const unsigned lw2k = lds0 + wid * 2048;
  auto dma_x = [&](auto lc) AI { constexpr int ln = decltype(lc)::v; dma<ln * 128, ln * 16384>(xr, xv0, lw2k); dma<ln * 128, ln * 16384 + 1024>(xr, xv1, lw2k); };
  sfor<0, 4>(dma_x);
  { unsigned long long pc; asm volatile("s_getpc_b64 %0" : "=s"(pc));
    if (wid < 4 && (idx & PF_CODE) == 0) touch(SRD(pc & ~127ull, 30720), tid * 128, lds0 + L_PF + wid * 256); }
  SBAR();
  const int r = lane & 31, h = lane >> 5, q = (lane & 15) >> 2, p = lane & 3, grp = (lane >> 4) & 1;
  const srd_t wsr = SRD(ws, WS_END);
  const f32x16 Z = {};
  const int head = 2 * xcd + (idx & 1), qg = idx >> 1, odd = head & 1;
  const int idx3 = idx < 28 ? idx : idx - 28, mq3 = (idx3 * 37) >> 8, mg3 = xcd * 4 + mq3, ng3 = idx3 - mq3 * 7;
  const unsigned kvv = (unsigned)(WS_QKV + head * QH + (wid >> 1) * 16 * QT + 2048 + (wid & 1) * 2048 + lane * 16);
  unsigned nonce;
  u32x4 QA0, QA1, QB0, QB1;
  {
    const int wm = wid & 3, wn = wid >> 2;
    const bool bq = ng >= 12;
    const int lineA = ng < 15 ? ng : 0, lineB = ng < 15 ? 2 * ng - 9 : 15;
    const unsigned vq = (unsigned)((28 * wm + rr) * 2688 + ((cc ^ (((rr >> 1) & 1) << 2)) << 4));
    const unsigned voC = (unsigned)((28 * wm + rr) * 3584 + ((cc ^ (((rr >> 1) & 1) << 1) ^ (wn << 2)) << 4)) + ng * 128;
    const unsigned voA = vq + lineA * 128 + wn * 112 * 2688, voB = bq ? vq + (lineB + wn) * 128 : voC + 2048;
    const unsigned stB = bq ? 2688 : 3584;
    const char* wgu = wn ? wu : wg;
    const srd_t sA = SRD(wqkv, 448 * 2688), sC = SRD(wgu, 448 * 3584), sB = SRD(bq ? wqkv : wgu, 448 * stB);
    const unsigned wdst = lds0 + wn * PART + wm * 3584;
    auto dma_w = [&](auto ic, auto pcc) AI {
      constexpr int i = decltype(ic)::v, pc = decltype(pcc)::v, L = ((i + 4) % 5) * SLOT + pc * 1024, M = pc == 3;
      if constexpr (i < 2) dma<(i * 224 + pc * 8) * 2688, L, M>(sA, voA, wdst);
      else if constexpr (i < 6) dmam<(i - 2) * 112 + pc * 8, L, M>(sB, voB, wdst, stB);
      else dma<((i - 6) * 112 + pc * 8) * 3584, L, M>(sC, voC, wdst);
    };
    auto dma_w4 = [&](auto ic) AI { sfor<0, 4>([&](auto pc) AI { dma_w(ic, pc); }); };
    if (wm < 3) {
      const int col0 = wm == 0 ? lineA * 64 + wn * 32 : lineB * 64 + wn * 64 + (wm - 1) * 32;
      const int sec = col0 >= 896 ? 2 : col0 >= 448 ? 1 : 0, base = ng < 15 ? WS_QKV + sec * 2048 : OOB;
      unsigned o[2], m[2], o2 = OOB;
      UNROLL for (int pp = 0; pp < 2; ++pp) {
        const int cu = ((col0 - sec * 448) >> 3) + 2 * pp + h, j = (cu * 37) >> 8, w = cu - 7 * j;
        const int hd = 2 * j + (w > 3), ch = w > 3 ? w - 3 : w;
        o[pp] = base + hd * QH + ch * 512 + ((r ^ (sec == 2 ? 4 * ch : 0)) << 4); m[pp] = w == 3 ? ~0u : 0u;
        if (w == 3) o2 = base + (hd + 1) * QH + (r << 4);
      }
      const lptr tb = lds + L_TB + (wn * 3 + wm) * 1536;
      *(LAS u32x4*)(tb + lane * 16) = (u32x4){o[0], o[1], o2, __float_as_uint(sec == 0 ? 0.18898223650461362f * 1.4426950408889634f : 1.f)};
      *(LAS u32x2*)(tb + 1024 + lane * 8) = (u32x2){m[0], m[1]};
    }
    const int rowb = (8 * h + q) * 128 + p * 8;
    const int bA = rowb + (((2 * wn + grp) ^ ((q >> 1) << 1)) << 5);
    const int bC0 = grp * PART + rowb + (((2 * wn) ^ (q >> 1) ^ (grp << 1)) << 5), bC1 = bC0 ^ 32;
    const int bB0 = bq ? wn * PART + rowb + ((grp ^ ((q >> 1) << 1)) << 5) : bC0, bB1 = bB0 ^ (bq ? 64 : 32);
    u32x4 xf[28];
    float st0 = 0.f, st1 = 0.f;
    {
      const int j = r & 7, rg = 4 * wm + (r >> 3), i = j ^ (rg & 1);
      const lptr xb0 = lds + rg * 1024 + i * 128;
      sfor<0, 4>([&](auto gc) AI {
        constexpr int g = decltype(gc)::v, k0 = 8 * g, k1 = g < 3 ? k0 + 8 : 28;
        SBAR();
        if (g == 0) wait_bar<4>(); else if (g == 1) wait_bar<4>(); else if (g == 2) wait_bar<6>(); else wait_bar<4>();
        if (g == 0) TS(1);
        UNROLL for (int ks = k0; ks < k1; ++ks) xf[ks] = ld128(xb0 + (ks >> 2) * 16384 + (((((ks & 3) << 1) + h) ^ j) << 4));
        if constexpr (g == 0) sfor<4, 6>(dma_x);
        if constexpr (g == 1) { dma_x(IC<6>{}); dma_w4(IC<0>{}); }
        if constexpr (g >= 2) dma_w4(IC<g - 1>{});
        if (wn == 0) { UNROLL for (int ks = k0; ks < k1; ++ks) { st0 = dot2(xf[ks][0], ONES2, st0); st1 = dot2(xf[ks][1], ONES2, st1); st0 = dot2(xf[ks][2], ONES2, st0); st1 = dot2(xf[ks][3], ONES2, st1); } }
        else { UNROLL for (int ks = k0; ks < k1; ++ks) { st0 = dot2(xf[ks][0], xf[ks][0], st0); st1 = dot2(xf[ks][1], xf[ks][1], st1); st0 = dot2(xf[ks][2], xf[ks][2], st0); st1 = dot2(xf[ks][3], xf[ks][3], st1); } }
        PIN(st0); PIN(st1);
      });
    }
    *(LAS float*)(lds + L_ST + (wm * 32 + r) * 16 + (wn * 2 + h) * 4) = st0 + st1;
    u32x2 wf[NWF][4];
    int ra0 = 0, ra1 = 0;
    auto rd_w = [&](int n) AI {
      const int i = n / 7, u = n % 7;
      if (u == 0) { ra0 = (i < 2 ? bA : i < 6 ? bB0 : bC0) + ((i + 4) % 5) * SLOT; ra1 = (i < 2 ? bA : i < 6 ? bB1 : bC1) + ((i + 4) % 5) * SLOT; PIN(ra0); if (i >= 2) PIN(ra1); }
      const int o0 = i < 2 ? u * 4096 : u * 2048, o1 = i < 2 ? u * 4096 + 2048 : u * 2048, a1 = i < 2 ? ra0 : ra1;
      wf[n % NWF][0] = trd(lds + ra0 + o0); wf[n % NWF][1] = trd(lds + ra0 + o0 + 512); wf[n % NWF][2] = trd(lds + a1 + o1); wf[n % NWF][3] = trd(lds + a1 + o1 + 512);
    };
    UNROLL for (int n = 0; n < RA; ++n) rd_w(n);
    TS(2);
    f32x16 A0 = Z, B0 = Z, B1 = Z, C0 = Z, C1 = Z;
    float cs0 = 0.f, cs1 = 0.f, rstd = 0.f, sig = 0.f, kg = 0.f; unsigned mm = 0, epoch = 0;
    const int mt = mg * 4 + wm;
    const unsigned csel = ((wm & 1) ? 0x05040c0cu : 0x01000c0cu) + ((wm >> 1) ? 0x02020000u : 0u);
    auto colsum = [&](int n, int hh) AI {
      cs0 += __uint_as_float(__builtin_amdgcn_perm(wf[n % NWF][2 + hh][0], wf[n % NWF][hh][0], csel)); cs1 += __uint_as_float(__builtin_amdgcn_perm(wf[n % NWF][2 + hh][1], wf[n % NWF][hh][1], csel));
      PIN(cs0); PIN(cs1);
    };
    auto fix1 = [&](f32x16& acc) AI {
      acc = MFMA(ld128(lds + L_CS + wn * 1024 + lane * 16), ((u32x4){mm, mm, mm, mm}), acc); };
    auto fix2 = [&](f32x16& a0, f32x16& a1) AI {
      const u32x4 c = ld128(lds + L_CS + wn * 1024 + lane * 16);
      a0 = MFMA(c, ((u32x4){mm, mm, 0u, 0u}), a0); a1 = MFMA(c, ((u32x4){0u, 0u, mm, mm}), a1); };
    auto epi_qkv = [&](const f32x16& acc, int t) AI {
      const lptr tb = lds + L_TB + (wn * 3 + t) * 1536;
      const u32x4 o = ld128(tb + lane * 16); const u32x2 m = *(LAS u32x2*)(tb + 1024 + lane * 8);
      const float sc = rstd * __uint_as_float(o[3]);
      u32x4 v[2];
      UNROLL for (int pp = 0; pp < 2; ++pp) {
        v[pp] = widen((u32x2){pk(acc[8 * pp] * sc, acc[8 * pp + 1] * sc), pk(acc[8 * pp + 2] * sc, acc[8 * pp + 3] * sc)},
                      (u32x2){pk(acc[8 * pp + 4] * sc, acc[8 * pp + 5] * sc), pk(acc[8 * pp + 6] * sc, acc[8 * pp + 7] * sc)});
        st128((u32x4){v[pp][0], v[pp][1], bfi(m[pp], ONES2, v[pp][2]), bfi(m[pp], PAD1, v[pp][3])}, wsr, o[pp], mt * QT, SC1);
      }
      st128((u32x4){ONES2, PAD1, bfi(m[0], v[0][2], v[1][2]), bfi(m[0], v[0][3], v[1][3])}, wsr, o[2], mt * QT, SC1);
    };
    auto glu = [&](const f32x16& acc, int i) AI { return acc[i + 8] * __builtin_amdgcn_rcpf(__builtin_fmaf(EX(acc[i] * kg), sig, sig)); };
    const int hsC = WS_H + (mt * 112 + ng * 4 + wn * 2) * 1024, hvB = bq ? OOB : lane * 16;
    float hv[8]; u32x4 hp; unsigned fl0 = 0, fl1 = 1, fl2 = 0, fl3 = 0;

    sfor<0, 70>([&](auto nc) AI {
      constexpr int n = decltype(nc)::v, i = n / 7, u = n % 7;
      constexpr int k0 = i < 2 ? 2 * n : ((i - 2) & 3) * 7 + u, k1 = i < 2 ? 2 * n + 1 : k0;
      f32x16& D0 = i < 2 ? A0 : i < 6 ? B0 : C0; f32x16& D1 = i < 2 ? A0 : i < 6 ? B1 : C1;
      constexpr bool last = i == 1 || i == 5 || i == 9;
      PIN(wf[n % NWF][3]);
      if (u == UB + 1) __builtin_amdgcn_s_setprio(3); if (u == 1) __builtin_amdgcn_s_setprio(2); if (u == 2) __builtin_amdgcn_s_setprio(1); if (u == 4) __builtin_amdgcn_s_setprio(0);
      SBAR();
      D0 = MFMA(cat(wf[n % NWF][0], wf[n % NWF][1]), xf[k0], D0);
      SBAR();
      if constexpr (u == UB) {
        if constexpr (last) {
          UNROLL for (int e = 0; e < RA; ++e) { colsum(n + e, 0); colsum(n + e, 1); }
          const float cs = cs0 + cs1, hi = rbf(cs); *(LAS unsigned*)(lds + L_CS + wn * 1024 + lane * 16 + ((wm & 1) * 2 + (wm >> 1)) * 4) = pk(hi, cs - hi); cs0 = 0.f; cs1 = 0.f;
        }
        if (i == 9 && wid == 0) {
          _Pragma("nounroll") for (unsigned sp = 0; !__all(fl0 == nonce && fl1 == nonce && fl2 == nonce && fl3 == nonce) && sp < SPIN_CAP; ++sp) {
            fl0 = flag_ld(ws, WS_FQ + lane * 128); fl1 = flag_ld(ws, WS_FQ + 8192 + lane * 128); fl2 = flag_ld(ws, WS_FQ + 16384 + lane * 128); fl3 = flag_ld(ws, WS_FQ + 24576 + lane * 128);
          }
          if (b == 0 && lane == 0) flag_st(ws, WS_E, nonce);
        }
        if (i == 0) wait_bar<8>(); else if (i < 6) wait_bar<12>(); else if (i == 6) wait_bar<8>(); else if (i == 7) wait_bar<0>(); else lbar();
        if constexpr (i == 7) { PIN(epoch); nonce = __builtin_amdgcn_readfirstlane(epoch) * 0x9E3779B1u + 0x7F4A7C15u; if (tid == 0) flag_st(ws, WS_FQ + b * 128, nonce); }
        if constexpr (i == 9) {
          sfor<0, 6>([&](auto zc) AI { constexpr int z = decltype(zc)::v; dma<(z >> 1) * QT + (z & 1) * 1024, (z >> 1) * 16384 + (z & 1) * 1024, 2>(wsr, kvv, lw2k); });
          const int qo = WS_QKV + head * QH + (qg * 4 + (wid & 1) * 2) * QT;
          QA0 = __builtin_amdgcn_raw_buffer_load_b128(wsr, lane * 16, qo, SC1); QA1 = __builtin_amdgcn_raw_buffer_load_b128(wsr, lane * 16 + 1024, qo, SC1);
          QB0 = __builtin_amdgcn_raw_buffer_load_b128(wsr, lane * 16, qo + QT, SC1); QB1 = __builtin_amdgcn_raw_buffer_load_b128(wsr, lane * 16 + 1024, qo + QT, SC1);
        }
        if (i == 1) TS(3); if (i == 3) TS(4); if (i == 5) TS(5); if (i == 7) TS(6); if (i == 9) TS(7);
        SBAR();
      }
      if constexpr (n + RA < 70) rd_w(n + RA);
      if (!(last && u >= UB)) colsum(n, 0);
      SBAR();
      D1 = MFMA(cat(wf[n % NWF][2], wf[n % NWF][3]), xf[k1], D1);
      SBAR();
      if (!(last && u >= UB)) colsum(n, 1);
      if constexpr (n < 4) dma_w(IC<3>{}, IC<n>{});
      if constexpr (n >= UB && n < UB + 7) { constexpr int v = n - UB; if constexpr (v < 4) dma_w(IC<4>{}, IC<v>{}); if constexpr (v >= 3) dma_w(IC<5>{}, IC<v - 3>{}); }
      if constexpr (n >= UB + 7 && n < 35 + UB) { constexpr int j = (n - UB) / 7, v = (n - UB) % 7; if constexpr ((v & 1) == 0) dma_w(IC<j + 5>{}, IC<v / 2>{}); }
      if constexpr (n == UB + 1) {
        const f32x4 a = *(LAS f32x4*)(lds + L_ST + (wm * 32 + r) * 16);
        const float mean = (a[0] + a[1]) * (1.f / 448.f);
        const float var = __builtin_fmaxf((a[2] + a[3]) * (1.f / 448.f) - mean * mean, 0.f);
        sig = rbf(__builtin_amdgcn_sqrtf(rbf(rbf(var) + 1e-5f))); rstd = __builtin_amdgcn_rcpf(sig); mm = pk(-mean, -mean); kg = -1.4426950408889634f * rstd;
        PIN(sig); PIN(rstd); PIN(mm); PIN(kg);
      }
      if constexpr (n == 13) fix1(A0);
      if ((n == 14 && wn == 0) || (n == 16 && wn == 1)) epi_qkv(A0, 0);
      if constexpr (n == 30) epoch = flag_ld(ws, WS_E);
      if constexpr (n == 41) fix2(B0, B1);
      if (n == 42 && bq) { epi_qkv(B0, 1); epi_qkv(B1, 2); }
      if constexpr (n >= 42 && n < 66 && (n - 42) % 3 != 2) {
        constexpr int e = (n - 42) / 3 * 2 + (n - 42) % 3; hv[e & 7] = glu(e < 8 ? B0 : B1, e & 7); PIN(hv[e & 7]);
        if ((e & 7) == 7) { hp = (u32x4){pk(hv[0], hv[1]), pk(hv[2], hv[3]), pk(hv[4], hv[5]), pk(hv[6], hv[7])}; PIN(hp); }
        if (e == 9 || e == 15) st128(hp, wsr, hvB + (e == 9 ? 0 : 1024), hsC + 65536, SC1);
      }
      if (n == 64 && wid == 0) { fl0 = flag_ld(ws, WS_FQ + lane * 128); fl1 = flag_ld(ws, WS_FQ + 8192 + lane * 128); fl2 = flag_ld(ws, WS_FQ + 16384 + lane * 128); fl3 = flag_ld(ws, WS_FQ + 24576 + lane * 128); }
      if constexpr (n == 56) { const int l = idx * 448 + wid * 56 + lane; touch(SRD(wd, 1792 * 896), lane < 56 ? l * 128 : OOB, lds0 + L_PF + wid * 256);
                               touch(SRD(wo, 448 * 896), (idx * 8 + wid) * 64 + lane < 3136 && wid < 2 ? ((idx * 2 + wid) * 64 + lane) * 128 : OOB, lds0 + L_PF + wid * 256); }
    });
    SBAR();
    fix2(C0, C1);
    UNROLL for (int t = 0; t < 2; ++t) {
      UNROLL for (int e = 0; e < 8; ++e) hv[e] = glu(t ? C1 : C0, e);
      st128((u32x4){pk(hv[0], hv[1]), pk(hv[2], hv[3]), pk(hv[4], hv[5]), pk(hv[6], hv[7])}, wsr, lane * 16 + t * 1024, hsC, SC1);
    }
    SBAR();
    TS(8);
  }
  const int w3m = wid & 1, w3k = wid >> 1;
  u32x4 hf[28];
  const unsigned vrow = (unsigned)(rr * 896 + ((cc ^ (((rr >> 1) & 1) << 2)) << 4) + ng3 * 128);
  const unsigned vo_d = vrow + (w3k * 448 + w3m * 32) * 896, ld_d = lds0 + L_WD + wid * 4096;
  const srd_t sD = SRD(wd, 1792 * 896);
  auto dma_d = [&](auto zc) AI { constexpr int z = decltype(zc)::v; dma<((z >> 2) * 64 + (z & 3) * 8) * 896, ((z >> 2) % 3) * 32768 + (z & 3) * 1024>(sD, vo_d, ld_d); };
  const int b30 = (4 * h + q) * 128 + p * 8 + ((grp ^ ((q >> 1) << 1)) << 5), b31 = b30 ^ 64;
  const lptr db0 = lds + L_WD + w3k * 8192 + b30, db1 = lds + L_WD + w3k * 8192 + b31;
  f32x16 F0 = Z, F1 = Z;
  u32x4 dw0, dw1;
  auto rd_d0 = [&](int d) AI { const int o = ((d >> 2) % 3) * 32768 + (d & 3) * 2048; dw0 = cat(trd(db0 + o), trd(db0 + o + 1024)); };
  auto rd_d1 = [&](int d) AI { const int o = ((d >> 2) % 3) * 32768 + (d & 3) * 2048; dw1 = cat(trd(db1 + o), trd(db1 + o + 1024)); };
  {
    const int wq = wid & 1, kq = wid >> 1;
    const lptr kb = lds + kq * 4096 + lane * 16;
    const int vc = 2 * grp + (p >> 1);
    const lptr vb0 = lds + kq * 4096 + 2048 + vc * 512 + ((((4 * h) ^ (4 * vc)) + q) << 4) + (p & 1) * 8;
    const lptr vb1 = lds + kq * 4096 + 2048 + vc * 512 + ((((4 * h + 8) ^ (4 * vc)) + q) << 4) + (p & 1) * 8;
    const bool padl = h != odd;
    auto set_pad = [&](u32x4& q0, u32x4& q1, unsigned a, unsigned c) AI {
      const bool p0 = padl && odd, p1 = padl && !odd;
      q0[0] = p0 ? a : q0[0]; q0[1] = p0 ? c : q0[1]; q1[2] = p1 ? a : q1[2]; q1[3] = p1 ? c : q1[3]; };
    auto set_shift = [&](u32x4& q0, u32x4& q1, float m) AI {
      const float m0 = rbf(m), m1 = rbf(m - m0), m2 = (m - m0) - m1; set_pad(q0, q1, pk(-m0, -m1), pk(-m2, 0.f)); };
    wait_bar<2>();
    PIN(QB1); PIN(QB0); PIN(QA1); PIN(QA0);
    TS(9);
    dma<3 * QT, 3 * 16384, 2>(wsr, kvv, lw2k);
    {
      const u32x4 k0 = ld128(lds + lane * 16), k1 = ld128(lds + 1024 + lane * 16);
      f32x16 sa = MFMA(k0, QA0, Z), sb = MFMA(k0, QB0, Z); sa = MFMA(k1, QA1, sa); sb = MFMA(k1, QB1, sb);
      float ma = sa[0], mb = sb[0];
      UNROLL for (int i = 1; i < 16; ++i) { ma = __builtin_fmaxf(ma, sa[i]); mb = __builtin_fmaxf(mb, sb[i]); }
      ma = __builtin_fmaxf(ma, swap32(ma, h)); mb = __builtin_fmaxf(mb, swap32(mb, h));
      set_shift(QA0, QA1, ma - 3.f); set_shift(QB0, QB1, mb - 3.f);
    }
    u32x4 k0 = ld128(kb), k1 = ld128(kb + 1024), v0 = cat(trd(vb0), trd(vb1)), v1 = {0u, 0u, 0u, 0u}, pa = v1, pb = v1;
    float e0 = 0.f, e1 = 0.f, e2 = 0.f, e3 = 0.f;
    f32x16 OA = Z, OB = Z, sa = MFMA(k0, QA0, Z), sb; sa = MFMA(k1, QA1, sa);
    unsigned fh = ~nonce; const int fho = WS_FH + ((mg3 >> 1) * 16 + (lane & 15)) * 128;
    static_assert(mid_cnt(14) == 0 && mid_cnt(5) == n_at(4, 7) + n_at(5, 0), "schedule");
    const int hld = WS_H + ((mg3 * 2 + w3m) * 112 + w3k * 28) * 1024;
    TS(10);
#define EX2(S, i, a, c) do { a = EX(S[i]); c = EX(S[(i) + 1]); PIN(a); PIN(c); } while (0)
#define STEP(SC, SN, QN0, QN1, OP, OC, L0, L1, L2, L3, X0, X1) do {                                                       \
      SBAR(); SN = MFMA(k0, QN0, Z); SBAR(); L0;                                                                          \
      pb[2] = pk(e0, e1); pb[3] = pk(e2, e3); PIN(pb); SBAR(); EX2(SC, 0, e0, e1); SBAR(); X0; SBAR(); EX2(SC, 2, e2, e3); \
      SBAR(); OP = MFMA(v1, pb, OP); SBAR(); L1;                                                                          \
      pa[0] = pk(e0, e1); pa[1] = pk(e2, e3); PIN(pa); SBAR(); EX2(SC, 4, e0, e1); EX2(SC, 6, e2, e3);                    \
      SBAR(); SN = MFMA(k1, QN1, SN); SBAR(); L2;                                                                         \
      pa[2] = pk(e0, e1); pa[3] = pk(e2, e3); PIN(pa); SBAR(); EX2(SC, 8, e0, e1); SBAR(); X1; SBAR(); EX2(SC, 10, e2, e3); \
      SBAR(); OC = MFMA(v0, pa, OC); SBAR(); L3;                                                                          \
      pb[0] = pk(e0, e1); pb[1] = pk(e2, e3); PIN(pb); SBAR(); EX2(SC, 12, e0, e1); EX2(SC, 14, e2, e3); SBAR(); } while (0)
    sfor<0, 16>([&](auto sc_) AI {
      constexpr int s = decltype(sc_)::v, so = (s & 3) * 16384, sn = ((s + 1) & 3) * 16384;
      auto side = [&](auto pc) AI {
        constexpr int P = decltype(pc)::v, kv = kv_at(s, P), wz = wd_at(s, P), hz = h_at(s, P);
        if constexpr (kv >= 0) dma<(kv >> 1) * QT + (kv & 1) * 1024, ((kv >> 1) & 3) * 16384 + (kv & 1) * 1024, 2>(wsr, kvv, lw2k);
        if constexpr (wz >= 0) dma_d(IC<wz>{});
        if constexpr (hz >= 0) hf[hz] = __builtin_amdgcn_raw_buffer_load_b128(wsr, lane * 16 + (hz & 3) * 1024, hld + (hz >> 2) * 4096, SC1);
      };
      auto mid = [&]() AI {
        if (s == 5 && wid == 0) {
          asm volatile("s_waitcnt vmcnt(%1)" : "+v"(fh) : "n"(mid_cnt(5)) : "memory");
          _Pragma("nounroll") for (unsigned sp = 0; !__all(fh == nonce) && sp < SPIN_CAP; ++sp) { fh = flag_ld(ws, fho); PIN(fh); }
        }
        if constexpr (s == 0) vwait_bar<63>(); else if constexpr (s == 1) vwait_bar<0>(); else if constexpr (s < 15) vwait_bar<mid_cnt(s)>();
        if (s == 1 && tid == 0) flag_st(ws, WS_FH + (mg * 16 + ng) * 128, nonce);
        if (s == 4 && wid == 0) asm volatile("buffer_load_dword %0, %1, %2, 0 offen sc1" : "+v"(fh) : "v"(fho), "s"(wsr) : "memory");
        if constexpr (s == 14) { UNROLL for (int k = 27; k >= 13; --k) PIN(hf[k]); }
        __builtin_amdgcn_s_setprio(3);
      };
      auto dn0 = [&](int d) AI { if (d >= 0) { F0 = MFMA(dw0, hf[d], F0); PIN(F0); } if (d >= -1) rd_d0(d + 1); };
      auto dn1 = [&](int d) AI { if (d >= 0) { F1 = MFMA(dw1, hf[d], F1); PIN(F1); } if (d >= -1) rd_d1(d + 1); };
      STEP(sa, sb, QB0, QB1, OB, OA,
           k0 = ld128(kb + sn); side(IC<0>{}); __builtin_amdgcn_s_setprio(2),
           v1 = cat(trd(vb0 + so + 256), trd(vb1 + so + 256)); side(IC<1>{}),
           k1 = ld128(kb + sn + 1024); side(IC<2>{}); __builtin_amdgcn_s_setprio(1),
           side(IC<3>{}), dn0(2 * s - 16), dn1(2 * s - 16));
      STEP(sb, sa, QA0, QA1, OA, OB,
           __builtin_amdgcn_s_setprio(0); side(IC<4>{}),
           side(IC<5>{}),
           mid(),
           v0 = cat(trd(vb0 + sn), trd(vb1 + sn)); side(IC<7>{}), dn0(2 * s - 15), dn1(2 * s - 15));
    });
    pb[2] = pk(e0, e1); pb[3] = pk(e2, e3);
    OB = MFMA(v1, pb, OB);
    __builtin_amdgcn_s_setprio(0);
    TS(11);
    lbar();
    UNROLL for (int g = 0; g < 4; ++g) {
      *(LAS f32x4*)(lds + wid * 8192 + g * 1024 + lane * 16) = (f32x4){OA[4 * g], OA[4 * g + 1], OA[4 * g + 2], OA[4 * g + 3]};
      *(LAS f32x4*)(lds + wid * 8192 + 4096 + g * 1024 + lane * 16) = (f32x4){OB[4 * g], OB[4 * g + 1], OB[4 * g + 2], OB[4 * g + 3]};
    }
    sfor<24, 28>(dma_d);
    wait_bar<4>();
    const int ft = kq & 1, fs = kq >> 1, qt = qg * 4 + wq * 2 + ft;
    f32x4 o0 = {0.f, 0.f, 0.f, 0.f}, o1 = o0; float l = 0.f;
    UNROLL for (int k = 0; k < 4; ++k) {
      const lptr pp = lds + (k * 2 + wq) * 8192 + ft * 4096;
      o0 += *(LAS f32x4*)(pp + fs * 2048 + lane * 16); o1 += *(LAS f32x4*)(pp + fs * 2048 + 1024 + lane * 16);
      l += *(LAS float*)(pp + (odd ? 0 : 3072 + 512) + r * 16);
    }
    if (__builtin_expect(__any(!(l <= 1.8446744e19f)), 0)) {
      const int qo = WS_QKV + head * QH + qt * QT + lane * 16;
      u32x4 q0 = __builtin_amdgcn_raw_buffer_load_b128(wsr, qo, 0, SC1), q1 = __builtin_amdgcn_raw_buffer_load_b128(wsr, qo + 1024, 0, SC1);
      set_pad(q0, q1, 0u, 0u);
      f32x16 O = Z; float m = -__builtin_inff();
      _Pragma("nounroll") for (int t = 0; t < 64; ++t) {
        const int ko = WS_QKV + head * QH + t * QT + 2048;
        f32x16 s = MFMA(__builtin_amdgcn_raw_buffer_load_b128(wsr, ko + lane * 16, 0, SC1), q0, Z);
        s = MFMA(__builtin_amdgcn_raw_buffer_load_b128(wsr, ko + 1024 + lane * 16, 0, SC1), q1, s);
        float mn = m;
        UNROLL for (int i = 0; i < 16; ++i) mn = __builtin_fmaxf(mn, s[i]);
        mn = __builtin_fmaxf(mn, swap32(mn, h));
        const float f = EX(m - mn); m = mn;
        UNROLL for (int i = 0; i < 16; ++i) O[i] *= f;
        UNROLL for (int st = 0; st < 2; ++st) {
          unsigned vv[8];
          UNROLL for (int j = 0; j < 8; ++j) vv[j] = __builtin_amdgcn_raw_buffer_load_b16(wsr, ko + 2048 + (r >> 3) * 512 + (((16 * st + 8 * (j >> 2) + 4 * h + (j & 3)) ^ (4 * (r >> 3))) << 4) + (r & 7) * 2, 0, SC1) & 0xffffu;
          O = MFMA(((u32x4){vv[0] | vv[1] << 16, vv[2] | vv[3] << 16, vv[4] | vv[5] << 16, vv[6] | vv[7] << 16}),
                   ((u32x4){pk(EX(s[8 * st] - m), EX(s[8 * st + 1] - m)), pk(EX(s[8 * st + 2] - m), EX(s[8 * st + 3] - m)),
                            pk(EX(s[8 * st + 4] - m), EX(s[8 * st + 5] - m)), pk(EX(s[8 * st + 6] - m), EX(s[8 * st + 7] - m))}), O);
        }
      }
      const float lc = odd ? O[0] : O[12], lx = swap32(lc, h); l = padl ? lc : lx;
      o0 = fs ? (f32x4){O[8], O[9], O[10], O[11]} : (f32x4){O[0], O[1], O[2], O[3]};
      o1 = fs ? (f32x4){O[12], O[13], O[14], O[15]} : (f32x4){O[4], O[5], O[6], O[7]};
    }
    const float ri = __builtin_amdgcn_rcpf(l);
    const float r0 = padl && odd && fs == 0 ? 0.f : ri, r1 = padl && !odd && fs == 1 ? 0.f : ri;
    st128((u32x4){pk(o0[0] * r0, o0[1] * r0), pk(o0[2] * r0, o0[3] * r0), pk(o1[0] * r1, o1[1] * r1), pk(o1[2] * r1, o1[3] * r1)},
                                           wsr, lane * 16, WS_O + (qt * 16 + head) * 2048 + fs * 1024, SC1);
  }
  TS(12);
  if (idx >= 28) { wait_bar<0>(); if (tid == 0) flag_st(ws, WS_FO + (qg * 16 + head) * 128, nonce); return; }
  {
    lbar();
    {
      const srd_t sO = SRD(wo, 448 * 896); const unsigned vo = vrow + wid * 56 * 896, lo = lds0 + wid * 7168;
      sfor<0, 7>([&](auto ic) AI { constexpr int i = decltype(ic)::v; dma<i * 8 * 896, i * 1024>(sO, vo, lo); });
    }
    const int mt = mg3 * 2 + w3m, ft = w3k & 1, fs = w3k >> 1;
    const unsigned xo = (unsigned)((mt * 32 + r) * 896 + ng3 * 128 + ft * 64 + fs * 32);
    const u32x2 x0 = *(const u32x2*)(x + xo + h * 8), x1 = *(const u32x2*)(x + xo + 16 + h * 8);
    u32x4 ew0, ew1;
    auto down2 = [&](int d, bool more) AI {
      const int o = (((d + 1) >> 2) % 3) * 32768 + ((d + 1) & 3) * 2048;
      ew0 = cat(trd(db0 + o), trd(db0 + o + 1024)); ew1 = cat(trd(db1 + o), trd(db1 + o + 1024));
      SBAR(); F0 = MFMA(dw0, hf[d], F0); F1 = MFMA(dw1, hf[d], F1); PIN(F0); PIN(F1); SBAR();
      if (more) { rd_d0(d + 2); rd_d1(d + 2); }
      SBAR(); F0 = MFMA(ew0, hf[d + 1], F0); F1 = MFMA(ew1, hf[d + 1], F1); PIN(F0); PIN(F1); SBAR();
    };
    down2(16, true); down2(18, true); down2(20, true); down2(22, false);
    wait_bar<0>();
    if (tid == 0) flag_st(ws, WS_FO + (qg * 16 + head) * 128, nonce);
    TS(13);
    rd_d0(24); rd_d1(24); down2(24, true); down2(26, false);
    const lptr ex = lds + L_WD + 32768;
    UNROLL for (int g = 0; g < 4; ++g) {
      *(LAS f32x4*)(ex + wid * 8192 + g * 1024 + lane * 16) = (f32x4){F0[4 * g], F0[4 * g + 1], F0[4 * g + 2], F0[4 * g + 3]};
      *(LAS f32x4*)(ex + wid * 8192 + 4096 + g * 1024 + lane * 16) = (f32x4){F1[4 * g], F1[4 * g + 1], F1[4 * g + 2], F1[4 * g + 3]};
    }
    TS(14);
    if (wid == 0) {
      unsigned fo = 0;
      _Pragma("nounroll") for (unsigned sp = 0; sp < SPIN_CAP; ++sp) { fo = flag_ld(ws, WS_FO + ((mg3 >> 1) * 16 + (lane & 15)) * 128); if (__all(fo == nonce)) break; }
    }
    lbar();
    TS(15);
    u32x4 of[8];
    UNROLL for (int k = 0; k < 8; ++k) of[k] = __builtin_amdgcn_raw_buffer_load_b128(wsr, lane * 16 + (k & 3) * 1024, WS_O + (mt * 16 + w3k * 4) * 2048 + (k >> 2) * 4096, SC1);
    SBAR();
    f32x4 g0 = {0.f, 0.f, 0.f, 0.f}, g1 = g0;
    UNROLL for (int k = 0; k < 4; ++k) { const lptr pp = ex + (k * 2 + w3m) * 8192 + ft * 4096 + fs * 2048 + lane * 16; g0 += *(LAS f32x4*)pp; g1 += *(LAS f32x4*)(pp + 1024); }
    UNROLL for (int i = 0; i < 4; ++i) {
      g0[i] = g0[i] * __builtin_amdgcn_rcpf(1.f + EX(g0[i] * -1.4426950408889634f)); g1[i] = g1[i] * __builtin_amdgcn_rcpf(1.f + EX(g1[i] * -1.4426950408889634f)); }
    PIN(g0); PIN(g1);
    u32x4 wo0[8], wo1[8];
    UNROLL for (int k = 0; k < 8; ++k) {
      const lptr sl = lds + w3k * PART + ((k >> 2) * 56 + ((k >> 1) & 1) * 24 + (k & 1) * 16) * 128;
      wo0[k] = cat(trd(sl + b30), trd(sl + b30 + 1024)); wo1[k] = cat(trd(sl + b31), trd(sl + b31 + 1024));
    }
    lbar();
    f32x16 S0 = Z, S1 = Z;
    UNROLL for (int k = 0; k < 8; ++k) { S0 = MFMA(wo0[k], of[k], S0); S1 = MFMA(wo1[k], of[k], S1); }
    TS(16);
    UNROLL for (int g = 0; g < 4; ++g) {
      *(LAS f32x4*)(ex + wid * 8192 + g * 1024 + lane * 16) = (f32x4){S0[4 * g], S0[4 * g + 1], S0[4 * g + 2], S0[4 * g + 3]};
      *(LAS f32x4*)(ex + wid * 8192 + 4096 + g * 1024 + lane * 16) = (f32x4){S1[4 * g], S1[4 * g + 1], S1[4 * g + 2], S1[4 * g + 3]};
    }
    lbar();
    f32x4 s0 = {0.f, 0.f, 0.f, 0.f}, s1 = s0;
    UNROLL for (int k = 0; k < 4; ++k) { const lptr pp = ex + (k * 2 + w3m) * 8192 + ft * 4096 + fs * 2048 + lane * 16; s0 += *(LAS f32x4*)pp; s1 += *(LAS f32x4*)(pp + 1024); }
    auto lo = [](unsigned v) AI { return __uint_as_float(v << 16); }; auto hi = [](unsigned v) AI { return __uint_as_float(v & 0xffff0000u); };
    const u32x2 a = {pk(__builtin_fmaf(s0[0], g0[0], lo(x0[0])), __builtin_fmaf(s0[1], g0[1], hi(x0[0]))), pk(__builtin_fmaf(s0[2], g0[2], lo(x0[1])), __builtin_fmaf(s0[3], g0[3], hi(x0[1])))};
    const u32x2 c = {pk(__builtin_fmaf(s1[0], g1[0], lo(x1[0])), __builtin_fmaf(s1[1], g1[1], hi(x1[0]))), pk(__builtin_fmaf(s1[2], g1[2], lo(x1[1])), __builtin_fmaf(s1[3], g1[3], hi(x1[1])))};
    *(u32x4*)(out + xo + h * 16) = widen(a, c);
    TS(17); TS_OUT();
  }
}

extern "C" void kernel_launch(void* const* d_in, const int* in_sizes, int n_in, void* d_out, int out_size, void* d_ws, size_t ws_size,
                              hipStream_t stream) {
  (void)hipFuncSetAttribute((const void*)fused_kernel, hipFuncAttributeMaxDynamicSharedMemorySize, LDS_BYTES);
  fused_kernel<<<256, 512, LDS_BYTES, stream>>>((const char*)d_in[0], (const char*)d_in[1], (const char*)d_in[2], (const char*)d_in[4], (const char*)d_in[5],
                                                (const char*)d_in[6], (char*)d_ws, (char*)d_out);
}
```

```cpp
#define PF_CODE 7
#define PF_WD
#include <hip/hip_runtime.h>
#include <hip/hip_bf16.h>
#include <stdint.h>

typedef short bf16x8 __attribute__((ext_vector_type(8)));
typedef short s16x4 __attribute__((ext_vector_type(4)));
typedef float f32x16 __attribute__((ext_vector_type(16)));
typedef float f32x4 __attribute__((ext_vector_type(4)));
typedef float f32x2 __attribute__((ext_vector_type(2)));
typedef unsigned u32x4 __attribute__((ext_vector_type(4)));
typedef unsigned u32x2 __attribute__((ext_vector_type(2)));
typedef __bf16 bf2 __attribute__((ext_vector_type(2)));
typedef __amdgpu_buffer_rsrc_t srd_t;
#define LAS __attribute__((address_space(3)))
#define GAS __attribute__((address_space(1)))
typedef LAS char* lptr;
#define DI __device__ __forceinline__
#define AI __attribute__((always_inline))
#define MFMA(a, b, c) __builtin_amdgcn_mfma_f32_32x32x16_bf16(__builtin_bit_cast(bf16x8, (a)), __builtin_bit_cast(bf16x8, (b)), (c), 0, 0, 0)
#define PIN(x) asm volatile("" : "+v"(x))
#define SBAR() __builtin_amdgcn_sched_barrier(0)
#define RLX_AGENT __ATOMIC_RELAXED, __HIP_MEMORY_SCOPE_AGENT
#define EX(v) __builtin_amdgcn_exp2f(v)
#define UNROLL _Pragma("unroll")
#define SRD(p, n) __builtin_amdgcn_make_buffer_rsrc((void*)(p), 0, (n), 0x00020000)

constexpr int WS_E = 0;
constexpr int WS_FQ = 4096;
constexpr int WS_FH = WS_FQ + 256 * 128;
constexpr int WS_FO = WS_FH + 256 * 128;
constexpr int WS_QKV = 1 << 20;
constexpr int QT = 6144, QH = 64 * QT;
constexpr int WS_O = WS_QKV + 16 * QH;
constexpr int WS_H = WS_O + (2 << 20);
constexpr int WS_END = WS_H + 64 * 112 * 1024;
constexpr int OOB = 1 << 30;
constexpr int SC1 = 16;
constexpr unsigned SPIN_CAP = 1u << 20;

#define PADPT 0

#define RTIME() ({ unsigned long long t_; asm volatile("s_memrealtime %0\n\ts_waitcnt lgkmcnt(0)" : "=s"(t_) :: "memory"); (unsigned)t_; })
#define TS(i) do { if ((i) == PADPT) tmid = RTIME(); } while (0)
#define TSR(i) do { tbeg = RTIME(); tmid = tbeg; } while (0)
#define TS_OUT() do { const unsigned tend = tmid + 6000; while ((int)(RTIME() - tend) < 0) __builtin_amdgcn_s_sleep(1); } while (0)

template <int I> struct IC { static constexpr int v = I; };
template <int B, int E, class F> DI void sfor(F&& f) { if constexpr (B < E) { f(IC<B>{}); sfor<B + 1, E>(f); } }
template <int N> DI void wait_bar() { asm volatile("s_waitcnt vmcnt(%0) lgkmcnt(0)\n\ts_barrier" :: "n"(N) : "memory"); }
template <int N> DI void vwait_bar() { asm volatile("s_waitcnt vmcnt(%0)\n\ts_barrier" :: "n"(N) : "memory"); }
DI void lbar() { asm volatile("s_waitcnt lgkmcnt(0)\n\ts_barrier" ::: "memory"); }
DI unsigned pk(float a, float b) { f32x2 v = {a, b}; return __builtin_bit_cast(unsigned, __builtin_convertvector(v, bf2)); }
DI float rbf(float a) { return __uint_as_float(pk(a, a) & 0xffff0000u); }
DI float dot2(unsigned a, unsigned b, float c) { return __builtin_amdgcn_fdot2_f32_bf16(__builtin_bit_cast(bf2, a), __builtin_bit_cast(bf2, b), c, false); }
DI u32x2 trd(lptr p) { return __builtin_bit_cast(u32x2, __builtin_amdgcn_ds_read_tr16_b64_v4i16((LAS s16x4*)p)); }
DI u32x4 cat(u32x2 a, u32x2 b) { return (u32x4){a[0], a[1], b[0], b[1]}; }
DI u32x4 ld128(lptr p) { return *(LAS u32x4*)p; }
DI unsigned bfi(unsigned m, unsigned a, unsigned b) { return (m & a) | (~m & b); }
template <int G, int L, int MODE = 0> DI void dma(srd_t srd, unsigned voff, unsigned lbase) {
  unsigned t;
  if constexpr (MODE == 1)
    asm volatile("s_add_u32 m0, %3, %5\n\ts_mov_b32 %0, %4\n\ts_mov_b32 exec_hi, 0\n\tbuffer_load_dwordx4 %1, %2, %0 offen lds\n\ts_mov_b32 exec_hi, -1"
                 : "=&s"(t) : "v"(voff), "s"(srd), "s"(lbase), "n"(G), "n"(L) : "memory", "scc");
  else if constexpr (MODE == 2)
    asm volatile("s_add_u32 m0, %3, %5\n\ts_mov_b32 %0, %4\n\tbuffer_load_dwordx4 %1, %2, %0 offen sc1 lds" : "=&s"(t) : "v"(voff), "s"(srd), "s"(lbase), "n"(G), "n"(L) : "memory", "scc");
  else
    asm volatile("s_add_u32 m0, %3, %5\n\ts_mov_b32 %0, %4\n\tbuffer_load_dwordx4 %1, %2, %0 offen lds" : "=&s"(t) : "v"(voff), "s"(srd), "s"(lbase), "n"(G), "n"(L) : "memory", "scc");
}
template <int G, int L, int MODE = 0> DI void dmam(srd_t srd, unsigned voff, unsigned lbase, unsigned gmul) {
  unsigned t;
  if constexpr (MODE == 1)
    asm volatile("s_add_u32 m0, %3, %5\n\ts_mul_i32 %0, %6, %4\n\ts_mov_b32 exec_hi, 0\n\tbuffer_load_dwordx4 %1, %2, %0 offen lds\n\ts_mov_b32 exec_hi, -1"
                 : "=&s"(t) : "v"(voff), "s"(srd), "s"(lbase), "n"(G), "n"(L), "s"(gmul) : "memory", "scc");
  else
    asm volatile("s_add_u32 m0, %3, %5\n\ts_mul_i32 %0, %6, %4\n\tbuffer_load_dwordx4 %1, %2, %0 offen lds" : "=&s"(t) : "v"(voff), "s"(srd), "s"(lbase), "n"(G), "n"(L), "s"(gmul) : "memory", "scc");
}
DI void touch(srd_t srd, unsigned voff, unsigned ldst) { asm volatile("s_mov_b32 m0, %2\n\ts_nop 0\n\tbuffer_load_dword %0, %1, 0 offen lds" :: "v"(voff), "s"(srd), "s"(ldst) : "memory"); }
DI void st128(u32x4 v, srd_t srd, int voff, int soff, int aux) { __builtin_amdgcn_raw_buffer_store_b128(v, srd, voff, soff, SC1); SBAR(); asm volatile("s_nop 1"); SBAR(); }
DI float swap32(float v, int h) {
  auto r = __builtin_amdgcn_permlane32_swap(__float_as_uint(v), __float_as_uint(v), false, false);
  return __uint_as_float(h ? r[0] : r[1]);
}
DI u32x4 widen(u32x2 a, u32x2 b) {
  auto r0 = __builtin_amdgcn_permlane32_swap(a[0], b[0], false, false); auto r1 = __builtin_amdgcn_permlane32_swap(a[1], b[1], false, false);
  return (u32x4){r0[0], r1[0], r0[1], r1[1]};
}
DI unsigned flag_ld(const char* ws, int off) { return __hip_atomic_load((GAS unsigned*)(ws + off), RLX_AGENT); }
DI void flag_st(char* ws, int off, unsigned v) { __hip_atomic_store((GAS unsigned*)(ws + off), v, RLX_AGENT); }

constexpr int RA = 2, NWF = RA + 1, UB = 7 - RA;
constexpr int SLOT = 28672, PART = 14336;
constexpr int L_CS = 5 * SLOT;
constexpr int L_ST = L_CS + 2048;
constexpr int L_TB = L_ST + 2048;
constexpr int L_PF = L_TB + 9216;
constexpr int L_WD = 65536;
constexpr int LDS_BYTES = 163840;
constexpr unsigned ONES2 = 0x3f803f80u, PAD1 = 0x00003f80u;

constexpr int kv_at(int s, int p) { return p == 0 && s + 3 < 16 ? (s + 3) * 2 + 1 : p == 7 && s + 4 < 16 ? (s + 4) * 2 : -1; }
constexpr int wd_at(int s, int p) {
  if (s < 4) return p == 1 ? s * 3 : p == 3 ? s * 3 + 1 : p == 4 ? s * 3 + 2 : -1;
  for (int g = 0; g < 3; ++g) { if (s == 10 + 2 * g && p == 7) return 12 + 4 * g; if (s == 11 + 2 * g) return p == 0 ? 13 + 4 * g : p == 2 ? 14 + 4 * g : p == 5 ? 15 + 4 * g : -1; }
  return -1;
}
constexpr int h_at(int s, int p) { return s >= 6 && s <= 12 ? (p == 1 ? (s - 6) * 4 : p == 3 ? (s - 6) * 4 + 1 : p == 4 ? (s - 6) * 4 + 2 : p == 7 ? (s - 6) * 4 + 3 : -1) : -1; }
constexpr int n_at(int s, int p) { return (kv_at(s, p) >= 0) + (wd_at(s, p) >= 0) + (h_at(s, p) >= 0); }
constexpr int mid_cnt(int s) {
  int last = (s - 1) * 8;
  if (s == 12 || s == 14) last = (s - 1) * 8 + 5;
  int n = 0;
  for (int z = last + 1; z < s * 8 + 6; ++z) n += n_at(z / 8, z % 8);
  return n;
}

__global__ __launch_bounds__(512, 2) void fused_kernel(const char* x, const char* wqkv, const char* wo, const char* wg, const char* wu, const char* wd,
                                                       char* ws, char* out) {
  extern __shared__ __attribute__((aligned(16))) char smem[];
  const lptr lds = (lptr)smem;
  const unsigned lds0 = (unsigned)(uintptr_t)lds;
  const int tid = threadIdx.x, lane = tid & 63, wid = __builtin_amdgcn_readfirstlane(tid >> 6);
  const int rr = lane >> 3, cc = lane & 7;
  const int b = blockIdx.x, xcd = b & 7, idx = b >> 3;
  const int ng = (xcd & 3) * 4 + (idx & 3), mg = (xcd >> 2) * 8 + (idx >> 2);
  unsigned tbeg = 0, tmid = 0; TSR(62); TS(0);
  const srd_t xr = SRD(x, 2048 * 896);
  const unsigned xv0 = (unsigned)((mg * 128 + wid * 16 + rr) * 896 + ((cc ^ rr) << 4));
  const unsigned xv1 = (unsigned)((mg * 128 + wid * 16 + 8 + (rr ^ 1)) * 896 + ((cc ^ rr ^ 1) << 4));
  const unsigned lw2k = lds0 + wid * 2048;
  auto dma_x = [&](auto lc) AI { constexpr int ln = decltype(lc)::v; dma<ln * 128, ln * 16384>(xr, xv0, lw2k); dma<ln * 128, ln * 16384 + 1024>(xr, xv1, lw2k); };
  sfor<0, 4>(dma_x);
  { unsigned long long pc; asm volatile("s_getpc_b64 %0" : "=s"(pc));
    if (wid < 4 && (idx & PF_CODE) == 0) touch(SRD(pc & ~127ull, 30720), tid * 128, lds0 + L_PF + wid * 256); }
  SBAR();
  const int r = lane & 31, h = lane >> 5, q = (lane & 15) >> 2, p = lane & 3, grp = (lane >> 4) & 1;
  const srd_t wsr = SRD(ws, WS_END);
  const f32x16 Z = {};
  const int head = 2 * xcd + (idx & 1), qg = idx >> 1, odd = head & 1;
  const int idx3 = idx < 28 ? idx : idx - 28, mq3 = (idx3 * 37) >> 8, mg3 = xcd * 4 + mq3, ng3 = idx3 - mq3 * 7;
  const unsigned kvv = (unsigned)(WS_QKV + head * QH + (wid >> 1) * 16 * QT + 2048 + (wid & 1) * 2048 + lane * 16);
  unsigned nonce;
  u32x4 QA0, QA1, QB0, QB1;
  {
    const int wm = wid & 3, wn = wid >> 2;
    const bool bq = ng >= 12;
    const int lineA = ng < 15 ? ng : 0, lineB = ng < 15 ? 2 * ng - 9 : 15;
    const unsigned vq = (unsigned)((28 * wm + rr) * 2688 + ((cc ^ (((rr >> 1) & 1) << 2)) << 4));
    const unsigned voC = (unsigned)((28 * wm + rr) * 3584 + ((cc ^ (((rr >> 1) & 1) << 1) ^ (wn << 2)) << 4)) + ng * 128;
    const unsigned voA = vq + lineA * 128 + wn * 112 * 2688, voB = bq ? vq + (lineB + wn) * 128 : voC + 2048;
    const unsigned stB = bq ? 2688 : 3584;
    const char* wgu = wn ? wu : wg;
    const srd_t sA = SRD(wqkv, 448 * 2688), sC = SRD(wgu, 448 * 3584), sB = SRD(bq ? wqkv : wgu, 448 * stB);
    const unsigned wdst = lds0 + wn * PART + wm * 3584;
    auto dma_w = [&](auto ic, auto pcc) AI {
      constexpr int i = decltype(ic)::v, pc = decltype(pcc)::v, L = ((i + 4) % 5) * SLOT + pc * 1024, M = pc == 3;
      if constexpr (i < 2) dma<(i * 224 + pc * 8) * 2688, L, M>(sA, voA, wdst);
      else if constexpr (i < 6) dmam<(i - 2) * 112 + pc * 8, L, M>(sB, voB, wdst, stB);
      else dma<((i - 6) * 112 + pc * 8) * 3584, L, M>(sC, voC, wdst);
    };
    auto dma_w4 = [&](auto ic) AI { sfor<0, 4>([&](auto pc) AI { dma_w(ic, pc); }); };
    if (wm < 3) {
      const int col0 = wm == 0 ? lineA * 64 + wn * 32 : lineB * 64 + wn * 64 + (wm - 1) * 32;
      const int sec = col0 >= 896 ? 2 : col0 >= 448 ? 1 : 0, base = ng < 15 ? WS_QKV + sec * 2048 : OOB;
      unsigned o[2], m[2], o2 = OOB;
      UNROLL for (int pp = 0; pp < 2; ++pp) {
        const int cu = ((col0 - sec * 448) >> 3) + 2 * pp + h, j = (cu * 37) >> 8, w = cu - 7 * j;
        const int hd = 2 * j + (w > 3), ch = w > 3 ? w - 3 : w;
        o[pp] = base + hd * QH + ch * 512 + ((r ^ (sec == 2 ? 4 * ch : 0)) << 4); m[pp] = w == 3 ? ~0u : 0u;
        if (w == 3) o2 = base + (hd + 1) * QH + (r << 4);
      }
      const lptr tb = lds + L_TB + (wn * 3 + wm) * 1536;
      *(LAS u32x4*)(tb + lane * 16) = (u32x4){o[0], o[1], o2, __float_as_uint(sec == 0 ? 0.18898223650461362f * 1.4426950408889634f : 1.f)};
      *(LAS u32x2*)(tb + 1024 + lane * 8) = (u32x2){m[0], m[1]};
    }
    const int rowb = (8 * h + q) * 128 + p * 8;
    const int bA = rowb + (((2 * wn + grp) ^ ((q >> 1) << 1)) << 5);
    const int bC0 = grp * PART + rowb + (((2 * wn) ^ (q >> 1) ^ (grp << 1)) << 5), bC1 = bC0 ^ 32;
    const int bB0 = bq ? wn * PART + rowb + ((grp ^ ((q >> 1) << 1)) << 5) : bC0, bB1 = bB0 ^ (bq ? 64 : 32);
    u32x4 xf[28];
    float st0 = 0.f, st1 = 0.f;
    {
      const int j = r & 7, rg = 4 * wm + (r >> 3), i = j ^ (rg & 1);
      const lptr xb0 = lds + rg * 1024 + i * 128;
      sfor<0, 4>([&](auto gc) AI {
        constexpr int g = decltype(gc)::v, k0 = 8 * g, k1 = g < 3 ? k0 + 8 : 28;
        SBAR();
        if (g == 0) wait_bar<4>(); else if (g == 1) wait_bar<4>(); else if (g == 2) wait_bar<6>(); else wait_bar<4>();
        if (g == 0) TS(1);
        UNROLL for (int ks = k0; ks < k1; ++ks) xf[ks] = ld128(xb0 + (ks >> 2) * 16384 + (((((ks & 3) << 1) + h) ^ j) << 4));
        if constexpr (g == 0) sfor<4, 6>(dma_x);
        if constexpr (g == 1) { dma_x(IC<6>{}); dma_w4(IC<0>{}); }
        if constexpr (g >= 2) dma_w4(IC<g - 1>{});
        if (wn == 0) { UNROLL for (int ks = k0; ks < k1; ++ks) { st0 = dot2(xf[ks][0], ONES2, st0); st1 = dot2(xf[ks][1], ONES2, st1); st0 = dot2(xf[ks][2], ONES2, st0); st1 = dot2(xf[ks][3], ONES2, st1); } }
        else { UNROLL for (int ks = k0; ks < k1; ++ks) { st0 = dot2(xf[ks][0], xf[ks][0], st0); st1 = dot2(xf[ks][1], xf[ks][1], st1); st0 = dot2(xf[ks][2], xf[ks][2], st0); st1 = dot2(xf[ks][3], xf[ks][3], st1); } }
        PIN(st0); PIN(st1);
      });
    }
    *(LAS float*)(lds + L_ST + (wm * 32 + r) * 16 + (wn * 2 + h) * 4) = st0 + st1;
    u32x2 wf[NWF][4];
    int ra0 = 0, ra1 = 0;
    auto rd_w = [&](int n) AI {
      const int i = n / 7, u = n % 7;
      if (u == 0) { ra0 = (i < 2 ? bA : i < 6 ? bB0 : bC0) + ((i + 4) % 5) * SLOT; ra1 = (i < 2 ? bA : i < 6 ? bB1 : bC1) + ((i + 4) % 5) * SLOT; PIN(ra0); if (i >= 2) PIN(ra1); }
      const int o0 = i < 2 ? u * 4096 : u * 2048, o1 = i < 2 ? u * 4096 + 2048 : u * 2048, a1 = i < 2 ? ra0 : ra1;
      wf[n % NWF][0] = trd(lds + ra0 + o0); wf[n % NWF][1] = trd(lds + ra0 + o0 + 512); wf[n % NWF][2] = trd(lds + a1 + o1); wf[n % NWF][3] = trd(lds + a1 + o1 + 512);
    };
    UNROLL for (int n = 0; n < RA; ++n) rd_w(n);
    TS(2);
    f32x16 A0 = Z, B0 = Z, B1 = Z, C0 = Z, C1 = Z;
    float cs0 = 0.f, cs1 = 0.f, rstd = 0.f, sig = 0.f, kg = 0.f; unsigned mm = 0, epoch = 0;
    const int mt = mg * 4 + wm;
    const unsigned csel = ((wm & 1) ? 0x05040c0cu : 0x01000c0cu) + ((wm >> 1) ? 0x02020000u : 0u);
    auto colsum = [&](int n, int hh) AI {
      cs0 += __uint_as_float(__builtin_amdgcn_perm(wf[n % NWF][2 + hh][0], wf[n % NWF][hh][0], csel)); cs1 += __uint_as_float(__builtin_amdgcn_perm(wf[n % NWF][2 + hh][1], wf[n % NWF][hh][1], csel));
      PIN(cs0); PIN(cs1);
    };
    auto fix1 = [&](f32x16& acc) AI {
      acc = MFMA(ld128(lds + L_CS + wn * 1024 + lane * 16), ((u32x4){mm, mm, mm, mm}), acc); };
    auto fix2 = [&](f32x16& a0, f32x16& a1) AI {
      const u32x4 c = ld128(lds + L_CS + wn * 1024 + lane * 16);
      a0 = MFMA(c, ((u32x4){mm, mm, 0u, 0u}), a0); a1 = MFMA(c, ((u32x4){0u, 0u, mm, mm}), a1); };
    auto epi_qkv = [&](const f32x16& acc, int t) AI {
      const lptr tb = lds + L_TB + (wn * 3 + t) * 1536;
      const u32x4 o = ld128(tb + lane * 16); const u32x2 m = *(LAS u32x2*)(tb + 1024 + lane * 8);
      const float sc = rstd * __uint_as_float(o[3]);
      u32x4 v[2];
      UNROLL for (int pp = 0; pp < 2; ++pp) {
        v[pp] = widen((u32x2){pk(acc[8 * pp] * sc, acc[8 * pp + 1] * sc), pk(acc[8 * pp + 2] * sc, acc[8 * pp + 3] * sc)},
                      (u32x2){pk(acc[8 * pp + 4] * sc, acc[8 * pp + 5] * sc), pk(acc[8 * pp + 6] * sc, acc[8 * pp + 7] * sc)});
        st128((u32x4){v[pp][0], v[pp][1], bfi(m[pp], ONES2, v[pp][2]), bfi(m[pp], PAD1, v[pp][3])}, wsr, o[pp], mt * QT, SC1);
      }
      st128((u32x4){ONES2, PAD1, bfi(m[0], v[0][2], v[1][2]), bfi(m[0], v[0][3], v[1][3])}, wsr, o[2], mt * QT, SC1);
    };
    auto glu = [&](const f32x16& acc, int i) AI { return acc[i + 8] * __builtin_amdgcn_rcpf(__builtin_fmaf(EX(acc[i] * kg), sig, sig)); };
    const int hsC = WS_H + (mt * 112 + ng * 4 + wn * 2) * 1024, hvB = bq ? OOB : lane * 16;
    float hv[8]; u32x4 hp; unsigned fl0 = 0, fl1 = 1, fl2 = 0, fl3 = 0;

    sfor<0, 70>([&](auto nc) AI {
      constexpr int n = decltype(nc)::v, i = n / 7, u = n % 7;
      constexpr int k0 = i < 2 ? 2 * n : ((i - 2) & 3) * 7 + u, k1 = i < 2 ? 2 * n + 1 : k0;
      f32x16& D0 = i < 2 ? A0 : i < 6 ? B0 : C0; f32x16& D1 = i < 2 ? A0 : i < 6 ? B1 : C1;
      constexpr bool last = i == 1 || i == 5 || i == 9;
      PIN(wf[n % NWF][3]);
      if (u == UB + 1) __builtin_amdgcn_s_setprio(3); if (u == 1) __builtin_amdgcn_s_setprio(2); if (u == 2) __builtin_amdgcn_s_setprio(1); if (u == 4) __builtin_amdgcn_s_setprio(0);
      SBAR();
      D0 = MFMA(cat(wf[n % NWF][0], wf[n % NWF][1]), xf[k0], D0);
      SBAR();
      if constexpr (u == UB) {
        if constexpr (last) {
          UNROLL for (int e = 0; e < RA; ++e) { colsum(n + e, 0); colsum(n + e, 1); }
          const float cs = cs0 + cs1, hi = rbf(cs); *(LAS unsigned*)(lds + L_CS + wn * 1024 + lane * 16 + ((wm & 1) * 2 + (wm >> 1)) * 4) = pk(hi, cs - hi); cs0 = 0.f; cs1 = 0.f;
        }
        if (i == 9 && wid == 0) {
          _Pragma("nounroll") for (unsigned sp = 0; !__all(fl0 == nonce && fl1 == nonce && fl2 == nonce && fl3 == nonce) && sp < SPIN_CAP; ++sp) {
            fl0 = flag_ld(ws, WS_FQ + lane * 128); fl1 = flag_ld(ws, WS_FQ + 8192 + lane * 128); fl2 = flag_ld(ws, WS_FQ + 16384 + lane * 128); fl3 = flag_ld(ws, WS_FQ + 24576 + lane * 128);
          }
          if (b == 0 && lane == 0) flag_st(ws, WS_E, nonce);
        }
        if (i == 0) wait_bar<8>(); else if (i < 6) wait_bar<12>(); else if (i == 6) wait_bar<8>(); else if (i == 7) wait_bar<0>(); else lbar();
        if constexpr (i == 7) { PIN(epoch); nonce = __builtin_amdgcn_readfirstlane(epoch) * 0x9E3779B1u + 0x7F4A7C15u; if (tid == 0) flag_st(ws, WS_FQ + b * 128, nonce); }
        if constexpr (i == 9) {
          sfor<0, 6>([&](auto zc) AI { constexpr int z = decltype(zc)::v; dma<(z >> 1) * QT + (z & 1) * 1024, (z >> 1) * 16384 + (z & 1) * 1024, 2>(wsr, kvv, lw2k); });
          const int qo = WS_QKV + head * QH + (qg * 4 + (wid & 1) * 2) * QT;
          QA0 = __builtin_amdgcn_raw_buffer_load_b128(wsr, lane * 16, qo, SC1); QA1 = __builtin_amdgcn_raw_buffer_load_b128(wsr, lane * 16 + 1024, qo, SC1);
          QB0 = __builtin_amdgcn_raw_buffer_load_b128(wsr, lane * 16, qo + QT, SC1); QB1 = __builtin_amdgcn_raw_buffer_load_b128(wsr, lane * 16 + 1024, qo + QT, SC1);
        }
        if (i == 1) TS(3); if (i == 3) TS(4); if (i == 5) TS(5); if (i == 7) TS(6); if (i == 9) TS(7);
        SBAR();
      }
      if constexpr (n + RA < 70) rd_w(n + RA);
      if (!(last && u >= UB)) colsum(n, 0);
      SBAR();
      D1 = MFMA(cat(wf[n % NWF][2], wf[n % NWF][3]), xf[k1], D1);
      SBAR();
      if (!(last && u >= UB)) colsum(n, 1);
      if constexpr (n < 4) dma_w(IC<3>{}, IC<n>{});
      if constexpr (n >= UB && n < UB + 7) { constexpr int v = n - UB; if constexpr (v < 4) dma_w(IC<4>{}, IC<v>{}); if constexpr (v >= 3) dma_w(IC<5>{}, IC<v - 3>{}); }
      if constexpr (n >= UB + 7 && n < 35 + UB) { constexpr int j = (n - UB) / 7, v = (n - UB) % 7; if constexpr ((v & 1) == 0) dma_w(IC<j + 5>{}, IC<v / 2>{}); }
      if constexpr (n == UB + 1) {
        const f32x4 a = *(LAS f32x4*)(lds + L_ST + (wm * 32 + r) * 16);
        const float mean = (a[0] + a[1]) * (1.f / 448.f);
        const float var = __builtin_fmaxf((a[2] + a[3]) * (1.f / 448.f) - mean * mean, 0.f);
        sig = rbf(__builtin_amdgcn_sqrtf(rbf(rbf(var) + 1e-5f))); rstd = __builtin_amdgcn_rcpf(sig); mm = pk(-mean, -mean); kg = -1.4426950408889634f * rstd;
        PIN(sig); PIN(rstd); PIN(mm); PIN(kg);
      }
      if constexpr (n == 13) fix1(A0);
      if ((n == 14 && wn == 0) || (n == 16 && wn == 1)) epi_qkv(A0, 0);
      if constexpr (n == 30) epoch = flag_ld(ws, WS_E);
      if constexpr (n == 41) fix2(B0, B1);
      if (n == 42 && bq) { epi_qkv(B0, 1); epi_qkv(B1, 2); }
      if constexpr (n >= 42 && n < 66 && (n - 42) % 3 != 2) {
        constexpr int e = (n - 42) / 3 * 2 + (n - 42) % 3; hv[e & 7] = glu(e < 8 ? B0 : B1, e & 7); PIN(hv[e & 7]);
        if ((e & 7) == 7) { hp = (u32x4){pk(hv[0], hv[1]), pk(hv[2], hv[3]), pk(hv[4], hv[5]), pk(hv[6], hv[7])}; PIN(hp); }
        if (e == 9 || e == 15) st128(hp, wsr, hvB + (e == 9 ? 0 : 1024), hsC + 65536, SC1);
      }
      if (n == 64 && wid == 0) { fl0 = flag_ld(ws, WS_FQ + lane * 128); fl1 = flag_ld(ws, WS_FQ + 8192 + lane * 128); fl2 = flag_ld(ws, WS_FQ + 16384 + lane * 128); fl3 = flag_ld(ws, WS_FQ + 24576 + lane * 128); }
      if constexpr (n == 56) { const int l = idx * 448 + wid * 56 + lane; touch(SRD(wd, 1792 * 896), lane < 56 ? l * 128 : OOB, lds0 + L_PF + wid * 256);
                               touch(SRD(wo, 448 * 896), (idx * 8 + wid) * 64 + lane < 3136 && wid < 2 ? ((idx * 2 + wid) * 64 + lane) * 128 : OOB, lds0 + L_PF + wid * 256); }
    });
    SBAR();
    fix2(C0, C1);
    UNROLL for (int t = 0; t < 2; ++t) {
      UNROLL for (int e = 0; e < 8; ++e) hv[e] = glu(t ? C1 : C0, e);
      st128((u32x4){pk(hv[0], hv[1]), pk(hv[2], hv[3]), pk(hv[4], hv[5]), pk(hv[6], hv[7])}, wsr, lane * 16 + t * 1024, hsC, SC1);
    }
    SBAR();
    TS(8);
  }
  const int w3m = wid & 1, w3k = wid >> 1;
  u32x4 hf[28];
  const unsigned vrow = (unsigned)(rr * 896 + ((cc ^ (((rr >> 1) & 1) << 2)) << 4) + ng3 * 128);
  const unsigned vo_d = vrow + (w3k * 448 + w3m * 32) * 896, ld_d = lds0 + L_WD + wid * 4096;
  const srd_t sD = SRD(wd, 1792 * 896);
  auto dma_d = [&](auto zc) AI { constexpr int z = decltype(zc)::v; dma<((z >> 2) * 64 + (z & 3) * 8) * 896, ((z >> 2) % 3) * 32768 + (z & 3) * 1024>(sD, vo_d, ld_d); };
  const int b30 = (4 * h + q) * 128 + p * 8 + ((grp ^ ((q >> 1) << 1)) << 5), b31 = b30 ^ 64;
  const lptr db0 = lds + L_WD + w3k * 8192 + b30, db1 = lds + L_WD + w3k * 8192 + b31;
  f32x16 F0 = Z, F1 = Z;
  u32x4 dw0, dw1;
  auto rd_d0 = [&](int d) AI { const int o = ((d >> 2) % 3) * 32768 + (d & 3) * 2048; dw0 = cat(trd(db0 + o), trd(db0 + o + 1024)); };
  auto rd_d1 = [&](int d) AI { const int o = ((d >> 2) % 3) * 32768 + (d & 3) * 2048; dw1 = cat(trd(db1 + o), trd(db1 + o + 1024)); };
  {
    const int wq = wid & 1, kq = wid >> 1;
    const lptr kb = lds + kq * 4096 + lane * 16;
    const int vc = 2 * grp + (p >> 1);
    const lptr vb0 = lds + kq * 4096 + 2048 + vc * 512 + ((((4 * h) ^ (4 * vc)) + q) << 4) + (p & 1) * 8;
    const lptr vb1 = lds + kq * 4096 + 2048 + vc * 512 + ((((4 * h + 8) ^ (4 * vc)) + q) << 4) + (p & 1) * 8;
    const bool padl = h != odd;
    auto set_pad = [&](u32x4& q0, u32x4& q1, unsigned a, unsigned c) AI {
      const bool p0 = padl && odd, p1 = padl && !odd;
      q0[0] = p0 ? a : q0[0]; q0[1] = p0 ? c : q0[1]; q1[2] = p1 ? a : q1[2]; q1[3] = p1 ? c : q1[3]; };
    auto set_shift = [&](u32x4& q0, u32x4& q1, float m) AI {
      const float m0 = rbf(m), m1 = rbf(m - m0), m2 = (m - m0) - m1; set_pad(q0, q1, pk(-m0, -m1), pk(-m2, 0.f)); };
    wait_bar<2>();
    PIN(QB1); PIN(QB0); PIN(QA1); PIN(QA0);
    TS(9);
    dma<3 * QT, 3 * 16384, 2>(wsr, kvv, lw2k);
    {
      const u32x4 k0 = ld128(lds + lane * 16), k1 = ld128(lds + 1024 + lane * 16);
      f32x16 sa = MFMA(k0, QA0, Z), sb = MFMA(k0, QB0, Z); sa = MFMA(k1, QA1, sa); sb = MFMA(k1, QB1, sb);
      float ma = sa[0], mb = sb[0];
      UNROLL for (int i = 1; i < 16; ++i) { ma = __builtin_fmaxf(ma, sa[i]); mb = __builtin_fmaxf(mb, sb[i]); }
      ma = __builtin_fmaxf(ma, swap32(ma, h)); mb = __builtin_fmaxf(mb, swap32(mb, h));
      set_shift(QA0, QA1, ma - 3.f); set_shift(QB0, QB1, mb - 3.f);
    }
    u32x4 k0 = ld128(kb), k1 = ld128(kb + 1024), v0 = cat(trd(vb0), trd(vb1)), v1 = {0u, 0u, 0u, 0u}, pa = v1, pb = v1;
    float e0 = 0.f, e1 = 0.f, e2 = 0.f, e3 = 0.f;
    f32x16 OA = Z, OB = Z, sa = MFMA(k0, QA0, Z), sb; sa = MFMA(k1, QA1, sa);
    unsigned fh = ~nonce; const int fho = WS_FH + ((mg3 >> 1) * 16 + (lane & 15)) * 128;
    static_assert(mid_cnt(14) == 0 && mid_cnt(5) == n_at(4, 7) + n_at(5, 0), "schedule");
    const int hld = WS_H + ((mg3 * 2 + w3m) * 112 + w3k * 28) * 1024;
    TS(10);
#define EX2(S, i, a, c) do { a = EX(S[i]); c = EX(S[(i) + 1]); PIN(a); PIN(c); } while (0)
#define STEP(SC, SN, QN0, QN1, OP, OC, L0, L1, L2, L3, X0, X1) do {                                                       \
      SBAR(); SN = MFMA(k0, QN0, Z); SBAR(); L0;                                                                          \
      pb[2] = pk(e0, e1); pb[3] = pk(e2, e3); PIN(pb); SBAR(); EX2(SC, 0, e0, e1); SBAR(); X0; SBAR(); EX2(SC, 2, e2, e3); \
      SBAR(); OP = MFMA(v1, pb, OP); SBAR(); L1;                                                                          \
      pa[0] = pk(e0, e1); pa[1] = pk(e2, e3); PIN(pa); SBAR(); EX2(SC, 4, e0, e1); EX2(SC, 6, e2, e3);                    \
      SBAR(); SN = MFMA(k1, QN1, SN); SBAR(); L2;                                                                         \
      pa[2] = pk(e0, e1); pa[3] = pk(e2, e3); PIN(pa); SBAR(); EX2(SC, 8, e0, e1); SBAR(); X1; SBAR(); EX2(SC, 10, e2, e3); \
      SBAR(); OC = MFMA(v0, pa, OC); SBAR(); L3;                                                                          \
      pb[0] = pk(e0, e1); pb[1] = pk(e2, e3); PIN(pb); SBAR(); EX2(SC, 12, e0, e1); EX2(SC, 14, e2, e3); SBAR(); } while (0)
    sfor<0, 16>([&](auto sc_) AI {
      constexpr int s = decltype(sc_)::v, so = (s & 3) * 16384, sn = ((s + 1) & 3) * 16384;
      auto side = [&](auto pc) AI {
        constexpr int P = decltype(pc)::v, kv = kv_at(s, P), wz = wd_at(s, P), hz = h_at(s, P);
        if constexpr (kv >= 0) dma<(kv >> 1) * QT + (kv & 1) * 1024, ((kv >> 1) & 3) * 16384 + (kv & 1) * 1024, 2>(wsr, kvv, lw2k);
        if constexpr (wz >= 0) dma_d(IC<wz>{});
        if constexpr (hz >= 0) hf[hz] = __builtin_amdgcn_raw_buffer_load_b128(wsr, lane * 16 + (hz & 3) * 1024, hld + (hz >> 2) * 4096, SC1);
      };
      auto mid = [&]() AI {
        if (s == 5 && wid == 0) {
          asm volatile("s_waitcnt vmcnt(%1)" : "+v"(fh) : "n"(mid_cnt(5)) : "memory");
          _Pragma("nounroll") for (unsigned sp = 0; !__all(fh == nonce) && sp < SPIN_CAP; ++sp) { fh = flag_ld(ws, fho); PIN(fh); }
        }
        if constexpr (s == 0) vwait_bar<63>(); else if constexpr (s == 1) vwait_bar<0>(); else if constexpr (s < 15) vwait_bar<mid_cnt(s)>();
        if (s == 1 && tid == 0) flag_st(ws, WS_FH + (mg * 16 + ng) * 128, nonce);
        if (s == 4 && wid == 0) asm volatile("buffer_load_dword %0, %1, %2, 0 offen sc1" : "+v"(fh) : "v"(fho), "s"(wsr) : "memory");
        if constexpr (s == 14) { UNROLL for (int k = 27; k >= 13; --k) PIN(hf[k]); }
        __builtin_amdgcn_s_setprio(3);
      };
      auto dn0 = [&](int d) AI { if (d >= 0) { F0 = MFMA(dw0, hf[d], F0); PIN(F0); } if (d >= -1) rd_d0(d + 1); };
      auto dn1 = [&](int d) AI { if (d >= 0) { F1 = MFMA(dw1, hf[d], F1); PIN(F1); } if (d >= -1) rd_d1(d + 1); };
      STEP(sa, sb, QB0, QB1, OB, OA,
           k0 = ld128(kb + sn); side(IC<0>{}); __builtin_amdgcn_s_setprio(2),
           v1 = cat(trd(vb0 + so + 256), trd(vb1 + so + 256)); side(IC<1>{}),
           k1 = ld128(kb + sn + 1024); side(IC<2>{}); __builtin_amdgcn_s_setprio(1),
           side(IC<3>{}), dn0(2 * s - 16), dn1(2 * s - 16));
      STEP(sb, sa, QA0, QA1, OA, OB,
           __builtin_amdgcn_s_setprio(0); side(IC<4>{}),
           side(IC<5>{}),
           mid(),
           v0 = cat(trd(vb0 + sn), trd(vb1 + sn)); side(IC<7>{}), dn0(2 * s - 15), dn1(2 * s - 15));
    });
    pb[2] = pk(e0, e1); pb[3] = pk(e2, e3);
    OB = MFMA(v1, pb, OB);
    __builtin_amdgcn_s_setprio(0);
    TS(11);
    lbar();
    UNROLL for (int g = 0; g < 4; ++g) {
      *(LAS f32x4*)(lds + wid * 8192 + g * 1024 + lane * 16) = (f32x4){OA[4 * g], OA[4 * g + 1], OA[4 * g + 2], OA[4 * g + 3]};
      *(LAS f32x4*)(lds + wid * 8192 + 4096 + g * 1024 + lane * 16) = (f32x4){OB[4 * g], OB[4 * g + 1], OB[4 * g + 2], OB[4 * g + 3]};
    }
    sfor<24, 28>(dma_d);
    wait_bar<4>();
    const int ft = kq & 1, fs = kq >> 1, qt = qg * 4 + wq * 2 + ft;
    f32x4 o0 = {0.f, 0.f, 0.f, 0.f}, o1 = o0; float l = 0.f;
    UNROLL for (int k = 0; k < 4; ++k) {
      const lptr pp = lds + (k * 2 + wq) * 8192 + ft * 4096;
      o0 += *(LAS f32x4*)(pp + fs * 2048 + lane * 16); o1 += *(LAS f32x4*)(pp + fs * 2048 + 1024 + lane * 16);
      l += *(LAS float*)(pp + (odd ? 0 : 3072 + 512) + r * 16);
    }
    if (__builtin_expect(__any(!(l <= 1.8446744e19f)), 0)) {
      const int qo = WS_QKV + head * QH + qt * QT + lane * 16;
      u32x4 q0 = __builtin_amdgcn_raw_buffer_load_b128(wsr, qo, 0, SC1), q1 = __builtin_amdgcn_raw_buffer_load_b128(wsr, qo + 1024, 0, SC1);
      set_pad(q0, q1, 0u, 0u);
      f32x16 O = Z; float m = -__builtin_inff();
      _Pragma("nounroll") for (int t = 0; t < 64; ++t) {
        const int ko = WS_QKV + head * QH + t * QT + 2048;
        f32x16 s = MFMA(__builtin_amdgcn_raw_buffer_load_b128(wsr, ko + lane * 16, 0, SC1), q0, Z);
        s = MFMA(__builtin_amdgcn_raw_buffer_load_b128(wsr, ko + 1024 + lane * 16, 0, SC1), q1, s);
        float mn = m;
        UNROLL for (int i = 0; i < 16; ++i) mn = __builtin_fmaxf(mn, s[i]);
        mn = __builtin_fmaxf(mn, swap32(mn, h));
        const float f = EX(m - mn); m = mn;
        UNROLL for (int i = 0; i < 16; ++i) O[i] *= f;
        UNROLL for (int st = 0; st < 2; ++st) {
          unsigned vv[8];
          UNROLL for (int j = 0; j < 8; ++j) vv[j] = __builtin_amdgcn_raw_buffer_load_b16(wsr, ko + 2048 + (r >> 3) * 512 + (((16 * st + 8 * (j >> 2) + 4 * h + (j & 3)) ^ (4 * (r >> 3))) << 4) + (r & 7) * 2, 0, SC1) & 0xffffu;
          O = MFMA(((u32x4){vv[0] | vv[1] << 16, vv[2] | vv[3] << 16, vv[4] | vv[5] << 16, vv[6] | vv[7] << 16}),
                   ((u32x4){pk(EX(s[8 * st] - m), EX(s[8 * st + 1] - m)), pk(EX(s[8 * st + 2] - m), EX(s[8 * st + 3] - m)),
                            pk(EX(s[8 * st + 4] - m), EX(s[8 * st + 5] - m)), pk(EX(s[8 * st + 6] - m), EX(s[8 * st + 7] - m))}), O);
        }
      }
      const float lc = odd ? O[0] : O[12], lx = swap32(lc, h); l = padl ? lc : lx;
      o0 = fs ? (f32x4){O[8], O[9], O[10], O[11]} : (f32x4){O[0], O[1], O[2], O[3]};
      o1 = fs ? (f32x4){O[12], O[13], O[14], O[15]} : (f32x4){O[4], O[5], O[6], O[7]};
    }
    const float ri = __builtin_amdgcn_rcpf(l);
    const float r0 = padl && odd && fs == 0 ? 0.f : ri, r1 = padl && !odd && fs == 1 ? 0.f : ri;
    st128((u32x4){pk(o0[0] * r0, o0[1] * r0), pk(o0[2] * r0, o0[3] * r0), pk(o1[0] * r1, o1[1] * r1), pk(o1[2] * r1, o1[3] * r1)},
                                           wsr, lane * 16, WS_O + (qt * 16 + head) * 2048 + fs * 1024, SC1);
  }
  TS(12);
  if (idx >= 28) { wait_bar<0>(); if (tid == 0) flag_st(ws, WS_FO + (qg * 16 + head) * 128, nonce); return; }
  {
    lbar();
    {
      const srd_t sO = SRD(wo, 448 * 896); const unsigned vo = vrow + wid * 56 * 896, lo = lds0 + wid * 7168;
      sfor<0, 7>([&](auto ic) AI { constexpr int i = decltype(ic)::v; dma<i * 8 * 896, i * 1024>(sO, vo, lo); });
    }
    const int mt = mg3 * 2 + w3m, ft = w3k & 1, fs = w3k >> 1;
    const unsigned xo = (unsigned)((mt * 32 + r) * 896 + ng3 * 128 + ft * 64 + fs * 32);
    const u32x2 x0 = *(const u32x2*)(x + xo + h * 8), x1 = *(const u32x2*)(x + xo + 16 + h * 8);
    u32x4 ew0, ew1;
    auto down2 = [&](int d, bool more) AI {
      const int o = (((d + 1) >> 2) % 3) * 32768 + ((d + 1) & 3) * 2048;
      ew0 = cat(trd(db0 + o), trd(db0 + o + 1024)); ew1 = cat(trd(db1 + o), trd(db1 + o + 1024));
      SBAR(); F0 = MFMA(dw0, hf[d], F0); F1 = MFMA(dw1, hf[d], F1); PIN(F0); PIN(F1); SBAR();
      if (more) { rd_d0(d + 2); rd_d1(d + 2); }
      SBAR(); F0 = MFMA(ew0, hf[d + 1], F0); F1 = MFMA(ew1, hf[d + 1], F1); PIN(F0); PIN(F1); SBAR();
    };
    down2(16, true); down2(18, true); down2(20, true); down2(22, false);
    wait_bar<0>();
    if (tid == 0) flag_st(ws, WS_FO + (qg * 16 + head) * 128, nonce);
    TS(13);
    rd_d0(24); rd_d1(24); down2(24, true); down2(26, false);
    const lptr ex = lds + L_WD + 32768;
    UNROLL for (int g = 0; g < 4; ++g) {
      *(LAS f32x4*)(ex + wid * 8192 + g * 1024 + lane * 16) = (f32x4){F0[4 * g], F0[4 * g + 1], F0[4 * g + 2], F0[4 * g + 3]};
      *(LAS f32x4*)(ex + wid * 8192 + 4096 + g * 1024 + lane * 16) = (f32x4){F1[4 * g], F1[4 * g + 1], F1[4 * g + 2], F1[4 * g + 3]};
    }
    TS(14);
    if (wid == 0) {
      unsigned fo = 0;
      _Pragma("nounroll") for (unsigned sp = 0; sp < SPIN_CAP; ++sp) { fo = flag_ld(ws, WS_FO + ((mg3 >> 1) * 16 + (lane & 15)) * 128); if (__all(fo == nonce)) break; }
    }
    lbar();
    TS(15);
    u32x4 of[8];
    UNROLL for (int k = 0; k < 8; ++k) of[k] = __builtin_amdgcn_raw_buffer_load_b128(wsr, lane * 16 + (k & 3) * 1024, WS_O + (mt * 16 + w3k * 4) * 2048 + (k >> 2) * 4096, SC1);
    SBAR();
    f32x4 g0 = {0.f, 0.f, 0.f, 0.f}, g1 = g0;
    UNROLL for (int k = 0; k < 4; ++k) { const lptr pp = ex + (k * 2 + w3m) * 8192 + ft * 4096 + fs * 2048 + lane * 16; g0 += *(LAS f32x4*)pp; g1 += *(LAS f32x4*)(pp + 1024); }
    UNROLL for (int i = 0; i < 4; ++i) {
      g0[i] = g0[i] * __builtin_amdgcn_rcpf(1.f + EX(g0[i] * -1.4426950408889634f)); g1[i] = g1[i] * __builtin_amdgcn_rcpf(1.f + EX(g1[i] * -1.4426950408889634f)); }
    PIN(g0); PIN(g1);
    u32x4 wo0[8], wo1[8];
    UNROLL for (int k = 0; k < 8; ++k) {
      const lptr sl = lds + w3k * PART + ((k >> 2) * 56 + ((k >> 1) & 1) * 24 + (k & 1) * 16) * 128;
      wo0[k] = cat(trd(sl + b30), trd(sl + b30 + 1024)); wo1[k] = cat(trd(sl + b31), trd(sl + b31 + 1024));
    }
    lbar();
    f32x16 S0 = Z, S1 = Z;
    UNROLL for (int k = 0; k < 8; ++k) { S0 = MFMA(wo0[k], of[k], S0); S1 = MFMA(wo1[k], of[k], S1); }
    TS(16);
    UNROLL for (int g = 0; g < 4; ++g) {
      *(LAS f32x4*)(ex + wid * 8192 + g * 1024 + lane * 16) = (f32x4){S0[4 * g], S0[4 * g + 1], S0[4 * g + 2], S0[4 * g + 3]};
      *(LAS f32x4*)(ex + wid * 8192 + 4096 + g * 1024 + lane * 16) = (f32x4){S1[4 * g], S1[4 * g + 1], S1[4 * g + 2], S1[4 * g + 3]};
    }
    lbar();
    f32x4 s0 = {0.f, 0.f, 0.f, 0.f}, s1 = s0;
    UNROLL for (int k = 0; k < 4; ++k) { const lptr pp = ex + (k * 2 + w3m) * 8192 + ft * 4096 + fs * 2048 + lane * 16; s0 += *(LAS f32x4*)pp; s1 += *(LAS f32x4*)(pp + 1024); }
    auto lo = [](unsigned v) AI { return __uint_as_float(v << 16); }; auto hi = [](unsigned v) AI { return __uint_as_float(v & 0xffff0000u); };
    const u32x2 a = {pk(__builtin_fmaf(s0[0], g0[0], lo(x0[0])), __builtin_fmaf(s0[1], g0[1], hi(x0[0]))), pk(__builtin_fmaf(s0[2], g0[2], lo(x0[1])), __builtin_fmaf(s0[3], g0[3], hi(x0[1])))};
    const u32x2 c = {pk(__builtin_fmaf(s1[0], g1[0], lo(x1[0])), __builtin_fmaf(s1[1], g1[1], hi(x1[0]))), pk(__builtin_fmaf(s1[2], g1[2], lo(x1[1])), __builtin_fmaf(s1[3], g1[3], hi(x1[1])))};
    *(u32x4*)(out + xo + h * 16) = widen(a, c);
    TS(17); TS_OUT();
  }
}

extern "C" void kernel_launch(void* const* d_in, const int* in_sizes, int n_in, void* d_out, int out_size, void* d_ws, size_t ws_size,
                              hipStream_t stream) {
  (void)hipFuncSetAttribute((const void*)fused_kernel, hipFuncAttributeMaxDynamicSharedMemorySize, LDS_BYTES);
  fused_kernel<<<256, 512, LDS_BYTES, stream>>>((const char*)d_in[0], (const char*)d_in[1], (const char*)d_in[2], (const char*)d_in[4], (const char*)d_in[5],
                                                (const char*)d_in[6], (char*)d_ws, (char*)d_out);
}
```

```cpp
#include <hip/hip_runtime.h>
#include <hip/hip_bf16.h>
#include <stdint.h>

typedef short bf16x8 __attribute__((ext_vector_type(8)));
typedef short s16x4 __attribute__((ext_vector_type(4)));
typedef float f32x16 __attribute__((ext_vector_type(16)));
typedef float f32x4 __attribute__((ext_vector_type(4)));
typedef float f32x2 __attribute__((ext_vector_type(2)));
typedef unsigned u32x4 __attribute__((ext_vector_type(4)));
typedef unsigned u32x2 __attribute__((ext_vector_type(2)));
typedef __bf16 bf2 __attribute__((ext_vector_type(2)));
typedef __amdgpu_buffer_rsrc_t srd_t;
#define LAS __attribute__((address_space(3)))
#define GAS __attribute__((address_space(1)))
typedef LAS char* lptr;
#define DI __device__ __forceinline__
#define AI __attribute__((always_inline))
#define MFMA(a, b, c) __builtin_amdgcn_mfma_f32_32x32x16_bf16(__builtin_bit_cast(bf16x8, (a)), __builtin_bit_cast(bf16x8, (b)), (c), 0, 0, 0)
#define PIN(x) asm volatile("" : "+v"(x))
#define SBAR() __builtin_amdgcn_sched_barrier(0)
#define RLX_AGENT __ATOMIC_RELAXED, __HIP_MEMORY_SCOPE_AGENT
#define EX(v) __builtin_amdgcn_exp2f(v)
#define F2U(x) __builtin_bit_cast(unsigned, (float)(x))
#define U2F(x) __builtin_bit_cast(float, (unsigned)(x))
#define ANY(p) (__builtin_amdgcn_ballot_w64(p) != 0)
#define ALL(p) (__builtin_amdgcn_ballot_w64(!(p)) == 0)
#define UNROLL _Pragma("unroll")
#define SRD(p, n) __builtin_amdgcn_make_buffer_rsrc((void*)(p), 0, (n), 0x00020000)

constexpr int WS_E = 0;
constexpr int WS_FQ = 4096;
constexpr int WS_FH = WS_FQ + 256 * 128;
constexpr int WS_FO = WS_FH + 256 * 128;
constexpr int WS_ST = WS_FO + 256 * 128;
constexpr int WS_QKV = 1 << 20;
constexpr int QT = 6144, QH = 64 * QT;
constexpr int WS_O = WS_QKV + 16 * QH;
constexpr int WS_H = WS_O + (2 << 20);
constexpr int WS_END = WS_H + 64 * 112 * 1024;
constexpr int OOB = 1 << 30;
constexpr int SC1 = 16;
constexpr unsigned SPIN_CAP = 1u << 20;

#define TS(i) do { } while (0)
#define TSR(i) do { } while (0)
#define TS_OUT() do { } while (0)

template <int I> struct IC { static constexpr int v = I; };
template <int B, int E, class F> DI void sfor(F&& f) { if constexpr (B < E) { f(IC<B>{}); sfor<B + 1, E>(f); } }
template <int N> DI void wait_bar() { asm volatile("s_waitcnt vmcnt(%0) lgkmcnt(0)\n\ts_barrier" :: "n"(N) : "memory"); }
template <int N> DI void vwait_bar() { asm volatile("s_waitcnt vmcnt(%0)\n\ts_barrier" :: "n"(N) : "memory"); }
DI void lbar() { asm volatile("s_waitcnt lgkmcnt(0)\n\ts_barrier" ::: "memory"); }
DI unsigned pk(float a, float b) { f32x2 v = {a, b}; return __builtin_bit_cast(unsigned, __builtin_convertvector(v, bf2)); }
DI float rbf(float a) { return U2F(pk(a, a) & 0xffff0000u); }
DI float dot2(unsigned a, unsigned b, float c) { return __builtin_amdgcn_fdot2_f32_bf16(__builtin_bit_cast(bf2, a), __builtin_bit_cast(bf2, b), c, false); }
DI u32x2 trd(lptr p) { return __builtin_bit_cast(u32x2, __builtin_amdgcn_ds_read_tr16_b64_v4i16((LAS s16x4*)p)); }
DI u32x4 cat(u32x2 a, u32x2 b) { return (u32x4){a[0], a[1], b[0], b[1]}; }
DI u32x4 ld128(lptr p) { return *(LAS u32x4*)p; }
DI unsigned bfi(unsigned m, unsigned a, unsigned b) { return (m & a) | (~m & b); }
template <int G, int L, bool DEV = false> DI void dma(srd_t srd, unsigned voff, unsigned lbase) {
  unsigned t;
  if constexpr (DEV)
    asm volatile("s_add_u32 m0, %3, %5\n\ts_mov_b32 %0, %4\n\tbuffer_load_dwordx4 %1, %2, %0 offen sc1 lds" : "=&s"(t) : "v"(voff), "s"(srd), "s"(lbase), "n"(G), "n"(L) : "memory", "scc");
  else
    asm volatile("s_add_u32 m0, %3, %5\n\ts_mov_b32 %0, %4\n\tbuffer_load_dwordx4 %1, %2, %0 offen lds" : "=&s"(t) : "v"(voff), "s"(srd), "s"(lbase), "n"(G), "n"(L) : "memory", "scc");
}
template <int L> DI void dma2(srd_t srd, unsigned voff0, unsigned voff1, unsigned lbase, unsigned ga, unsigned gb) {
  unsigned t;
  asm volatile("s_add_u32 m0, %4, %5\n\ts_mul_i32 %0, %6, %7\n\tbuffer_load_dwordx4 %1, %3, %0 offen lds\n\ts_add_u32 m0, m0, 1024\n\ts_nop 0\n\tbuffer_load_dwordx4 %2, %3, %0 offen lds"
               : "=&s"(t) : "v"(voff0), "v"(voff1), "s"(srd), "s"(lbase), "n"(L), "s"(ga), "s"(gb) : "memory", "scc");
}
template <int L> DI void dma1(srd_t srd, unsigned voff, unsigned lbase, unsigned ga, unsigned gb) {
  unsigned t;
  asm volatile("s_add_u32 m0, %3, %4\n\ts_mul_i32 %0, %5, %6\n\tbuffer_load_dwordx4 %1, %2, %0 offen lds" : "=&s"(t) : "v"(voff), "s"(srd), "s"(lbase), "n"(L), "s"(ga), "s"(gb) : "memory", "scc");
}
DI void touch(srd_t srd, unsigned voff, unsigned ldst) { asm volatile("s_mov_b32 m0, %2\n\ts_nop 0\n\tbuffer_load_dword %0, %1, 0 offen lds" :: "v"(voff), "s"(srd), "s"(ldst) : "memory"); }
DI void st128(u32x4 v, srd_t srd, int voff, int soff, int aux) { __builtin_amdgcn_raw_buffer_store_b128(v, srd, voff, soff, SC1); SBAR(); asm volatile("s_nop 1"); SBAR(); }
DI float swap32(float v, int h) {
  auto r = __builtin_amdgcn_permlane32_swap(F2U(v), F2U(v), false, false);
  return U2F(h ? r[0] : r[1]);
}
DI u32x4 widen(u32x2 a, u32x2 b) {
  auto r0 = __builtin_amdgcn_permlane32_swap(a[0], b[0], false, false); auto r1 = __builtin_amdgcn_permlane32_swap(a[1], b[1], false, false);
  return (u32x4){r0[0], r1[0], r0[1], r1[1]};
}
DI float wave_sum(float v) {
  v += U2F(__builtin_amdgcn_update_dpp(0u, F2U(v), 0x111, 0xf, 0xf, true));
  v += U2F(__builtin_amdgcn_update_dpp(0u, F2U(v), 0x112, 0xf, 0xf, true));
  v += U2F(__builtin_amdgcn_update_dpp(0u, F2U(v), 0x114, 0xf, 0xf, true));
  v += U2F(__builtin_amdgcn_update_dpp(0u, F2U(v), 0x118, 0xf, 0xf, true));
  v += U2F(__builtin_amdgcn_update_dpp(0u, F2U(v), 0x142, 0xa, 0xf, true));
  v += U2F(__builtin_amdgcn_update_dpp(0u, F2U(v), 0x143, 0xc, 0xf, true));
  return v;
}
DI unsigned flag_ld(const char* ws, int off) { return __hip_atomic_load((GAS unsigned*)(ws + off), RLX_AGENT); }
DI void flag_st(char* ws, int off, unsigned v) {
  if (__builtin_amdgcn_mbcnt_hi(~0u, __builtin_amdgcn_mbcnt_lo(~0u, 0u)) == 0) __hip_atomic_store((GAS unsigned*)(ws + off), v, RLX_AGENT); }

constexpr int RA = 2, NWF = RA + 1;
constexpr int PART = 14336;
constexpr int L_W = 65536;
constexpr int L_CS = 9 * 16384;
constexpr int L_PF = L_CS + 2048;
constexpr int L_Q = 65536;
constexpr int wpos(int m) { return m < 11 ? L_W + (m % 5) * 16384 : m < 13 ? (m - 11) * 16384 : m == 13 ? 49152 : L_W + 16384 + (m - 14) * 8192; }
#define ROTM 3
constexpr int L_WD = 65536;
constexpr int LDS_BYTES = 163840;
constexpr unsigned ONES2 = 0x3f803f80u, PAD1 = 0x00003f80u;
static_assert(L_PF + 2048 <= LDS_BYTES, "LDS map");

constexpr int DN0 = 7;
constexpr int kv_at(int s, int p) { return p == 0 && s > 0 && s + 3 < 16 ? (s + 3) * 2 + 1 : p == 7 && s + 4 < 16 ? (s + 4) * 2 : -1; }
constexpr int wd_at(int s, int p) {
  if (s >= 1 && s <= 3) return p == 1 ? s * 4 - 4 : p == 3 ? s * 4 - 3 : p == 4 ? s * 4 - 2 : p == 5 ? s * 4 - 1 : -1;
  for (int g = 0; g < 2; ++g) { if (s == 10 + 4 * g && p == 7) return 12 + 4 * g; if (s == 11 + 4 * g) return p == 0 ? 13 + 4 * g : p == 2 ? 14 + 4 * g : p == 5 ? 15 + 4 * g : -1; }
  return -1;
}
constexpr int h_at(int s, int p) { const int z = (s - 5) * 3 + (p == 1 ? 1 : p == 3 ? 2 : 3); if (s == 15) return p == 1 ? 24 : p == 3 ? 25 : p == 4 ? 26 : p == 7 ? 27 : -1;
  return s == 4 && p == 7 ? 0 : s >= 5 && (p == 1 || p == 3 || p == 4) && z < 24 ? z : -1; }
constexpr int n_at(int s, int p) { return (kv_at(s, p) >= 0) + (wd_at(s, p) >= 0) + (h_at(s, p) >= 0); }
constexpr int cnt_after(int s0, int p0, int s) { int n = 0; for (int z = s0 * 8 + p0 + 1; z < s * 8 + 6; ++z) n += n_at(z / 8, z % 8); return n; }
constexpr int dslot(int sub) { return sub < 5 ? L_WD + (sub % 3) * 32768 : (sub - 5) * 32768; }

struct QkvTab { unsigned v[44][2][4]; };
constexpr QkvTab make_tab() {
  QkvTab t = {};
  for (int tile = 0; tile < 44; ++tile) for (int h = 0; h < 2; ++h) {
    const int col0 = tile * 32, sec = col0 / 448; unsigned* e = t.v[tile][h];
    e[0] = e[1] = e[2] = OOB; e[3] = 0;
    if (tile < 42) for (int pp = 0; pp < 2; ++pp) {
      const int cu = (col0 - sec * 448) / 8 + 2 * pp + h, j = cu / 7, w = cu % 7, hd = 2 * j + (w > 3), ch = w > 3 ? w - 3 : w;
      e[pp] = WS_QKV + sec * 2048 + hd * QH + ch * 512 + (sec == 2 ? ch * 64 : 0);
      if (w == 3) { e[2] = WS_QKV + sec * 2048 + (hd + 1) * QH; e[3] |= 1u << pp; }
    }
  }
  return t;
}
__device__ const QkvTab qkv_tab = make_tab();

#define TSARG
#define TSPAR
#define LANE_IDS() int lane; asm volatile("v_mbcnt_lo_u32_b32 %0, -1, 0\n\tv_mbcnt_hi_u32_b32 %0, -1, %0" : "=v"(lane));        \
  const int r = lane & 31, h = lane >> 5, q = (lane & 15) >> 2, p = lane & 3, grp = (lane >> 4) & 1, rr = lane >> 3, cc = lane & 7
DI void phase23(const char* x, const char* wo, const char* wd, char* ws, char* out, lptr lds, unsigned nonce TSPAR) {
  const unsigned lds0 = (unsigned)(uintptr_t)lds;
  const int wid = __builtin_amdgcn_readfirstlane(__builtin_amdgcn_workitem_id_x() >> 6);
  const int b = __builtin_amdgcn_workgroup_id_x(), xcd = b & 7, idx = b >> 3;
  const int ng = (xcd & 3) * 4 + (idx & 3), mg = (xcd >> 2) * 8 + (idx >> 2);
  const srd_t wsr = SRD(ws, WS_END);
  const f32x16 Z = {};
  const int head = 2 * xcd + (idx & 1), qg = idx >> 1, odd = head & 1;
  const int idx3 = idx < 28 ? idx : idx - 28, mq3 = (idx3 * 37) >> 8, mg3 = xcd * 4 + mq3, ng3 = idx3 - mq3 * 7;
  LANE_IDS();
  const unsigned lw2k = lds0 + wid * 2048;
  const unsigned kvv = (unsigned)(WS_QKV + head * QH + (wid >> 1) * 16 * QT + 2048 + (wid & 1) * 2048 + lane * 16);
  auto dma_kv = [&](auto zc) AI { constexpr int z = decltype(zc)::v; dma<(z >> 1) * QT + (z & 1) * 1024, ((z >> 1) & 3) * 16384 + (z & 1) * 1024, true>(wsr, kvv, lw2k); };
  const int w3m = wid & 1, w3k = wid >> 1;
  u32x4 hf[28];
  const unsigned vrow = (unsigned)(rr * 896 + ((cc ^ (((rr >> 1) & 1) << 2)) << 4) + ng3 * 128);
  const unsigned vo_d = vrow + (w3k * 448 + w3m * 32) * 896, ld_d = lds0 + wid * 4096;
  const srd_t sD = SRD(wd, 1792 * 896);
  auto dma_d = [&](auto zc) AI { constexpr int z = decltype(zc)::v; dma<((z >> 2) * 64 + (z & 3) * 8) * 896, dslot(z >> 2) + (z & 3) * 1024>(sD, vo_d, ld_d); };
  const int b30 = (4 * h + q) * 128 + p * 8 + ((grp ^ ((q >> 1) << 1)) << 5), b31 = b30 ^ 64;
  int db0 = L_WD + w3k * 8192 + b30, db1 = L_WD + w3k * 8192 + b31, dbo = L_WD;
  PIN(db0); PIN(db1);
  auto rebase = [&](int o) AI { db0 += o - dbo; db1 += o - dbo; dbo = o; PIN(db0); PIN(db1); };
  f32x16 F0 = Z, F1 = Z;
  u32x4 dw0, dw1;
  auto rd_d0 = [&](int d) AI { const int o = dslot(d >> 2) + (d & 3) * 2048 - dbo; dw0 = cat(trd(lds + db0 + o), trd(lds + db0 + o + 1024)); };
  auto rd_d1 = [&](int d) AI { const int o = dslot(d >> 2) + (d & 3) * 2048 - dbo; dw1 = cat(trd(lds + db1 + o), trd(lds + db1 + o + 1024)); };
  {
    const int wq = wid & 1, kq = wid >> 1;
    const lptr kb = lds + kq * 4096 + lane * 16;
    const int vc = 2 * grp + (p >> 1);
    const lptr vb0 = lds + kq * 4096 + 2048 + vc * 512 + ((((4 * h) ^ (4 * vc)) + q) << 4) + (p & 1) * 8;
    const lptr vb1 = lds + kq * 4096 + 2048 + vc * 512 + ((((4 * h + 8) ^ (4 * vc)) + q) << 4) + (p & 1) * 8;
    const bool padl = h != odd;
    auto set_pad = [&](u32x4& q0, u32x4& q1, unsigned a, unsigned c, bool pl) AI {
      const bool p0 = pl && odd, p1 = pl && !odd;
      q0[0] = p0 ? a : q0[0]; q0[1] = p0 ? c : q0[1]; q1[2] = p1 ? a : q1[2]; q1[3] = p1 ? c : q1[3]; };
    auto set_shift = [&](u32x4& q0, u32x4& q1, float m) AI {
      const float m0 = rbf(m), m1 = rbf(m - m0), m2 = (m - m0) - m1; set_pad(q0, q1, pk(-m0, -m1), pk(-m2, 0.f), padl); };
    u32x4 QA0 = ld128(lds + L_Q + wq * 4096 + lane * 16), QA1 = ld128(lds + L_Q + wq * 4096 + 1024 + lane * 16);
    u32x4 QB0 = ld128(lds + L_Q + wq * 4096 + 2048 + lane * 16), QB1 = ld128(lds + L_Q + wq * 4096 + 3072 + lane * 16);
    {
      const u32x4 k0 = ld128(lds + lane * 16), k1 = ld128(lds + 1024 + lane * 16);
      f32x16 sa = MFMA(k0, QA0, Z), sb = MFMA(k0, QB0, Z); sa = MFMA(k1, QA1, sa); sb = MFMA(k1, QB1, sb);
      float ma = sa[0], mb = sb[0];
      UNROLL for (int i = 1; i < 16; ++i) { ma = __builtin_fmaxf(ma, sa[i]); mb = __builtin_fmaxf(mb, sb[i]); }
      ma = __builtin_fmaxf(ma, swap32(ma, h)); mb = __builtin_fmaxf(mb, swap32(mb, h));
      set_shift(QA0, QA1, ma - 3.f); set_shift(QB0, QB1, mb - 3.f);
    }
    u32x4 k0 = ld128(kb), k1 = ld128(kb + 1024), v0 = cat(trd(vb0), trd(vb1)), v1 = {0u, 0u, 0u, 0u}, pa = v1, pb = v1;
    float e0 = 0.f, e1 = 0.f, e2 = 0.f, e3 = 0.f;
    f32x16 OA = Z, OB = Z, sa = MFMA(k0, QA0, Z), sb; sa = MFMA(k1, QA1, sa);
    unsigned fh = ~nonce; const int fho = WS_FH + ((mg3 >> 1) * 16 + (lane & 15)) * 128;
    const int hld = WS_H + ((mg3 * 2 + w3m) * 112 + w3k * 28) * 1024;
    TS(10); TS(19);
#define EX2(S, i, a, c) do { a = EX(S[i]); c = EX(S[(i) + 1]); PIN(a); PIN(c); } while (0)
#define STEP(SC, SN, QN0, QN1, OP, OC, L0, L1, L2, L3, X0, X1) do {                                                       \
      SBAR(); SN = MFMA(k0, QN0, Z); SBAR(); L0;                                                                          \
      pb[2] = pk(e0, e1); pb[3] = pk(e2, e3); PIN(pb); SBAR(); EX2(SC, 0, e0, e1); SBAR(); X0; SBAR(); EX2(SC, 2, e2, e3); \
      SBAR(); OP = MFMA(v1, pb, OP); SBAR(); L1;                                                                          \
      pa[0] = pk(e0, e1); pa[1] = pk(e2, e3); PIN(pa); SBAR(); EX2(SC, 4, e0, e1); EX2(SC, 6, e2, e3);                    \
      SBAR(); SN = MFMA(k1, QN1, SN); SBAR(); L2;                                                                         \
      pa[2] = pk(e0, e1); pa[3] = pk(e2, e3); PIN(pa); SBAR(); EX2(SC, 8, e0, e1); SBAR(); X1; SBAR(); EX2(SC, 10, e2, e3); \
      SBAR(); OC = MFMA(v0, pa, OC); SBAR(); L3;                                                                          \
      pb[0] = pk(e0, e1); pb[1] = pk(e2, e3); PIN(pb); SBAR(); EX2(SC, 12, e0, e1); EX2(SC, 14, e2, e3); SBAR(); } while (0)
    sfor<0, 16>([&](auto sc_) AI {
      constexpr int s = decltype(sc_)::v, so = (s & 3) * 16384, sn = ((s + 1) & 3) * 16384;
      auto side = [&](auto pc) AI {
        constexpr int P = decltype(pc)::v, kv = kv_at(s, P), wz = wd_at(s, P), hz = h_at(s, P);
        if constexpr (kv >= 0) dma_kv(IC<kv>{});
        if constexpr (wz >= 0) dma_d(IC<wz>{});
        if constexpr (hz >= 0) hf[hz] = __builtin_amdgcn_raw_buffer_load_b128(wsr, lane * 16 + (hz & 3) * 1024, hld + (hz >> 2) * 4096, SC1);
      };
      auto mid = [&]() AI {
        if (s == 4 && wid == 0) {
          asm volatile("s_waitcnt vmcnt(%1)" : "+v"(fh) : "n"(cnt_after(3, 6, 4)) : "memory");
          _Pragma("nounroll") for (unsigned sp = 0; !ALL(fh == nonce) && sp < SPIN_CAP; ++sp) { fh = flag_ld(ws, fho); PIN(fh); }
        }
        if constexpr (s == 0) vwait_bar<5 + cnt_after(0, -1, 0)>();
        else if constexpr (s == 1) vwait_bar<cnt_after(0, -1, 1)>();
        else if constexpr (s == 14) vwait_bar<0>(); else if constexpr (s < 14) vwait_bar<cnt_after(s - 1, 0, s)>();
        if (s == 1 && wid == 0) flag_st(ws, WS_FH + (mg * 16 + ng) * 128, nonce);
        if (s == 3 && wid == 0) asm volatile("buffer_load_dword %0, %1, %2, 0 offen sc1" : "+v"(fh) : "v"(fho), "s"(wsr) : "memory");
        if constexpr (s == 13) rebase(L_WD + 65536);
        if constexpr (s == 14) { UNROLL for (int k = 23; k >= 8; --k) PIN(hf[k]); }
        if (s == 1) TS(20); if (s == 3) TS(21); if (s == 5) TS(22); if (s == 7) TS(23); if (s == 9) TS(24); if (s == 11) TS(25); if (s == 13) TS(26);
        __builtin_amdgcn_s_setprio(3);
      };
      auto dn0 = [&](int d) AI { if (d >= 0) { F0 = MFMA(dw0, hf[d], F0); PIN(F0); } if (d >= -1) rd_d0(d + 1); };
      auto dn1 = [&](int d) AI { if (d >= 0) { F1 = MFMA(dw1, hf[d], F1); PIN(F1); } if (d >= -1) rd_d1(d + 1); };
      STEP(sa, sb, QB0, QB1, OB, OA,
           k0 = ld128(kb + sn); side(IC<0>{}); __builtin_amdgcn_s_setprio(2),
           v1 = cat(trd(vb0 + so + 256), trd(vb1 + so + 256)); side(IC<1>{}),
           k1 = ld128(kb + sn + 1024); side(IC<2>{}); __builtin_amdgcn_s_setprio(1),
           side(IC<3>{}), dn0(s - DN0), if (s == 15) dn1(8));
      STEP(sb, sa, QA0, QA1, OA, OB,
           __builtin_amdgcn_s_setprio(0); side(IC<4>{}),
           side(IC<5>{}),
           mid(),
           v0 = cat(trd(vb0 + sn), trd(vb1 + sn)); side(IC<7>{}), if (s < 15) dn1(s - DN0); else dn0(9), if (s == 15) dn1(9));
    });
    pb[2] = pk(e0, e1); pb[3] = pk(e2, e3);
    OB = MFMA(v1, pb, OB);
    __builtin_amdgcn_s_setprio(0);
    TS(11);
    {
    LANE_IDS();
    const bool padl = h != odd;
    lbar();
    UNROLL for (int g = 0; g < 4; ++g) {
      *(LAS f32x4*)(lds + wid * 8192 + g * 1024 + lane * 16) = (f32x4){OA[4 * g], OA[4 * g + 1], OA[4 * g + 2], OA[4 * g + 3]};
      *(LAS f32x4*)(lds + wid * 8192 + 4096 + g * 1024 + lane * 16) = (f32x4){OB[4 * g], OB[4 * g + 1], OB[4 * g + 2], OB[4 * g + 3]};
    }
    lbar();
    const int ft = kq & 1, fs = kq >> 1, qt = qg * 4 + wq * 2 + ft;
    f32x4 o0 = {0.f, 0.f, 0.f, 0.f}, o1 = o0; float l = 0.f;
    UNROLL for (int k = 0; k < 4; ++k) {
      const lptr pp = lds + (k * 2 + wq) * 8192 + ft * 4096;
      o0 += *(LAS f32x4*)(pp + fs * 2048 + lane * 16); o1 += *(LAS f32x4*)(pp + fs * 2048 + 1024 + lane * 16);
      l += *(LAS float*)(pp + (odd ? 0 : 3072 + 512) + r * 16);
    }
    if (__builtin_expect(ANY(!(l <= 1.8446744e19f)), 0)) {
      const int qo = WS_QKV + head * QH + qt * QT + lane * 16;
      u32x4 q0 = __builtin_amdgcn_raw_buffer_load_b128(wsr, qo, 0, SC1), q1 = __builtin_amdgcn_raw_buffer_load_b128(wsr, qo + 1024, 0, SC1);
      set_pad(q0, q1, 0u, 0u, padl);
      f32x16 O = Z; float m = -__builtin_inff();
      _Pragma("nounroll") for (int t = 0; t < 64; ++t) {
        const int ko = WS_QKV + head * QH + t * QT + 2048;
        f32x16 s = MFMA(__builtin_amdgcn_raw_buffer_load_b128(wsr, ko + lane * 16, 0, SC1), q0, Z);
        s = MFMA(__builtin_amdgcn_raw_buffer_load_b128(wsr, ko + 1024 + lane * 16, 0, SC1), q1, s);
        float mn = m;
        UNROLL for (int i = 0; i < 16; ++i) mn = __builtin_fmaxf(mn, s[i]);
        mn = __builtin_fmaxf(mn, swap32(mn, h));
        const float f = EX(m - mn); m = mn;
        UNROLL for (int i = 0; i < 16; ++i) O[i] *= f;
        UNROLL for (int st = 0; st < 2; ++st) {
          unsigned vv[8];
          UNROLL for (int j = 0; j < 8; ++j) vv[j] = __builtin_amdgcn_raw_buffer_load_b16(wsr, ko + 2048 + (r >> 3) * 512 + (((16 * st + 8 * (j >> 2) + 4 * h + (j & 3)) ^ (4 * (r >> 3))) << 4) + (r & 7) * 2, 0, SC1) & 0xffffu;
          O = MFMA(((u32x4){vv[0] | vv[1] << 16, vv[2] | vv[3] << 16, vv[4] | vv[5] << 16, vv[6] | vv[7] << 16}),
                   ((u32x4){pk(EX(s[8 * st] - m), EX(s[8 * st + 1] - m)), pk(EX(s[8 * st + 2] - m), EX(s[8 * st + 3] - m)),
                            pk(EX(s[8 * st + 4] - m), EX(s[8 * st + 5] - m)), pk(EX(s[8 * st + 6] - m), EX(s[8 * st + 7] - m))}), O);
        }
      }
      const float lc = odd ? O[0] : O[12], lx = swap32(lc, h); l = padl ? lc : lx;
      o0 = fs ? (f32x4){O[8], O[9], O[10], O[11]} : (f32x4){O[0], O[1], O[2], O[3]};
      o1 = fs ? (f32x4){O[12], O[13], O[14], O[15]} : (f32x4){O[4], O[5], O[6], O[7]};
    }
    const float ri = __builtin_amdgcn_rcpf(l);
    const float r0 = padl && odd && fs == 0 ? 0.f : ri, r1 = padl && !odd && fs == 1 ? 0.f : ri;
    st128((u32x4){pk(o0[0] * r0, o0[1] * r0), pk(o0[2] * r0, o0[3] * r0), pk(o1[0] * r1, o1[1] * r1), pk(o1[2] * r1, o1[3] * r1)},
                                           wsr, lane * 16, WS_O + (qt * 16 + head) * 2048 + fs * 1024, SC1);
    }
  }
  TS(12);
  if (idx >= 28) { wait_bar<0>(); if (wid == 0) flag_st(ws, WS_FO + (qg * 16 + head) * 128, nonce); return; }
  {
    lbar();
    sfor<20, 28>(dma_d);
    u32x4 ew0, ew1;
    auto down2 = [&](int d, bool more) AI {
      const int o = dslot((d + 1) >> 2) + ((d + 1) & 3) * 2048 - dbo;
      ew0 = cat(trd(lds + db0 + o), trd(lds + db0 + o + 1024)); ew1 = cat(trd(lds + db1 + o), trd(lds + db1 + o + 1024));
      if (d == 10) rebase(L_WD);
      SBAR(); F0 = MFMA(dw0, hf[d], F0); F1 = MFMA(dw1, hf[d], F1); PIN(F0); PIN(F1); SBAR();
      if (more) { rd_d0(d + 2); rd_d1(d + 2); }
      SBAR(); F0 = MFMA(ew0, hf[d + 1], F0); F1 = MFMA(ew1, hf[d + 1], F1); PIN(F0); PIN(F1); SBAR();
    };
    static_assert(DN0 == 7, "units 0..9 ran beside the attention");
    down2(10, true); down2(12, true); down2(14, false);
    wait_bar<8>();
    if (wid == 0) flag_st(ws, WS_FO + (qg * 16 + head) * 128, nonce);
    TS(13);
    rd_d0(16); rd_d1(16);
    {
      const srd_t sO = SRD(wo, 448 * 896); const unsigned vo = vo_d + (wid * 56 - w3k * 448 - w3m * 32) * 896, lo = lds0 + L_WD + (wid >> 2) * 65536 + (wid & 3) * 7168;
      sfor<0, 7>([&](auto ic) AI { constexpr int i = decltype(ic)::v; dma<i * 8 * 896, i * 1024>(sO, vo, lo); });
    }
    down2(16, true); down2(18, false);
    wait_bar<7>();
    LANE_IDS();
    const int b30 = (4 * h + q) * 128 + p * 8 + ((grp ^ ((q >> 1) << 1)) << 5), b31 = b30 ^ 64;
    const int mt = mg3 * 2 + w3m, ft = w3k & 1, fs = w3k >> 1;
    const unsigned xo = (unsigned)((mt * 32 + r) * 896 + ng3 * 128 + ft * 64 + fs * 32);
    const u32x2 x0 = *(const u32x2*)(x + xo + h * 8), x1 = *(const u32x2*)(x + xo + 16 + h * 8);
    unsigned fo = ~nonce; const int foo = WS_FO + ((mg3 >> 1) * 16 + (lane & 15)) * 128;
    rebase(0); rd_d0(20); rd_d1(20); down2(20, true);
    if (wid == 0) asm volatile("buffer_load_dword %0, %1, %2, 0 offen sc1" : "+v"(fo) : "v"(foo), "s"(wsr) : "memory");
    down2(22, true); down2(24, true); down2(26, false);
    TS(14);
    if (wid == 0) {
      asm volatile("s_waitcnt vmcnt(0)" : "+v"(fo) :: "memory");
      _Pragma("nounroll") for (unsigned sp = 0; !ALL(fo == nonce) && sp < SPIN_CAP; ++sp) { fo = flag_ld(ws, foo); PIN(fo); }
    }
    wait_bar<0>();
    TS(15);
    u32x4 of[8];
    UNROLL for (int k = 0; k < 8; ++k) of[k] = __builtin_amdgcn_raw_buffer_load_b128(wsr, lane * 16 + (k & 3) * 1024, WS_O + (mt * 16 + w3k * 4) * 2048 + (k >> 2) * 4096, SC1);
    SBAR();
    UNROLL for (int g = 0; g < 4; ++g) {
      *(LAS f32x4*)(lds + wid * 8192 + g * 1024 + lane * 16) = (f32x4){F0[4 * g], F0[4 * g + 1], F0[4 * g + 2], F0[4 * g + 3]};
      *(LAS f32x4*)(lds + wid * 8192 + 4096 + g * 1024 + lane * 16) = (f32x4){F1[4 * g], F1[4 * g + 1], F1[4 * g + 2], F1[4 * g + 3]};
    }
    u32x4 wo0[8], wo1[8];
    UNROLL for (int k = 0; k < 8; ++k) {
      const lptr sl = lds + L_WD + (w3k >> 1) * 65536 + (w3k & 1) * PART + ((k >> 2) * 56 + ((k >> 1) & 1) * 24 + (k & 1) * 16) * 128;
      wo0[k] = cat(trd(sl + b30), trd(sl + b30 + 1024)); wo1[k] = cat(trd(sl + b31), trd(sl + b31 + 1024));
    }
    lbar();
    f32x4 g0 = {0.f, 0.f, 0.f, 0.f}, g1 = g0;
    UNROLL for (int k = 0; k < 4; ++k) { const lptr pp = lds + (k * 2 + w3m) * 8192 + ft * 4096 + fs * 2048 + lane * 16; g0 += *(LAS f32x4*)pp; g1 += *(LAS f32x4*)(pp + 1024); }
    f32x16 S0 = Z, S1 = Z;
    UNROLL for (int k = 0; k < 8; ++k) { S0 = MFMA(wo0[k], of[k], S0); S1 = MFMA(wo1[k], of[k], S1); }
    UNROLL for (int i = 0; i < 4; ++i) {
      g0[i] = g0[i] * __builtin_amdgcn_rcpf(1.f + EX(g0[i] * -1.4426950408889634f)); g1[i] = g1[i] * __builtin_amdgcn_rcpf(1.f + EX(g1[i] * -1.4426950408889634f)); }
    TS(16);
    const lptr ex = lds + L_WD;
    UNROLL for (int g = 0; g < 4; ++g) {
      *(LAS f32x4*)(ex + wid * 8192 + g * 1024 + lane * 16) = (f32x4){S0[4 * g], S0[4 * g + 1], S0[4 * g + 2], S0[4 * g + 3]};
      *(LAS f32x4*)(ex + wid * 8192 + 4096 + g * 1024 + lane * 16) = (f32x4){S1[4 * g], S1[4 * g + 1], S1[4 * g + 2], S1[4 * g + 3]};
    }
    lbar();
    f32x4 s0 = {0.f, 0.f, 0.f, 0.f}, s1 = s0;
    UNROLL for (int k = 0; k < 4; ++k) { const lptr pp = ex + (k * 2 + w3m) * 8192 + ft * 4096 + fs * 2048 + lane * 16; s0 += *(LAS f32x4*)pp; s1 += *(LAS f32x4*)(pp + 1024); }
    auto lo = [](unsigned v) AI { return U2F(v << 16); }; auto hi = [](unsigned v) AI { return U2F(v & 0xffff0000u); };
    const u32x2 a = {pk(__builtin_fmaf(s0[0], g0[0], lo(x0[0])), __builtin_fmaf(s0[1], g0[1], hi(x0[0]))), pk(__builtin_fmaf(s0[2], g0[2], lo(x0[1])), __builtin_fmaf(s0[3], g0[3], hi(x0[1])))};
    const u32x2 c = {pk(__builtin_fmaf(s1[0], g1[0], lo(x1[0])), __builtin_fmaf(s1[1], g1[1], hi(x1[0]))), pk(__builtin_fmaf(s1[2], g1[2], lo(x1[1])), __builtin_fmaf(s1[3], g1[3], hi(x1[1])))};
    *(u32x4*)(out + xo + h * 16) = widen(a, c);
    TS(17); TS_OUT();
  }
}

#if defined(__HIP_DEVICE_COMPILE__)
#define NOPK __attribute__((target("no-packed-fp32-ops")))
#else
#define NOPK
#endif
__global__ __launch_bounds__(512, 2) NOPK void fused_kernel(const char* x, const char* wqkv, const char* wo, const char* wg, const char* wu, const char* wd,
                                                       char* ws, char* out) {
  extern __shared__ __attribute__((aligned(16))) char smem[];
  const lptr lds = (lptr)smem;
  const unsigned lds0 = (unsigned)(uintptr_t)lds;
  const int tid = __builtin_amdgcn_workitem_id_x(), lane = tid & 63, wid = __builtin_amdgcn_readfirstlane(tid >> 6);
  const int rr = lane >> 3, cc = lane & 7;
  const int b = __builtin_amdgcn_workgroup_id_x(), xcd = b & 7, idx = b >> 3;
  const int ng = (xcd & 3) * 4 + (idx & 3), mg = (xcd >> 2) * 8 + (idx >> 2);
  unsigned tsv = 0; TSR(62); TS(0);
  const int rot = 2 * ((idx ^ (idx >> 2)) & ROTM);
  unsigned kc[7];
  UNROLL for (int c = 0; c < 7; ++c) kc[c] = (unsigned)((c + rot >= 7 ? c + rot - 7 : c + rot) * 64);
  const srd_t xr = SRD(x, 2048 * 896);
  const unsigned xv0 = (unsigned)((mg * 128 + wid * 16 + rr) * 896 + ((cc ^ rr) << 4));
  const unsigned xv1 = (unsigned)((mg * 128 + wid * 16 + 8 + (rr ^ 1)) * 896 + ((cc ^ rr ^ 1) << 4));
  const unsigned lw2k = lds0 + wid * 2048;
  auto dma_x = [&](auto cc_) AI { constexpr int c = decltype(cc_)::v; dma2<(c & 3) * 16384>(xr, xv0, xv1, lw2k, kc[c], 2); };
  const srd_t wsr = SRD(ws, WS_END);
  u32x4 srow; unsigned nonce;
  const int srw = mg * 128 + ng * 8 + wid;
  asm volatile("buffer_load_dwordx4 %0, %2, %3, %4 offen\n\ts_load_dword %1, %5, 0" : "=&v"(srow), "=&s"(nonce) : "v"(lane * 16), "s"(SRD(x, srw * 896 + 896)), "s"(srw * 896), "s"(ws) : "memory");
  dma_x(IC<0>{});
  SBAR();
  const int r = lane & 31, h = lane >> 5, q = (lane & 15) >> 2, p = lane & 3, grp = (lane >> 4) & 1;
  const f32x16 Z = {};
  const int head = 2 * xcd + (idx & 1), qg = idx >> 1;
  auto dma_kv = [&](auto zc, unsigned kvv) AI { constexpr int z = decltype(zc)::v; dma<(z >> 1) * QT + (z & 1) * 1024, ((z >> 1) & 3) * 16384 + (z & 1) * 1024, true>(wsr, kvv, lw2k); };
  {
    const int wm = wid & 3, wn = wid >> 2;
    const bool bq = ng <= 10;
    const unsigned st1 = bq ? 2688 : 3584;
    unsigned va = (16 * wm + rr) * st1 + (((cc << 4) ^ (grp << (bq ? 6 : 5))) ^ (bq ? 0 : wn << 6)) + (bq ? 2 * ng + wn : ng + 12) * 128, vb = va + 8 * st1;
    const char* wgu = wn ? wu : wg;
    const srd_t s2 = SRD(wgu, 448 * 3584), s1 = SRD(bq ? wqkv : wgu, 448 * st1);
    const int pfw = (mg * 2 + (ng & 1)) * 8 + wid;
    const srd_t s3 = SRD(ng < 14 ? wgu : pfw < 196 ? wd : wo, ng < 14 ? 448 * 3584 : pfw < 196 ? 1792 * 896 : 448 * 896);
    auto dma_w = [&](auto mc) AI {
      constexpr int m = decltype(mc)::v;
      if constexpr (m == 7) { va = (unsigned)((16 * wm + rr) * 3584 + ((cc ^ (grp << 1) ^ (wn << 2)) << 4)) + ng * 128; vb = va + 8 * 3584; PIN(va); PIN(vb); }
      if constexpr (m == 14) {
        va = (unsigned)((16 * wm + (lane >> 2)) * 3584 + (((lane & 3) ^ (wn << 1)) << 4)) + 2048 + ng * 64;
        if (ng >= 14) va = (unsigned)(((pfw < 196 ? pfw : pfw - 196) * 64 + lane) * 128);
        PIN(va);
      }
      if constexpr (m < 7) dma2<wpos(m)>(s1, va, vb, lw2k, kc[m], st1);
      else if constexpr (m < 14) dma2<wpos(m)>(s2, va, vb, lw2k, kc[m - 7], 3584);
      else dma1<wpos(m)>(s3, va, lds0 + wid * 1024, kc[m - 14], ng < 14 ? 3584 : 1 << 20);
    };
    dma_w(IC<0>{});
    dma_x(IC<1>{});
    SBAR();
    const int rowb = (8 * h + q) * 128 + p * 8;
    int b20 = grp * 8192 + rowb + (((2 * wn) ^ (q >> 1) ^ (grp << 1)) << 5);
    int b10 = bq ? wn * 8192 + rowb + ((grp ^ ((q >> 1) << 1)) << 5) : b20, b11 = b10 ^ (bq ? 64 : 32);
    int xb[4];
    UNROLL for (int i = 0; i < 4; ++i) { xb[i] = (4 * wm + (r >> 3)) * 1024 + ((r & 7) ^ ((r >> 3) & 1)) * 128 + (((2 * i + h) ^ (r & 7)) << 4); PIN(xb[i]); }
    PIN(b10); PIN(b11); PIN(b20);
    UNROLL for (int c = 2; c < 7; ++c) asm volatile("" : "+s"(kc[c]));
    SBAR();
    u32x4 xf[28];
    auto row_stats = [&]() AI {
      float a = dot2(srow[0], ONES2, 0.f), c = dot2(srow[0], srow[0], 0.f);
      UNROLL for (int i = 1; i < 4; ++i) { a = dot2(srow[i], ONES2, a); c = dot2(srow[i], srow[i], c); }
      a = wave_sum(a); c = wave_sum(c);
      const float mean = a * (1.f / 448.f), var = __builtin_fmaxf(c * (1.f / 448.f) - mean * mean, 0.f);
      const unsigned g = pk(-mean, __builtin_amdgcn_sqrtf(rbf(rbf(var) + 1e-5f)));
      if (lane == 63) __hip_atomic_store((GAS unsigned long long*)(ws + WS_ST + srw * 8), (unsigned long long)nonce << 32 | g, RLX_AGENT);
    };
    auto rd_x = [&](int k0, int k1) AI { UNROLL for (int ks = k0; ks < k1; ++ks) xf[ks] = ld128(lds + xb[ks & 3] + ((ks >> 2) & 3) * 16384); };
    u32x2 wf[NWF][4];
    int ra0 = 0, ra1 = 0;
    auto rd_w = [&](int n) AI {
      if (n < 56) {
        const int m = n >> 2, o = (n & 3) * 2048;
        if ((n & 3) == 0) { ra0 = (m < 7 ? b10 : b20) + wpos(m); ra1 = (m < 7 ? b11 : b20 ^ 32) + wpos(m); PIN(ra0); PIN(ra1); }
        wf[n % NWF][0] = trd(lds + ra0 + o); wf[n % NWF][1] = trd(lds + ra0 + o + 512); wf[n % NWF][2] = trd(lds + ra1 + o); wf[n % NWF][3] = trd(lds + ra1 + o + 512);
      } else {
        const int o = ((n - 56) >> 1) * 8192 + (n & 1) * 2048;
        if (n == 56) { ra0 = wpos(14) + grp * 4096 + (8 * h + q) * 64 + ((wn ^ grp) << 5) + p * 8; PIN(ra0); }
        wf[n % NWF][0] = trd(lds + ra0 + o); wf[n % NWF][1] = trd(lds + ra0 + o + 256); wf[n % NWF][2] = trd(lds + ra0 + o + 1024); wf[n % NWF][3] = trd(lds + ra0 + o + 1280);
      }
    };
    SBAR();
    asm volatile("s_waitcnt vmcnt(6) lgkmcnt(0)" : "+v"(srow), "+s"(nonce) :: "memory");
    nonce = nonce * 0x9E3779B1u + 0x7F4A7C15u;
    row_stats();
    SBAR();
    dma_w(IC<1>{});
    wait_bar<5>();
    TS(1);
    UNROLL for (int n = 0; n < RA; ++n) rd_w(n);
    rd_x(0, 4);
    dma_x(IC<2>{});
    f32x16 A0 = Z, A1 = Z, B0 = Z, B1 = Z, C0 = Z;
    float cs0 = 0.f, cs1 = 0.f, rstd = 0.f, sig = 0.f, kg = 0.f; unsigned mm = 0; u32x2 sg = {0u, ~nonce};
    const int mt = mg * 4 + wm;
    const unsigned csel = ((wm & 1) ? 0x05040c0cu : 0x01000c0cu) + ((wm >> 1) ? 0x02020000u : 0u);
    auto colsum = [&](int n, int hh) AI {
      cs0 += U2F(__builtin_amdgcn_perm(wf[n % NWF][2 + hh][0], wf[n % NWF][hh][0], csel)); cs1 += U2F(__builtin_amdgcn_perm(wf[n % NWF][2 + hh][1], wf[n % NWF][hh][1], csel));
      PIN(cs0); PIN(cs1);
    };
    auto cs_out = [&]() AI {
      const float cs = cs0 + cs1, hi = rbf(cs); *(LAS unsigned*)(lds + L_CS + wn * 1024 + lane * 16 + ((wm & 1) * 2 + (wm >> 1)) * 4) = pk(hi, cs - hi); cs0 = 0.f; cs1 = 0.f; };
    auto fix1 = [&](f32x16& acc) AI {
      acc = MFMA(ld128(lds + L_CS + wn * 1024 + lane * 16), ((u32x4){mm, mm, mm, mm}), acc); };
    auto fix2 = [&](f32x16& a0, f32x16& a1) AI {
      const u32x4 c = ld128(lds + L_CS + wn * 1024 + lane * 16);
      a0 = MFMA(c, ((u32x4){mm, mm, 0u, 0u}), a0); a1 = MFMA(c, ((u32x4){0u, 0u, mm, mm}), a1); PIN(a0); PIN(a1); };
    auto glu = [&](const f32x16& acc, int i) AI { return acc[i + 8] * __builtin_amdgcn_rcpf(__builtin_fmaf(EX(acc[i] * kg), sig, sig)); };
    auto glu8 = [&](const f32x16& acc) AI { return (u32x4){pk(glu(acc, 0), glu(acc, 1)), pk(glu(acc, 2), glu(acc, 3)), pk(glu(acc, 4), glu(acc, 5)), pk(glu(acc, 6), glu(acc, 7))}; };
    const int hs2 = WS_H + (mt * 112 + ng * 4 + wn * 2) * 1024;
    u32x4 tb0 = {0u, 0u, 0u, 0u}, tb1 = tb0;
    auto epi1 = [&](const f32x16& acc, int t) AI {
      if (bq) {
        const u32x4 e = t ? tb1 : tb0; const unsigned o[3] = {e[0] ^ (r << 4), e[1] ^ (r << 4), e[2] ^ (r << 4)};
        const unsigned m[2] = {(unsigned)__builtin_amdgcn_sbfe(e[3], 0, 1), (unsigned)__builtin_amdgcn_sbfe(e[3], 1, 1)};
        const float sc = 2 * ng + wn < 7 ? rstd * (0.18898223650461362f * 1.4426950408889634f) : rstd;
        u32x4 v[2];
        UNROLL for (int pp = 0; pp < 2; ++pp)
          v[pp] = widen((u32x2){pk(acc[8 * pp] * sc, acc[8 * pp + 1] * sc), pk(acc[8 * pp + 2] * sc, acc[8 * pp + 3] * sc)},
                        (u32x2){pk(acc[8 * pp + 4] * sc, acc[8 * pp + 5] * sc), pk(acc[8 * pp + 6] * sc, acc[8 * pp + 7] * sc)});
        u32x4 d0 = {v[0][0], v[0][1], bfi(m[0], ONES2, v[0][2]), bfi(m[0], PAD1, v[0][3])}, d1 = {v[1][0], v[1][1], bfi(m[1], ONES2, v[1][2]), bfi(m[1], PAD1, v[1][3])};
        u32x4 d2 = {ONES2, PAD1, bfi(m[0], v[0][2], v[1][2]), bfi(m[0], v[0][3], v[1][3])};
        PIN(d0); PIN(d1); PIN(d2); SBAR();
        __builtin_amdgcn_raw_buffer_store_b128(d0, wsr, o[0], mt * QT, SC1); __builtin_amdgcn_raw_buffer_store_b128(d1, wsr, o[1], mt * QT, SC1);
        st128(d2, wsr, o[2], mt * QT, SC1);
      } else st128(glu8(acc), wsr, lane * 16 + t * 1024, hs2 + 12 * 4096, SC1);
    };
    float hv[8]; u32x4 hp, hq; unsigned fl0 = 0, fl1 = 1, fl2 = 0, fl3 = 0, kvv = 0;
    auto poll_fq = [&]() AI {
      asm volatile("buffer_load_dword %0, %4, %5, 0 offen sc1\n\tbuffer_load_dword %1, %4, %5, 0 offen offset:128 sc1\n\t"
                   "buffer_load_dword %2, %4, %5, 0 offen offset:256 sc1\n\tbuffer_load_dword %3, %4, %5, 0 offen offset:384 sc1"
                   : "+v"(fl0), "+v"(fl1), "+v"(fl2), "+v"(fl3) : "v"(WS_FQ + lane * 512), "s"(wsr) : "memory"); };
    float ge[16];
    auto glu_step = [&](auto zc) AI {
      constexpr int z = decltype(zc)::v, t = z - z / 3, on = z % 3 != 2;
      if constexpr (on && t >= 2 && t < 18) { constexpr int e = t - 2; hv[e & 7] = (e < 8 ? B0 : B1)[(e & 7) + 8] * ge[e]; PIN(hv[e & 7]);
        if constexpr ((e & 7) == 7) { (e < 8 ? hp : hq) = (u32x4){pk(hv[0], hv[1]), pk(hv[2], hv[3]), pk(hv[4], hv[5]), pk(hv[6], hv[7])}; PIN(e < 8 ? hp : hq); } }
      if constexpr (on && t >= 1 && t < 17) { constexpr int e = t - 1; ge[e] = __builtin_amdgcn_rcpf(__builtin_fmaf(ge[e], sig, sig)); PIN(ge[e]); }
      if constexpr (on && t < 16) { ge[t] = EX((t < 8 ? B0 : B1)[t & 7] * kg); PIN(ge[t]); }
      if constexpr (z == 27) { st128(hp, wsr, lane * 16, hs2, SC1); st128(hq, wsr, lane * 16 + 1024, hs2, SC1); }
    };
    const int sgo = WS_ST + (mg * 128 + wm * 32 + r) * 8;

    sfor<0, 70>([&](auto nc) AI {
      constexpr int n = decltype(nc)::v, u = n & 7;
      constexpr int k0 = n < 28 ? n : n < 56 ? n - 28 : 2 * (n - 56), k1 = n < 56 ? k0 : k0 + 1;
      f32x16& D0 = n < 28 ? A0 : n < 56 ? B0 : C0; f32x16& D1 = n < 28 ? A1 : n < 56 ? B1 : C0;
      constexpr bool early = n == 54 || n == 55 || n >= 68;
      PIN(wf[n % NWF][3]);
      if (u == 7) __builtin_amdgcn_s_setprio(3); if (u == 1) __builtin_amdgcn_s_setprio(2); if (u == 3) __builtin_amdgcn_s_setprio(1); if (u == 5) __builtin_amdgcn_s_setprio(0);
      SBAR();
      D0 = MFMA(cat(wf[n % NWF][0], wf[n % NWF][1]), xf[k0], D0); PIN(D0);
      SBAR();
      if constexpr (u == 6 || n == 68 || n == 2) {
        if constexpr (n == 54 || n == 68) { UNROLL for (int e = 0; e < RA; ++e) { colsum(n + e, 0); colsum(n + e, 1); } cs_out(); }
        if (n == 62 && wid == 0) {
          _Pragma("nounroll") for (unsigned sp = 0; sp < SPIN_CAP; ++sp) {
            asm volatile("s_waitcnt vmcnt(0)" : "+v"(fl0), "+v"(fl1), "+v"(fl2), "+v"(fl3) :: "memory");
            if (ALL(((fl0 ^ nonce) | (fl1 ^ nonce) | (fl2 ^ nonce) | (fl3 ^ nonce)) == 0)) break;
            poll_fq();
          }
          asm volatile("s_waitcnt vmcnt(0)" : "+v"(fl0), "+v"(fl1), "+v"(fl2), "+v"(fl3) :: "memory");
          if (b == 0) flag_st(ws, WS_E, nonce);
        }
        if (n == 2) wait_bar<6>(); else if (n < 24) wait_bar<2>(); else if (n == 30) wait_bar<8>(); else if (n == 38) wait_bar<6>(); else if (n == 46) wait_bar<4>(); else if (n == 54) wait_bar<3>();
        else if (n == 62) wait_bar<0>(); else wait_bar<4>();
        if (n == 46 && wid == 0) flag_st(ws, WS_FQ + b * 128, nonce);
        if (n == 6) TS(2); if (n == 14) TS(3); if (n == 30) TS(4); if (n == 46) TS(5); if (n == 62) TS(6); if (n == 68) TS(7);
        SBAR();
      }
      if constexpr (n + RA < 70) rd_w(n + RA);
      if constexpr (n == 2) rd_x(4, 8);
      if constexpr (u == 6 && n < 24) rd_x(n + 2, n + 6);
      if constexpr (u == 0 && n > 0 && n < 24) rd_x(n + 4, n + 8);
      if constexpr (!early) colsum(n, 0);
      if constexpr (n >= 56) glu_step(IC<2 * (n - 56)>{});
      SBAR();
      D1 = MFMA(cat(wf[n % NWF][2], wf[n % NWF][3]), xf[k1], D1); PIN(D1);
      SBAR();
      if constexpr (!early) colsum(n, 1);
      if constexpr (n == 0) dma_w(IC<2>{}); if constexpr (n == 1) dma_x(IC<3>{}); if constexpr (n == 2) dma_w(IC<3>{}); if constexpr (n == 4) dma_w(IC<4>{});
      if constexpr (n == 6) dma_x(IC<4>{}); if constexpr (n == 7) dma_x(IC<5>{}); if constexpr (n == 8) dma_w(IC<5>{}); if constexpr (n == 10) dma_w(IC<6>{});
      if constexpr (n == 14) dma_x(IC<6>{}); if constexpr (n == 15) dma_w(IC<7>{}); if constexpr (n == 18) dma_w(IC<8>{});
      if constexpr (n == 22) dma_w(IC<9>{}); if constexpr (n == 24) dma_w(IC<10>{}); if constexpr (n == 25) dma_w(IC<11>{}); if constexpr (n == 27) dma_w(IC<12>{}); if constexpr (n == 29) dma_w(IC<13>{});
      if constexpr (n == 17) asm volatile("buffer_load_dwordx2 %0, %1, %2, 0 offen sc1" : "+v"(sg) : "v"(sgo), "s"(wsr) : "memory");
      if constexpr (n == 16) asm volatile("global_load_dwordx4 %0, %2, %3\n\tglobal_load_dwordx4 %1, %2, %3 offset:32" : "+v"(tb0), "+v"(tb1) : "v"(h * 16), "s"(&qkv_tab.v[bq ? 4 * ng + 2 * wn : 0]) : "memory");
      if constexpr (n >= 39 && n < 53 && (n & 1)) dma_w(IC<14 + (n - 39) / 2>{});
      if constexpr (n == 27) cs_out();
      if constexpr (n == 23) {
        PIN(sg); PIN(tb0); PIN(tb1);
        _Pragma("nounroll") for (unsigned sp = 0; !ALL(sg[1] == nonce) && sp < SPIN_CAP; ++sp)
          asm volatile("buffer_load_dwordx2 %0, %1, %2, 0 offen sc1\n\ts_waitcnt vmcnt(0)" : "+v"(sg) : "v"(sgo), "s"(wsr) : "memory");
        sig = U2F(sg[0] & 0xffff0000u); rstd = __builtin_amdgcn_rcpf(sig); mm = __builtin_amdgcn_perm(sg[0], sg[0], 0x01000100u); kg = -1.4426950408889634f * rstd;
        PIN(sig); PIN(rstd); PIN(kg); PIN(mm);
      }
      if constexpr (n == 30) fix2(A0, A1);
      if ((n == 31 && wn == 0) || (n == 33 && wn == 1)) epi1(A0, 0);
      if ((n == 35 && wn == 0) || (n == 37 && wn == 1)) epi1(A1, 1);
      if (n == 54 && wid == 0) poll_fq();
      if constexpr (n == 55) fix2(B0, B1);
      if constexpr (n >= 56) glu_step(IC<2 * (n - 56) + 1>{});
      if constexpr (n == 62) {
        kvv = (unsigned)(WS_QKV + head * QH + (wid >> 1) * 16 * QT + 2048 + (wid & 1) * 2048 + lane * 16);
        dma<0, L_Q, true>(wsr, WS_QKV + head * QH + (qg * 4 + (wid >> 1)) * QT + (wid & 1) * 1024 + lane * 16, lds0 + wid * 1024); dma_kv(IC<0>{}, kvv); dma_kv(IC<1>{}, kvv); }
      if constexpr (n == 63) { dma_kv(IC<2>{}, kvv); dma_kv(IC<3>{}, kvv); }
      if constexpr (n >= 64 && n < 68) dma_kv(IC<n - 60>{}, kvv);
    });
    SBAR();
    fix1(C0);
    st128(glu8(C0), wsr, ng < 14 ? lane * 16 : OOB, WS_H + (mt * 112 + 64 + ng * 2 + wn) * 1024, SC1);
    SBAR();
    TS(8);
  }
  return phase23(x, wo, wd, ws, out, lds, nonce TSARG);
}

extern "C" void kernel_launch(void* const* d_in, const int* in_sizes, int n_in, void* d_out, int out_size, void* d_ws, size_t ws_size,
                              hipStream_t stream) {
  (void)hipFuncSetAttribute((const void*)fused_kernel, hipFuncAttributeMaxDynamicSharedMemorySize, LDS_BYTES);
  fused_kernel<<<256, 512, LDS_BYTES, stream>>>((const char*)d_in[0], (const char*)d_in[1], (const char*)d_in[2], (const char*)d_in[4], (const char*)d_in[5],
                                                (const char*)d_in[6], (char*)d_ws, (char*)d_out);
}
```

```cpp
#include <hip/hip_runtime.h>
#include <hip/hip_bf16.h>
#include <stdint.h>

typedef short bf16x8 __attribute__((ext_vector_type(8)));
typedef short s16x4 __attribute__((ext_vector_type(4)));
typedef float f32x16 __attribute__((ext_vector_type(16)));
typedef float f32x4 __attribute__((ext_vector_type(4)));
typedef float f32x2 __attribute__((ext_vector_type(2)));
typedef unsigned u32x4 __attribute__((ext_vector_type(4)));
typedef unsigned u32x2 __attribute__((ext_vector_type(2)));
typedef __bf16 bf2 __attribute__((ext_vector_type(2)));
typedef __amdgpu_buffer_rsrc_t srd_t;
#define LAS __attribute__((address_space(3)))
#define GAS __attribute__((address_space(1)))
typedef LAS char* lptr;
#define DI __device__ __forceinline__
#define AI __attribute__((always_inline))
#define MFMA(a, b, c) __builtin_amdgcn_mfma_f32_32x32x16_bf16(__builtin_bit_cast(bf16x8, (a)), __builtin_bit_cast(bf16x8, (b)), (c), 0, 0, 0)
#define PIN(x) asm volatile("" : "+v"(x))
#define SBAR() __builtin_amdgcn_sched_barrier(0)
#define RLX_AGENT __ATOMIC_RELAXED, __HIP_MEMORY_SCOPE_AGENT
#define EX(v) __builtin_amdgcn_exp2f(v)
#define F2U(x) __builtin_bit_cast(unsigned, (float)(x))
#define U2F(x) __builtin_bit_cast(float, (unsigned)(x))
#define ANY(p) (__builtin_amdgcn_ballot_w64(p) != 0)
#define ALL(p) (__builtin_amdgcn_ballot_w64(!(p)) == 0)
#define UNROLL _Pragma("unroll")
#define SRD(p, n) __builtin_amdgcn_make_buffer_rsrc((void*)(p), 0, (n), 0x00020000)

constexpr int WS_E = 0;
constexpr int WS_FQ = 4096;
constexpr int WS_FH = WS_FQ + 256 * 128;
constexpr int WS_FO = WS_FH + 256 * 128;
constexpr int WS_ST = WS_FO + 256 * 128;
constexpr int WS_QKV = 1 << 20;
constexpr int QT = 6144, QH = 64 * QT;
constexpr int WS_O = WS_QKV + 16 * QH;
constexpr int WS_H = WS_O + (2 << 20);
constexpr int WS_END = WS_H + 64 * 112 * 1024;
constexpr int OOB = 1 << 30;
constexpr int SC1 = 16;
constexpr unsigned SPIN_CAP = 1u << 20;

#define TS(i) do { } while (0)
#define TSR(i) do { } while (0)
#define TS_OUT() do { } while (0)

template <int I> struct IC { static constexpr int v = I; };
template <int B, int E, class F> DI void sfor(F&& f) { if constexpr (B < E) { f(IC<B>{}); sfor<B + 1, E>(f); } }
template <int N> DI void wait_bar() { asm volatile("s_waitcnt vmcnt(%0) lgkmcnt(0)\n\ts_barrier" :: "n"(N) : "memory"); }
template <int N> DI void vwait_bar() { asm volatile("s_waitcnt vmcnt(%0)\n\ts_barrier" :: "n"(N) : "memory"); }
DI void lbar() { asm volatile("s_waitcnt lgkmcnt(0)\n\ts_barrier" ::: "memory"); }
DI unsigned pk(float a, float b) { f32x2 v = {a, b}; return __builtin_bit_cast(unsigned, __builtin_convertvector(v, bf2)); }
DI float rbf(float a) { return U2F(pk(a, a) & 0xffff0000u); }
DI float dot2(unsigned a, unsigned b, float c) { return __builtin_amdgcn_fdot2_f32_bf16(__builtin_bit_cast(bf2, a), __builtin_bit_cast(bf2, b), c, false); }
DI u32x2 trd(lptr p) { return __builtin_bit_cast(u32x2, __builtin_amdgcn_ds_read_tr16_b64_v4i16((LAS s16x4*)p)); }
DI u32x4 cat(u32x2 a, u32x2 b) { return (u32x4){a[0], a[1], b[0], b[1]}; }
DI u32x4 ld128(lptr p) { return *(LAS u32x4*)p; }
DI unsigned bfi(unsigned m, unsigned a, unsigned b) { return (m & a) | (~m & b); }
template <int G, int L, bool DEV = false> DI void dma(srd_t srd, unsigned voff, unsigned lbase) {
  unsigned t;
  if constexpr (DEV)
    asm volatile("s_add_u32 m0, %3, %5\n\ts_mov_b32 %0, %4\n\tbuffer_load_dwordx4 %1, %2, %0 offen sc1 lds" : "=&s"(t) : "v"(voff), "s"(srd), "s"(lbase), "n"(G), "n"(L) : "memory", "scc");
  else
    asm volatile("s_add_u32 m0, %3, %5\n\ts_mov_b32 %0, %4\n\tbuffer_load_dwordx4 %1, %2, %0 offen lds" : "=&s"(t) : "v"(voff), "s"(srd), "s"(lbase), "n"(G), "n"(L) : "memory", "scc");
}
template <int L> DI void dma2(srd_t srd, unsigned voff0, unsigned voff1, unsigned lbase, unsigned ga, unsigned gb) {
  unsigned t;
  asm volatile("s_add_u32 m0, %4, %5\n\ts_mul_i32 %0, %6, %7\n\tbuffer_load_dwordx4 %1, %3, %0 offen lds\n\ts_add_u32 m0, m0, 1024\n\ts_nop 0\n\tbuffer_load_dwordx4 %2, %3, %0 offen lds"
               : "=&s"(t) : "v"(voff0), "v"(voff1), "s"(srd), "s"(lbase), "n"(L), "s"(ga), "s"(gb) : "memory", "scc");
}
template <int L> DI void dma1(srd_t srd, unsigned voff, unsigned lbase, unsigned ga, unsigned gb) {
  unsigned t;
  asm volatile("s_add_u32 m0, %3, %4\n\ts_mul_i32 %0, %5, %6\n\tbuffer_load_dwordx4 %1, %2, %0 offen lds" : "=&s"(t) : "v"(voff), "s"(srd), "s"(lbase), "n"(L), "s"(ga), "s"(gb) : "memory", "scc");
}
DI void touch(srd_t srd, unsigned voff, unsigned ldst) { asm volatile("s_mov_b32 m0, %2\n\ts_nop 0\n\tbuffer_load_dword %0, %1, 0 offen lds" :: "v"(voff), "s"(srd), "s"(ldst) : "memory"); }
DI void st128(u32x4 v, srd_t srd, int voff, int soff, int aux) { __builtin_amdgcn_raw_buffer_store_b128(v, srd, voff, soff, SC1); SBAR(); asm volatile("s_nop 1"); SBAR(); }
DI float swap32(float v, int h) {
  auto r = __builtin_amdgcn_permlane32_swap(F2U(v), F2U(v), false, false);
  return U2F(h ? r[0] : r[1]);
}
DI float max32(float v) {
  auto r = __builtin_amdgcn_permlane32_swap(F2U(v), F2U(v), false, false);
  return __builtin_fmaxf(U2F(r[0]), U2F(r[1]));
}
DI u32x4 widen(u32x2 a, u32x2 b) {
  auto r0 = __builtin_amdgcn_permlane32_swap(a[0], b[0], false, false); auto r1 = __builtin_amdgcn_permlane32_swap(a[1], b[1], false, false);
  return (u32x4){r0[0], r1[0], r0[1], r1[1]};
}
DI float wave_sum(float v) {
  v += U2F(__builtin_amdgcn_update_dpp(0u, F2U(v), 0x111, 0xf, 0xf, true));
  v += U2F(__builtin_amdgcn_update_dpp(0u, F2U(v), 0x112, 0xf, 0xf, true));
  v += U2F(__builtin_amdgcn_update_dpp(0u, F2U(v), 0x114, 0xf, 0xf, true));
  v += U2F(__builtin_amdgcn_update_dpp(0u, F2U(v), 0x118, 0xf, 0xf, true));
  v += U2F(__builtin_amdgcn_update_dpp(0u, F2U(v), 0x142, 0xa, 0xf, true));
  v += U2F(__builtin_amdgcn_update_dpp(0u, F2U(v), 0x143, 0xc, 0xf, true));
  return v;
}
DI unsigned flag_ld(const char* ws, int off) { return __hip_atomic_load((GAS unsigned*)(ws + off), RLX_AGENT); }
DI void flag_st(char* ws, int off, unsigned v) {
  if (__builtin_amdgcn_mbcnt_hi(~0u, __builtin_amdgcn_mbcnt_lo(~0u, 0u)) == 0) __hip_atomic_store((GAS unsigned*)(ws + off), v, RLX_AGENT); }

constexpr int RA = 2, NWF = RA + 1;
constexpr int PART = 14336;
constexpr int L_W = 65536;
constexpr int L_CS = 9 * 16384;
constexpr int L_PF = L_CS + 2048;
constexpr int L_Q = 65536;
constexpr int wpos(int m) { return m < 9 ? L_W + (m % 5) * 16384 : m == 9 ? 49152 : m == 10 ? L_W : m < 14 ? (m - 11) * 16384 : L_W + 16384 + (m - 14) * 8192; }
#define FQP 52
#define FQC 59
#define ROTM 3
constexpr int L_WD = 65536;
constexpr int LDS_BYTES = 163840;
constexpr unsigned ONES2 = 0x3f803f80u, PAD0 = 0x00003f80u;
static_assert(L_PF + 2048 <= LDS_BYTES, "LDS map");

constexpr int DN0 = 7;
constexpr int kv_at(int s, int p) { return p == 0 && s > 0 && s + 3 < 16 ? (s + 3) * 2 + 1 : p == 7 && s + 4 < 16 ? (s + 4) * 2 : -1; }
constexpr int wd_at(int s, int p) {
  if (s >= 1 && s <= 3) return p == 1 ? s * 4 - 4 : p == 3 ? s * 4 - 3 : p == 4 ? s * 4 - 2 : p == 5 ? s * 4 - 1 : -1;
  for (int g = 0; g < 2; ++g) { if (s == 10 + 4 * g && p == 7) return 12 + 4 * g; if (s == 11 + 4 * g) return p == 0 ? 13 + 4 * g : p == 2 ? 14 + 4 * g : p == 5 ? 15 + 4 * g : -1; }
  return -1;
}
constexpr int h_at(int s, int p) { const int z = (s - 5) * 3 + (p == 1 ? 1 : p == 3 ? 2 : 3); if (s == 15) return p == 1 ? 24 : p == 3 ? 25 : p == 4 ? 26 : p == 7 ? 27 : -1;
  return s == 4 && p == 7 ? 0 : s >= 5 && (p == 1 || p == 3 || p == 4) && z < 24 ? z : -1; }
constexpr bool pf_at(int s, int p) { return s == 4 && p == 1; }
constexpr int n_at(int s, int p) { return (kv_at(s, p) >= 0) + (wd_at(s, p) >= 0) + (h_at(s, p) >= 0) + pf_at(s, p); }
constexpr int cnt_after(int s0, int p0, int s) { int n = 0; for (int z = s0 * 8 + p0 + 1; z < s * 8 + 6; ++z) n += n_at(z / 8, z % 8); return n; }
constexpr int dslot(int sub) { return sub < 5 ? L_WD + (sub % 3) * 32768 : (sub - 5) * 32768; }

struct QkvTab { unsigned v[44][2][4]; };
constexpr QkvTab make_tab() {
  QkvTab t = {};
  for (int tile = 0; tile < 44; ++tile) for (int h = 0; h < 2; ++h) {
    const int col0 = tile * 32, sec = col0 / 448; unsigned* e = t.v[tile][h];
    e[0] = e[1] = e[2] = OOB; e[3] = 0;
    if (tile < 42) for (int pp = 0; pp < 2; ++pp) {
      const int cu = (col0 - sec * 448) / 8 + 2 * pp + h, j = cu / 7, w = cu % 7, hd = 2 * j + (w > 3), ch = w > 3 ? w - 3 : w;
      e[pp] = WS_QKV + sec * 2048 + hd * QH + ch * 512 + (sec == 2 ? ch * 64 : 0);
      if (w == 3) { e[2] = WS_QKV + sec * 2048 + (hd + 1) * QH; e[3] |= 1u << pp; }
    }
  }
  return t;
}
__device__ const QkvTab qkv_tab = make_tab();

#define TSARG
#define TSPAR
#define LANE_IDS() int lane; asm volatile("v_mbcnt_lo_u32_b32 %0, -1, 0\n\tv_mbcnt_hi_u32_b32 %0, -1, %0" : "=v"(lane));        \
  const int r = lane & 31, h = lane >> 5, q = (lane & 15) >> 2, p = lane & 3, grp = (lane >> 4) & 1, rr = lane >> 3, cc = lane & 7
DI void phase23(const char* x, const char* wo, const char* wd, char* ws, char* out, lptr lds, unsigned nonce, u32x4 QA0, u32x4 QA1, u32x4 QB0, u32x4 QB1,
                u32x4 k0, u32x4 k1, u32x4 v0, f32x16 sa, lptr kb, lptr vb0, lptr vb1, unsigned kvv, int wid TSPAR) {
  const unsigned lds0 = (unsigned)(uintptr_t)lds;
  const int b = __builtin_amdgcn_workgroup_id_x(), xcd = b & 7, idx = b >> 3;
  const int ng = (xcd & 3) * 4 + (idx & 3), mg = (xcd >> 2) * 8 + (idx >> 2);
  const srd_t wsr = SRD(ws, WS_END);
  const f32x16 Z = {};
  const int head = 2 * xcd + (idx & 1), qg = idx >> 1, odd = head & 1;
  const int idx3 = idx < 28 ? idx : idx - 28, mq3 = (idx3 * 37) >> 8, mg3 = xcd * 4 + mq3, ng3 = idx3 - mq3 * 7;
  LANE_IDS();
  const unsigned lw2k = lds0 + wid * 2048;
  auto dma_kv = [&](auto zc) AI { constexpr int z = decltype(zc)::v; dma<(z >> 1) * QT + (z & 1) * 1024, ((z >> 1) & 3) * 16384 + (z & 1) * 1024, true>(wsr, kvv, lw2k); };
  const int w3m = wid & 1, w3k = wid >> 1;
  u32x4 hf[28];
  const int ft = w3k & 1, fs = w3k >> 1;
  unsigned vo_d = 0; const unsigned ld_d = lds0 + wid * 4096;
  auto set_vo_d = [&]() AI { vo_d = (unsigned)(((idx < 28 ? w3k * 448 + w3m * 32 : OOB / 896) + rr) * 896 + ((cc ^ (grp << 2)) << 4) + ng3 * 128); PIN(vo_d); };
  const srd_t sD = SRD(wd, 1792 * 896);
  auto dma_d = [&](auto zc) AI { constexpr int z = decltype(zc)::v; dma<((z >> 2) * 64 + (z & 3) * 8) * 896, dslot(z >> 2) + (z & 3) * 1024>(sD, vo_d, ld_d); };
  int db0 = 0, db1 = 0, dbo = L_WD;
  auto set_db = [&]() AI { db0 = L_WD + w3k * 8192 + (4 * h + q) * 128 + p * 8 + ((grp ^ fs ^ (((q >> 1) ^ ft) << 1)) << 5); db1 = db0 ^ 64; PIN(db0); PIN(db1); };
  auto rebase = [&](int o) AI { db0 += o - dbo; db1 += o - dbo; dbo = o; PIN(db0); PIN(db1); };
  f32x16 F0 = Z, F1 = Z;
  u32x4 dw0, dw1;
  auto rd_d0 = [&](int d) AI { const int o = dslot(d >> 2) + (d & 3) * 2048 - dbo; dw0 = cat(trd(lds + db0 + o), trd(lds + db0 + o + 1024)); };
  auto rd_d1 = [&](int d) AI { const int o = dslot(d >> 2) + (d & 3) * 2048 - dbo; dw1 = cat(trd(lds + db1 + o), trd(lds + db1 + o + 1024)); };
  {
    const int wq = wid & 1, kq = wid >> 1;
    u32x4 v1, pa, pb;
    float e0, e1, e2, e3;
    f32x16 OA = Z, OB = Z, sb;
    unsigned junk, fh = ~nonce; const int fho = WS_FH + ((mg3 >> 1) * 16 + (lane & 15)) * 128;
    const int hld = WS_H + ((mg3 * 2 + w3m) * 112 + w3k * 28) * 1024, hlv = idx < 28 ? lane * 16 : OOB;
    auto send = [&](const f32x16& a, int hh, int j) AI {
      const lptr d = kb + (((wid ^ (2 * j)) * 6144 + (j - 1) * 2048) - kq * 4096);
      *(LAS f32x4*)d = (f32x4){a[8 * hh], a[8 * hh + 1], a[8 * hh + 2], a[8 * hh + 3]}; *(LAS f32x4*)(d + 1024) = (f32x4){a[8 * hh + 4], a[8 * hh + 5], a[8 * hh + 6], a[8 * hh + 7]}; };
    TS(10); TS(19);
#define EX2(S, i, a, c) do { a = EX(S[i]); c = EX(S[(i) + 1]); PIN(a); PIN(c); } while (0)
#define STEP(FIRST, SC, SN, QN0, QN1, OP, OC, L0, L1, L2, L3, X0, X1) do {                                                       \
      SBAR(); SN = MFMA(k0, QN0, Z); SBAR(); L0;                                                                          \
      if (!(FIRST)) { pb[2] = pk(e0, e1); pb[3] = pk(e2, e3); PIN(pb); } SBAR(); EX2(SC, 0, e0, e1); SBAR(); X0; SBAR(); EX2(SC, 2, e2, e3); \
      SBAR(); if (!(FIRST)) OP = MFMA(v1, pb, OP); SBAR(); L1;                                                                          \
      pa[0] = pk(e0, e1); pa[1] = pk(e2, e3); PIN(pa); SBAR(); EX2(SC, 4, e0, e1); EX2(SC, 6, e2, e3);                    \
      SBAR(); SN = MFMA(k1, QN1, SN); SBAR(); L2;                                                                         \
      pa[2] = pk(e0, e1); pa[3] = pk(e2, e3); PIN(pa); SBAR(); EX2(SC, 8, e0, e1); SBAR(); X1; SBAR(); EX2(SC, 10, e2, e3); \
      SBAR(); OC = MFMA(v0, pa, OC); SBAR(); L3;                                                                          \
      pb[0] = pk(e0, e1); pb[1] = pk(e2, e3); PIN(pb); SBAR(); EX2(SC, 12, e0, e1); EX2(SC, 14, e2, e3); SBAR(); } while (0)
    sfor<0, 16>([&](auto sc_) AI {
      constexpr int s = decltype(sc_)::v, so = (s & 3) * 16384, sn = ((s + 1) & 3) * 16384;
      auto side = [&](auto pc) AI {
        constexpr int P = decltype(pc)::v, kv = kv_at(s, P), wz = wd_at(s, P), hz = h_at(s, P);
        if constexpr (s == 0 && P == 3) set_vo_d();
        if constexpr (s == 0 && P == 5) set_db();
        if constexpr (kv >= 0) dma_kv(IC<kv>{});
        if constexpr (wz >= 0) dma_d(IC<wz>{});
        if constexpr (pf_at(s, P)) {
          const int row = wid < 2 ? mq3 * 448 + 320 + wid * 64 + lane : mq3 * 112 + (wid - 2) * 56 + (lane < 56 ? lane : 55);
          asm volatile("buffer_load_dword %0, %1, %2, 0 offen" : "=&v"(junk) : "v"(wid < 4 && idx < 28 ? row * 896 + ng3 * 128 : OOB), "s"(SRD(wid < 2 ? wd : wo, wid < 2 ? 1792 * 896 : 448 * 896)) : "memory");
        }
        if constexpr (hz >= 0) hf[hz] = __builtin_amdgcn_raw_buffer_load_b128(wsr, hlv + (hz & 3) * 1024, hld + (hz >> 2) * 4096, SC1);
      };
      auto mid = [&]() AI {
        if (s == 4 && wid == 0) {
          asm volatile("s_waitcnt vmcnt(%1)" : "+v"(fh) : "n"(cnt_after(3, 6, 4)) : "memory");
          _Pragma("nounroll") for (unsigned sp = 0; !ALL(fh == nonce) && sp < SPIN_CAP; ++sp) { fh = flag_ld(ws, fho); PIN(fh); }
        }
        if constexpr (s == 0) vwait_bar<5 + cnt_after(0, -1, 0)>();
        else if constexpr (s == 1) vwait_bar<cnt_after(0, -1, 1)>();
        else if constexpr (s == 14) vwait_bar<0>(); else if constexpr (s < 14) vwait_bar<cnt_after(s - 1, 0, s)>();
        if (s == 1 && wid == 0) flag_st(ws, WS_FH + (mg * 16 + ng) * 128, nonce);
        if (s == 3 && wid == 0) asm volatile("buffer_load_dword %0, %1, %2, 0 offen sc1" : "+v"(fh) : "v"(fho), "s"(wsr) : "memory");
        if constexpr (s == 6) asm volatile("" :: "v"(junk));
        if constexpr (s == 13) rebase(L_WD + 65536);
        if constexpr (s == 14) { UNROLL for (int k = 23; k >= 8; --k) PIN(hf[k]); }
        if (s == 1) TS(20); if (s == 3) TS(21); if (s == 5) TS(22); if (s == 7) TS(23); if (s == 9) TS(24); if (s == 11) TS(25); if (s == 13) TS(26);
        __builtin_amdgcn_s_setprio(3);
      };
      auto dn0 = [&](int d) AI { if (d >= 0) { F0 = MFMA(dw0, hf[d], F0); PIN(F0); } if (d >= -1) rd_d0(d + 1); };
      auto dn1 = [&](int d) AI { if (d >= 0) { F1 = MFMA(dw1, hf[d], F1); PIN(F1); } if (d >= -1) rd_d1(d + 1); };
      STEP(s == 0, sa, sb, QB0, QB1, OB, OA,
           k0 = ld128(kb + sn); side(IC<0>{}); __builtin_amdgcn_s_setprio(2),
           v1 = cat(trd(vb0 + so + 256), trd(vb1 + so + 256)); side(IC<1>{}),
           k1 = ld128(kb + sn + 1024); side(IC<2>{}); __builtin_amdgcn_s_setprio(1),
           side(IC<3>{}), dn0(s - DN0), if (s == 15) dn1(8));
      STEP(false, sb, sa, QA0, QA1, OA, OB,
           __builtin_amdgcn_s_setprio(0); side(IC<4>{}),
           side(IC<5>{}),
           mid(); if (s == 15) send(OA, 0, 1),
           v0 = cat(trd(vb0 + sn), trd(vb1 + sn)); side(IC<7>{}); if (s == 15) send(OA, 1, 3), if (s < 15) dn1(s - DN0); else dn0(9), if (s == 15) dn1(9));
    });
    pb[2] = pk(e0, e1); pb[3] = pk(e2, e3);
    OB = MFMA(v1, pb, OB);
    __builtin_amdgcn_s_setprio(0);
    TS(11);
    send(OB, 1, 2);
    *(LAS float*)(kb + (49152 + wq * 1024)) = odd ? OB[0] : OB[4];
    {
    LANE_IDS();
    const bool padl = h != odd;
    const int qt = qg * 4 + wq * 2 + ft, lw = (wid & 3) | (odd ? 0 : 4);
    const lptr pp = lds + wid * 6144 + lane * 16, lp = lds + lw * 6144 + (odd ? 0 : 1536) + r * 16;
    const lptr lq = lds + 49152 + (lw >> 1) * 4096 + wq * 1024 + (odd ? 0 : 512) + r * 16;
    f32x4 o0 = {OB[0], OB[1], OB[2], OB[3]}, o1 = {OB[4], OB[5], OB[6], OB[7]};
    lbar();
    const float l0 = *(LAS float*)lq, l1 = *(LAS float*)lp, l2 = *(LAS float*)(lp + 2048), l3 = *(LAS float*)(lp + 4096);
    f32x4 t[6];
    UNROLL for (int k = 0; k < 6; ++k) t[k] = *(LAS f32x4*)(pp + k * 1024);
    SBAR();
    float l = (l0 + l1) + (l2 + l3);
    UNROLL for (int k = 0; k < 3; ++k) { o0 += t[2 * k]; o1 += t[2 * k + 1]; }
    if (__builtin_expect(ANY(!(l <= 1.8446744e19f && l >= 5.4210109e-20f)), 0)) {
      const int qo = WS_QKV + head * QH + qt * QT + lane * 16;
      u32x4 q0 = __builtin_amdgcn_raw_buffer_load_b128(wsr, qo, 0, SC1), q1 = __builtin_amdgcn_raw_buffer_load_b128(wsr, qo + 1024, 0, SC1);
      f32x16 O = Z; float m = -__builtin_inff();
      _Pragma("nounroll") for (int t = 0; t < 64; ++t) {
        const int ko = WS_QKV + head * QH + t * QT + 2048;
        f32x16 s = MFMA(__builtin_amdgcn_raw_buffer_load_b128(wsr, ko + lane * 16, 0, SC1), q0, Z);
        s = MFMA(__builtin_amdgcn_raw_buffer_load_b128(wsr, ko + 1024 + lane * 16, 0, SC1), q1, s);
        float mn = m;
        UNROLL for (int i = 0; i < 16; ++i) mn = __builtin_fmaxf(mn, s[i]);
        mn = max32(mn);
        const float f = EX(m - mn); m = mn;
        UNROLL for (int i = 0; i < 16; ++i) O[i] *= f;
        UNROLL for (int st = 0; st < 2; ++st) {
          unsigned vv[8];
          UNROLL for (int j = 0; j < 8; ++j) vv[j] = __builtin_amdgcn_raw_buffer_load_b16(wsr, ko + 2048 + (r >> 3) * 512 + (((16 * st + 8 * (j >> 2) + 4 * h + (j & 3)) ^ (4 * (r >> 3))) << 4) + (r & 7) * 2, 0, SC1) & 0xffffu;
          O = MFMA(((u32x4){vv[0] | vv[1] << 16, vv[2] | vv[3] << 16, vv[4] | vv[5] << 16, vv[6] | vv[7] << 16}),
                   ((u32x4){pk(EX(s[8 * st] - m), EX(s[8 * st + 1] - m)), pk(EX(s[8 * st + 2] - m), EX(s[8 * st + 3] - m)),
                            pk(EX(s[8 * st + 4] - m), EX(s[8 * st + 5] - m)), pk(EX(s[8 * st + 6] - m), EX(s[8 * st + 7] - m))}), O);
        }
      }
      const float lc = odd ? O[0] : O[12], lx = swap32(lc, h); l = padl ? lc : lx;
      o0 = fs ? (f32x4){O[8], O[9], O[10], O[11]} : (f32x4){O[0], O[1], O[2], O[3]};
      o1 = fs ? (f32x4){O[12], O[13], O[14], O[15]} : (f32x4){O[4], O[5], O[6], O[7]};
    }
    const float ri = __builtin_amdgcn_rcpf(l);
    const float r0 = padl && odd && fs == 0 ? 0.f : ri, r1 = padl && !odd && fs == 1 ? 0.f : ri;
    st128((u32x4){pk(o0[0] * r0, o0[1] * r0), pk(o0[2] * r0, o0[3] * r0), pk(o1[0] * r1, o1[1] * r1), pk(o1[2] * r1, o1[3] * r1)},
                                           wsr, lane * 16, WS_O + (qt * 16 + head) * 2048 + fs * 1024, SC1);
    }
  }
  TS(12);
  if (idx >= 28) { wait_bar<0>();
    if (wid == 0) {
      flag_st(ws, WS_FO + (qg * 16 + head) * 128, nonce);
      if (b == 255) {
        LANE_IDS();
        _Pragma("nounroll") for (unsigned sp = 0; sp < SPIN_CAP; ++sp) {
          unsigned d = 0;
          UNROLL for (int i = 0; i < 4; ++i) d |= flag_ld(ws, WS_FQ + lane * 512 + i * 128) ^ nonce;
          if (ALL(d == 0)) break;
        }
        flag_st(ws, WS_E, nonce);
      }
    }
    return;
  }
  {
    lbar();
    sfor<20, 24>(dma_d);
    u32x4 ew0, ew1;
    auto down2 = [&](int d, bool more) AI {
      const int o = dslot((d + 1) >> 2) + ((d + 1) & 3) * 2048 - dbo;
      ew0 = cat(trd(lds + db0 + o), trd(lds + db0 + o + 1024)); ew1 = cat(trd(lds + db1 + o), trd(lds + db1 + o + 1024));
      if (d == 10) rebase(L_WD);
      SBAR(); F0 = MFMA(dw0, hf[d], F0); F1 = MFMA(dw1, hf[d], F1); PIN(F0); PIN(F1); SBAR();
      if (more) { rd_d0(d + 2); rd_d1(d + 2); }
      SBAR(); F0 = MFMA(ew0, hf[d + 1], F0); F1 = MFMA(ew1, hf[d + 1], F1); PIN(F0); PIN(F1); SBAR();
    };
    static_assert(DN0 == 7, "units 0..9 ran beside the attention");
    down2(10, true); sfor<24, 26>(dma_d); down2(12, true); sfor<26, 28>(dma_d);
    vwait_bar<8>();
    if (wid == 0) flag_st(ws, WS_FO + (qg * 16 + head) * 128, nonce);
    TS(13);
    LANE_IDS();
    const int mt = mg3 * 2 + w3m;
    const unsigned xo = (unsigned)((mt * 32 + r) * 896 + ng3 * 128 + ft * 64 + fs * 32);
    unsigned fo = ~nonce; const int foo = WS_FO + ((mg3 >> 1) * 16 + (lane & 15)) * 128;
    down2(14, true); down2(16, true); down2(18, false);
    UNROLL for (int k = 27; k >= 24; --k) PIN(hf[k]);
    wait_bar<0>();
    if (wid == 0) asm volatile("buffer_load_dword %0, %1, %2, 0 offen sc1" : "+v"(fo) : "v"(foo), "s"(wsr) : "memory");
    rebase(0); rd_d0(20); rd_d1(20);
    const srd_t sO = SRD(wo, 448 * 896); const unsigned wov = vo_d + (wid * 56 - w3k * 448 - w3m * 32) * 896, wol = lds0 + L_WD + wid * 7168;
    auto dma_o = [&](auto ic) AI { constexpr int i = decltype(ic)::v; dma<i * 8 * 896, i * 1024>(sO, wov, wol); };
    sfor<0, 4>(dma_o); down2(20, true); sfor<4, 7>(dma_o); down2(22, true);
    TS(14);
    if (wid == 0) {
      asm volatile("s_waitcnt vmcnt(7)" : "+v"(fo) :: "memory");
      _Pragma("nounroll") for (unsigned sp = 0; !ALL(fo == nonce) && sp < SPIN_CAP; ++sp) { fo = flag_ld(ws, foo); PIN(fo); }
    }
    wait_bar<0>();
    TS(15);
    u32x4 of[8];
    const int orot = ng3 & 1, oso = WS_O + (mt * 16 + w3k * 4) * 2048 + orot * 4096;
    UNROLL for (int k = 0; k < 4; ++k) of[k] = __builtin_amdgcn_raw_buffer_load_b128(wsr, lane * 16 + k * 1024, oso, SC1);
    SBAR();
    down2(24, true); down2(26, false);
    auto xch_addr = [&](int w, int lo, int hi) AI { return lds + ((w < 5 ? lo : hi - 5 * 6144) + w * 6144) + lane * 16; };
    auto xch_send = [&](const f32x16& a0, const f32x16& a1, int lo, int hi) AI {
      UNROLL for (int j = 1; j < 4; ++j) { const f32x16& a = (j & 1) ? a1 : a0; const int o = (j >> 1) * 8; const lptr d = xch_addr(wid ^ (2 * j), lo, hi) + (j - 1) * 2048;
        *(LAS f32x4*)d = (f32x4){a[o], a[o + 1], a[o + 2], a[o + 3]}; *(LAS f32x4*)(d + 1024) = (f32x4){a[o + 4], a[o + 5], a[o + 6], a[o + 7]}; } };
    u32x4 wo0[8], wo1[8];
    UNROLL for (int k = 0; k < 8; ++k) {
      const int wbo = L_WD + w3k * (PART - 8192) + ((k >> 2) ^ orot) * 7168, o = (((k >> 1) & 1) * 24 + (k & 1) * 16) * 128;
      const lptr wb0 = lds + db0 + wbo, wb1 = lds + db1 + wbo;
      wo0[k] = cat(trd(wb0 + o), trd(wb0 + o + 1024)); wo1[k] = cat(trd(wb1 + o), trd(wb1 + o + 1024));
    }
    xch_send(F0, F1, 0, 122880);
    SBAR();
    UNROLL for (int k = 4; k < 8; ++k) of[k] = __builtin_amdgcn_raw_buffer_load_b128(wsr, lane * 16 + (k & 3) * 1024, oso ^ 4096, SC1);
    const u32x2 x0 = *(const u32x2*)(x + xo + h * 8), x1 = *(const u32x2*)(x + xo + 16 + h * 8);
    SBAR();
    f32x16 S0 = Z, S1 = Z;
    UNROLL for (int k = 0; k < 4; ++k) { S1 = MFMA(wo1[k], of[k], S1); S0 = MFMA(wo0[k], of[k], S0); }
    PIN(S0); PIN(S1);
    lbar();
    f32x4 g0 = {F0[0], F0[1], F0[2], F0[3]}, g1 = {F0[4], F0[5], F0[6], F0[7]};
    { const lptr pp = xch_addr(wid, 0, 122880); UNROLL for (int k = 0; k < 3; ++k) { g0 += *(LAS f32x4*)(pp + k * 2048); g1 += *(LAS f32x4*)(pp + k * 2048 + 1024); } }
    UNROLL for (int k = 4; k < 8; ++k) { S1 = MFMA(wo1[k], of[k], S1); S0 = MFMA(wo0[k], of[k], S0); }
    UNROLL for (int i = 0; i < 4; ++i) {
      g0[i] = g0[i] * __builtin_amdgcn_rcpf(1.f + EX(g0[i] * -1.4426950408889634f)); g1[i] = g1[i] * __builtin_amdgcn_rcpf(1.f + EX(g1[i] * -1.4426950408889634f)); }
    TS(16);
    xch_send(S0, S1, 32768, 141312);
    f32x4 y0 = {U2F(x0[0] << 16), U2F(x0[0] & 0xffff0000u), U2F(x0[1] << 16), U2F(x0[1] & 0xffff0000u)}, y1 = {U2F(x1[0] << 16), U2F(x1[0] & 0xffff0000u), U2F(x1[1] << 16), U2F(x1[1] & 0xffff0000u)};
    UNROLL for (int i = 0; i < 4; ++i) { y0[i] = __builtin_fmaf(S0[i], g0[i], y0[i]); y1[i] = __builtin_fmaf(S0[4 + i], g1[i], y1[i]); }
    unsigned oo = xo + h * 16; lptr pp = xch_addr(wid, 32768, 141312);
    PIN(g0); PIN(g1); PIN(y0); PIN(y1); PIN(oo); PIN(pp);
    lbar();
    f32x4 t[6];
    UNROLL for (int k = 0; k < 6; ++k) t[k] = *(LAS f32x4*)(pp + k * 1024);
    SBAR();
    UNROLL for (int k = 0; k < 3; ++k) UNROLL for (int i = 0; i < 4; ++i) { y0[i] = __builtin_fmaf(t[2 * k][i], g0[i], y0[i]); y1[i] = __builtin_fmaf(t[2 * k + 1][i], g1[i], y1[i]); }
    *(u32x4*)(out + oo) = widen((u32x2){pk(y0[0], y0[1]), pk(y0[2], y0[3])}, (u32x2){pk(y1[0], y1[1]), pk(y1[2], y1[3])});
    TS(17); TS_OUT();
  }
}

#if defined(__HIP_DEVICE_COMPILE__)
#define NOPK __attribute__((target("no-packed-fp32-ops")))
#else
#define NOPK
#endif
__global__ __launch_bounds__(512, 2) NOPK void fused_kernel(const char* x, const char* wqkv, const char* wo, const char* wg, const char* wu, const char* wd,
                                                       char* ws, char* out) {
  extern __shared__ __attribute__((aligned(16))) char smem[];
  const lptr lds = (lptr)smem;
  const unsigned lds0 = (unsigned)(uintptr_t)lds;
  const int tid = __builtin_amdgcn_workitem_id_x(), lane = tid & 63, wid = __builtin_amdgcn_readfirstlane(tid >> 6);
  const int rr = lane >> 3, cc = lane & 7;
  const int b = __builtin_amdgcn_workgroup_id_x(), xcd = b & 7, idx = b >> 3;
  const int ng = (xcd & 3) * 4 + (idx & 3), mg = (xcd >> 2) * 8 + (idx >> 2);
  unsigned tsv = 0; TSR(62); TS(0);
  const int rot = 2 * ((idx ^ (idx >> 2)) & ROTM);
  unsigned kc[7];
  UNROLL for (int c = 0; c < 7; ++c) kc[c] = (unsigned)((c + rot >= 7 ? c + rot - 7 : c + rot) * 64);
  const srd_t xr = SRD(x, 2048 * 896);
  const unsigned xv0 = (unsigned)((mg * 128 + wid * 16 + rr) * 896 + ((cc ^ rr) << 4));
  const unsigned xv1 = (unsigned)((mg * 128 + wid * 16 + 8 + (rr ^ 1)) * 896 + ((cc ^ rr ^ 1) << 4));
  const unsigned lw2k = lds0 + wid * 2048;
  auto dma_x = [&](auto cc_) AI { constexpr int c = decltype(cc_)::v; dma2<(c & 3) * 16384>(xr, xv0, xv1, lw2k, kc[c], 2); };
  const srd_t wsr = SRD(ws, WS_END);
  u32x4 srow; unsigned nonce;
  const int srw = mg * 128 + ng * 8 + wid;
  asm volatile("buffer_load_dwordx4 %0, %1, %2, %3 offen" : "=&v"(srow) : "v"(lane * 16), "s"(SRD(x, srw * 896 + 896)), "s"(srw * 896) : "memory");
  dma_x(IC<0>{});
  SBAR();
  const int r = lane & 31, h = lane >> 5, q = (lane & 15) >> 2, p = lane & 3, grp = (lane >> 4) & 1;
  const f32x16 Z = {};
  const int head = 2 * xcd + (idx & 1), qg = idx >> 1;
  auto dma_kv = [&](auto zc, unsigned kvv) AI { constexpr int z = decltype(zc)::v; dma<(z >> 1) * QT + (z & 1) * 1024, ((z >> 1) & 3) * 16384 + (z & 1) * 1024, true>(wsr, kvv, lw2k); };
  u32x4 QA0, QA1, QB0, QB1, kf0, kf1, ka0, ka1, va0; f32x16 s0; lptr kb, vb0, vb1; unsigned kvv = 0;
  {
    const int wm = wid & 3, wn = wid >> 2;
    const bool bq = ng <= 10;
    const unsigned st1 = bq ? 2688 : 3584;
    unsigned va = (16 * wm + rr) * st1 + (((cc << 4) ^ (grp << (bq ? 6 : 5))) ^ (bq ? 0 : wn << 6)) + (bq ? 2 * ng + wn : ng + 12) * 128, vb = va + 8 * st1;
    const char* wgu = wn ? wu : wg;
    const srd_t s2 = SRD(wgu, 448 * 3584), s1 = SRD(bq ? wqkv : wgu, 448 * st1);
    auto dma_w = [&](auto mc) AI {
      constexpr int m = decltype(mc)::v;
      if constexpr (m == 7) { va = (unsigned)((16 * wm + rr) * 3584 + ((cc ^ (grp << 1) ^ (wn << 2)) << 4)) + ng * 128; vb = va + 8 * 3584; PIN(va); PIN(vb); }
      if constexpr (m == 14) {
        va = (unsigned)((16 * wm + (lane >> 2)) * 3584 + (((lane & 3) ^ (wn << 1)) << 4)) + 2048 + ng * 64;
        if (ng >= 14) va = OOB;
        PIN(va);
      }
      if constexpr (m < 7) dma2<wpos(m)>(s1, va, vb, lw2k, kc[m], st1);
      else if constexpr (m < 14) dma2<wpos(m)>(s2, va, vb, lw2k, kc[m - 7], 3584);
      else dma1<wpos(m)>(s2, va, lds0 + wid * 1024, kc[m - 14], 3584);
    };
    dma_w(IC<0>{});
    unsigned epoch;
    asm volatile("buffer_load_dword %0, off, %1, 0 sc1" : "=&v"(epoch) : "s"(wsr) : "memory");
    dma_x(IC<1>{});
    {
      unsigned long long pc; asm volatile("s_getpc_b64 %0" : "=s"(pc));
      if (wid == 7) touch(SRD(pc & ~127ull, 28672), lane < 8 ? (idx * 8 + lane) * 128 : OOB, lds0 + L_PF);
    }
    SBAR();
    const int rowb = (8 * h + q) * 128 + p * 8;
    int b20 = grp * 8192 + rowb + (((2 * wn) ^ (q >> 1) ^ (grp << 1)) << 5);
    int b10 = bq ? wn * 8192 + rowb + ((grp ^ ((q >> 1) << 1)) << 5) : b20, b11 = b10 ^ (bq ? 64 : 32);
    int xb[4];
    UNROLL for (int i = 0; i < 4; ++i) { xb[i] = (4 * wm + (r >> 3)) * 1024 + ((r & 7) ^ ((r >> 3) & 1)) * 128 + (((2 * i + h) ^ (r & 7)) << 4); PIN(xb[i]); }
    PIN(b10); PIN(b11); PIN(b20);
    UNROLL for (int c = 2; c < 7; ++c) asm volatile("" : "+s"(kc[c]));
    SBAR();
    u32x4 xf[28];
    auto row_stats = [&]() AI {
      float a = dot2(srow[0], ONES2, 0.f), c = dot2(srow[0], srow[0], 0.f);
      UNROLL for (int i = 1; i < 4; ++i) { a = dot2(srow[i], ONES2, a); c = dot2(srow[i], srow[i], c); }
      a = wave_sum(a); c = wave_sum(c);
      const float mean = a * (1.f / 448.f), var = __builtin_fmaxf(c * (1.f / 448.f) - mean * mean, 0.f);
      unsigned g = pk(-mean, __builtin_amdgcn_sqrtf(rbf(rbf(var) + 1e-5f))); PIN(g); SBAR();
      dma_w(IC<1>{});
      asm volatile("s_waitcnt vmcnt(4)" : "+v"(epoch) :: "memory");
      nonce = __builtin_amdgcn_readfirstlane(epoch) * 0x9E3779B1u + 0x7F4A7C15u;
      if (lane == 63) __hip_atomic_store((GAS unsigned long long*)(ws + WS_ST + srw * 8), (unsigned long long)nonce << 32 | g, RLX_AGENT);
    };
    auto rd_x = [&](int k0, int k1) AI { UNROLL for (int ks = k0; ks < k1; ++ks) xf[ks] = ld128(lds + xb[ks & 3] + ((ks >> 2) & 3) * 16384); };
    u32x2 wf[NWF][4];
    int ra0 = 0, ra1 = 0;
    auto rd_w = [&](int n) AI {
      if (n < 56) {
        const int m = n >> 2, o = (n & 3) * 2048;
        if ((n & 3) == 0) { ra0 = (m < 7 ? b10 : b20) + wpos(m); ra1 = (m < 7 ? b11 : b20 ^ 32) + wpos(m); PIN(ra0); PIN(ra1); }
        wf[n % NWF][0] = trd(lds + ra0 + o); wf[n % NWF][1] = trd(lds + ra0 + o + 512); wf[n % NWF][2] = trd(lds + ra1 + o); wf[n % NWF][3] = trd(lds + ra1 + o + 512);
      } else {
        const int o = ((n - 56) >> 1) * 8192 + (n & 1) * 2048;
        if (n == 56) { ra0 = wpos(14) + grp * 4096 + (8 * h + q) * 64 + ((wn ^ grp) << 5) + p * 8; PIN(ra0); }
        wf[n % NWF][0] = trd(lds + ra0 + o); wf[n % NWF][1] = trd(lds + ra0 + o + 256); wf[n % NWF][2] = trd(lds + ra0 + o + 1024); wf[n % NWF][3] = trd(lds + ra0 + o + 1280);
      }
    };
    SBAR();
    asm volatile("s_waitcnt vmcnt(7)" : "+v"(srow) :: "memory");
    row_stats();
    SBAR();
    lbar();
    TS(1);
    UNROLL for (int n = 0; n < RA; ++n) rd_w(n);
    rd_x(0, 4);
    dma_x(IC<2>{});
    f32x16 A0 = Z, A1 = Z, B0 = Z, B1 = Z, C0 = Z;
    float cs0 = 0.f, cs1 = 0.f, rstd = 0.f, sig = 0.f, kg = 0.f; unsigned mm = 0; u32x2 sg = {0u, ~nonce};
    const int mt = mg * 4 + wm;
    const unsigned csel = ((wm & 1) ? 0x05040c0cu : 0x01000c0cu) + ((wm >> 1) ? 0x02020000u : 0u);
    auto colsum = [&](int n, int hh) AI {
      cs0 += U2F(__builtin_amdgcn_perm(wf[n % NWF][2 + hh][0], wf[n % NWF][hh][0], csel)); cs1 += U2F(__builtin_amdgcn_perm(wf[n % NWF][2 + hh][1], wf[n % NWF][hh][1], csel));
      PIN(cs0); PIN(cs1);
    };
    auto cs_out = [&]() AI {
      const float cs = cs0 + cs1, hi = rbf(cs); *(LAS unsigned*)(lds + L_CS + wn * 1024 + lane * 16 + ((wm & 1) * 2 + (wm >> 1)) * 4) = pk(hi, cs - hi); cs0 = 0.f; cs1 = 0.f; };
    auto fix2 = [&](f32x16& a0, f32x16& a1) AI {
      const u32x4 c = ld128(lds + L_CS + wn * 1024 + lane * 16);
      a0 = MFMA(c, ((u32x4){mm, mm, 0u, 0u}), a0); a1 = MFMA(c, ((u32x4){0u, 0u, mm, mm}), a1); PIN(a0); PIN(a1); };
    auto glu = [&](const f32x16& acc, int i) AI { return acc[i + 8] * __builtin_amdgcn_rcpf(__builtin_fmaf(EX(acc[i] * kg), sig, sig)); };
    auto glu8 = [&](const f32x16& acc) AI { return (u32x4){pk(glu(acc, 0), glu(acc, 1)), pk(glu(acc, 2), glu(acc, 3)), pk(glu(acc, 4), glu(acc, 5)), pk(glu(acc, 6), glu(acc, 7))}; };
    const int hs2 = WS_H + (mt * 112 + ng * 4 + wn * 2) * 1024;
    u32x4 tb0 = {0u, 0u, 0u, 0u}, tb1 = tb0;
    auto epi1 = [&](const f32x16& acc, int t) AI {
      if (bq) {
        const u32x4 e = t ? tb1 : tb0; const unsigned o[3] = {e[0] ^ (r << 4), e[1] ^ (r << 4), e[2] ^ (r << 4)};
        const unsigned m[2] = {(unsigned)__builtin_amdgcn_sbfe(e[3], 0, 1), (unsigned)__builtin_amdgcn_sbfe(e[3], 1, 1)};
        const float sc = 2 * ng + wn < 7 ? rstd * (0.18898223650461362f * 1.4426950408889634f) : rstd;
        u32x4 v[2];
        UNROLL for (int pp = 0; pp < 2; ++pp)
          v[pp] = widen((u32x2){pk(acc[8 * pp] * sc, acc[8 * pp + 1] * sc), pk(acc[8 * pp + 2] * sc, acc[8 * pp + 3] * sc)},
                        (u32x2){pk(acc[8 * pp + 4] * sc, acc[8 * pp + 5] * sc), pk(acc[8 * pp + 6] * sc, acc[8 * pp + 7] * sc)});
        u32x4 d0 = {v[0][0], v[0][1], bfi(m[0], PAD0, v[0][2]), ~m[0] & v[0][3]}, d1 = {v[1][0], v[1][1], bfi(m[1], PAD0, v[1][2]), ~m[1] & v[1][3]};
        u32x4 d2 = {PAD0, 0u, bfi(m[0], v[0][2], v[1][2]), bfi(m[0], v[0][3], v[1][3])};
        PIN(d0); PIN(d1); PIN(d2); SBAR();
        __builtin_amdgcn_raw_buffer_store_b128(d0, wsr, o[0], mt * QT, SC1); __builtin_amdgcn_raw_buffer_store_b128(d1, wsr, o[1], mt * QT, SC1);
        st128(d2, wsr, o[2], mt * QT, SC1);
      } else st128(glu8(acc), wsr, lane * 16 + t * 1024, hs2 + 12 * 4096, SC1);
    };
    float hv[8]; u32x4 hp, hq; unsigned fl0 = ~nonce; f32x16 sa, sb; u32x4 cfix;
    int fqo = 0;
    auto fq_off = [&]() AI {
      const int c0 = (lane < 8 ? 448 + (wid & 1) * 448 : 0) + head * 28 - (head & 1) * 4 + (lane & 1) * 31, g = c0 >> 7, m2 = lane < 8 ? (wid >> 1) * 4 + (lane >> 1) : qg;
      fqo = WS_FQ + (((m2 & 7) * 4 + (g & 3)) * 8 + (m2 >> 3) * 4 + (g >> 2)) * 128; };
    float ge[16];
    auto glu_step = [&](auto zc) AI {
      constexpr int z = decltype(zc)::v, t = z - z / 3, on = z % 3 != 2;
      if constexpr (on && t >= 2 && t < 18) { constexpr int e = t - 2; hv[e & 7] = (e < 8 ? B0 : B1)[(e & 7) + 8] * ge[e]; PIN(hv[e & 7]);
        if constexpr ((e & 7) == 7) { (e < 8 ? hp : hq) = (u32x4){pk(hv[0], hv[1]), pk(hv[2], hv[3]), pk(hv[4], hv[5]), pk(hv[6], hv[7])}; PIN(e < 8 ? hp : hq); } }
      if constexpr (on && t >= 1 && t < 17) { constexpr int e = t - 1; ge[e] = __builtin_amdgcn_rcpf(__builtin_fmaf(ge[e], sig, sig)); PIN(ge[e]); }
      if constexpr (on && t < 16) { ge[t] = EX((t < 8 ? B0 : B1)[t & 7] * kg); PIN(ge[t]); }
      if constexpr (z == 27) { st128(hp, wsr, lane * 16, hs2, SC1); st128(hq, wsr, lane * 16 + 1024, hs2, SC1); }
    };
    const int sgo = WS_ST + (mg * 128 + wm * 32 + r) * 8;

    sfor<0, 70>([&](auto nc) AI {
      constexpr int n = decltype(nc)::v, u = n & 7;
      constexpr int k0 = n < 28 ? n : n < 56 ? n - 28 : 2 * (n - 56), k1 = n < 56 ? k0 : k0 + 1;
      f32x16& D0 = n < 28 ? A0 : n < 56 ? B0 : C0; f32x16& D1 = n < 28 ? A1 : n < 56 ? B1 : C0;
      constexpr bool early = n == 54 || n == 55 || n >= 68;
      PIN(wf[n % NWF][3]);
      if (u == 7) __builtin_amdgcn_s_setprio(3); if (u == 1) __builtin_amdgcn_s_setprio(2); if (u == 3) __builtin_amdgcn_s_setprio(1); if (u == 5) __builtin_amdgcn_s_setprio(0);
      SBAR();
      D0 = MFMA(cat(wf[n % NWF][0], wf[n % NWF][1]), xf[k0], D0); PIN(D0);
      SBAR();
      if constexpr (u == 6 || n == 68 || n == 2) {
        if constexpr (n == 54 || n == 68) { UNROLL for (int e = 0; e < RA; ++e) { colsum(n + e, 0); colsum(n + e, 1); } cs_out(); }
        if (n == 2) wait_bar<7>(); else if (n <= 14) wait_bar<2>(); else if (n == 22) wait_bar<4>(); else if (n == 30) wait_bar<6>();
        else if (n == 38) { if (bq) wait_bar<10>(); else wait_bar<6>(); } else if (n == 46) wait_bar<4>(); else if (n == 54) wait_bar<5>();
        else if (n == 62) lbar(); else wait_bar<4>();
        if (n == 46 && wid == 0) flag_st(ws, WS_FQ + b * 128, nonce);
        if (n == 6) TS(2); if (n == 14) TS(3); if (n == 30) TS(4); if (n == 46) TS(5); if (n == 62) TS(6); if (n == 68) TS(7);
        SBAR();
      }
      constexpr bool d1f = u == 6 || n == 68 || n == 2;
      if constexpr (d1f) { D1 = MFMA(cat(wf[n % NWF][2], wf[n % NWF][3]), xf[k1], D1); PIN(D1); SBAR(); }
      if constexpr (n + RA < 70) rd_w(n + RA);
      if constexpr (n == 68) {
        const lptr qb = lds + L_Q + (wid & 1) * 4096 + ((wid >> 1) & 1) * 2048 + lane * 16, qa = lds + L_Q + (wid & 1) * 4096 + (((wid >> 1) & 1) ^ 1) * 2048 + lane * 16;
        kf0 = ld128(lds + lane * 16); QA0 = ld128(qa); QB0 = ld128(qb); kf1 = ld128(lds + 1024 + lane * 16); QA1 = ld128(qa + 1024); QB1 = ld128(qb + 1024);
        cfix = ld128(lds + L_CS + wn * 1024 + lane * 16);
      }
      if constexpr (n == 69) { sa = MFMA(kf1, QA1, sa); sb = MFMA(kf1, QB1, sb); PIN(sa); PIN(sb); }
      if constexpr (n == 2) rd_x(4, 8);
      if constexpr (u == 6 && n < 24) rd_x(n + 2, n + 6);
      if constexpr (u == 0 && n > 0 && n < 24) rd_x(n + 4, n + 8);
      if constexpr (!early) colsum(n, 0);
      if constexpr (n >= 56) glu_step(IC<2 * (n - 56)>{});
      SBAR();
      if constexpr (!d1f) { D1 = MFMA(cat(wf[n % NWF][2], wf[n % NWF][3]), xf[k1], D1); PIN(D1); }
      SBAR();
      if constexpr (!early) colsum(n, 1);
      if constexpr (n == 0) dma_w(IC<2>{}); if constexpr (n == 1) dma_x(IC<3>{}); if constexpr (n == 2) dma_w(IC<3>{}); if constexpr (n == 4) dma_w(IC<4>{});
      if constexpr (n == 6) dma_x(IC<4>{}); if constexpr (n == 7) dma_x(IC<5>{}); if constexpr (n == 8) dma_w(IC<5>{}); if constexpr (n == 10) dma_w(IC<6>{});
      if constexpr (n == 14) dma_x(IC<6>{}); if constexpr (n == 15) dma_w(IC<7>{}); if constexpr (n == 18) dma_w(IC<8>{}); if constexpr (n == 20) dma_w(IC<9>{});
      if constexpr (n == 23) dma_w(IC<10>{}); if constexpr (n == 26) dma_w(IC<11>{}); if constexpr (n == 29) dma_w(IC<12>{}); if constexpr (n == 32) dma_w(IC<13>{});
      if constexpr (n == 48) touch(SRD(b < 196 ? wd : wo, b < 196 ? 1792 * 896 : 448 * 896), lane < 8 ? (((b < 196 ? b : b - 196) * 8 + wid) * 8 + lane) * 128 : OOB, lds0 + L_PF + wid * 256);
      if constexpr (n == 17) asm volatile("buffer_load_dwordx2 %0, %1, %2, 0 offen sc1" : "+v"(sg) : "v"(sgo), "s"(wsr) : "memory");
      if constexpr (n == 16) asm volatile("global_load_dwordx4 %0, %2, %3\n\tglobal_load_dwordx4 %1, %2, %3 offset:32" : "+v"(tb0), "+v"(tb1) : "v"(h * 16), "s"(&qkv_tab.v[bq ? 4 * ng + 2 * wn : 0]) : "memory");
      if constexpr (n >= 39 && n < 53 && (n & 1)) dma_w(IC<14 + (n - 39) / 2>{});
      if constexpr (n == 27) cs_out();
      if constexpr (n == 23) {
        PIN(sg); PIN(tb0); PIN(tb1);
        _Pragma("nounroll") for (unsigned sp = 0; !ALL(sg[1] == nonce) && sp < SPIN_CAP; ++sp)
          asm volatile("buffer_load_dwordx2 %0, %1, %2, 0 offen sc1\n\ts_waitcnt vmcnt(0)" : "+v"(sg) : "v"(sgo), "s"(wsr) : "memory");
        sig = U2F(sg[0] & 0xffff0000u); rstd = __builtin_amdgcn_rcpf(sig); mm = __builtin_amdgcn_perm(sg[0], sg[0], 0x01000100u); kg = -1.4426950408889634f * rstd;
        PIN(sig); PIN(rstd); PIN(kg); PIN(mm);
      }
      if constexpr (n == 30) fix2(A0, A1);
      if ((n == 31 && wn == 0) || (n == 33 && wn == 1)) epi1(A0, 0);
      if ((n == 35 && wn == 0) || (n == 37 && wn == 1)) epi1(A1, 1);
      if constexpr (n == FQP) { fq_off(); asm volatile("buffer_load_dword %0, %1, %2, 0 offen sc1" : "+v"(fl0) : "v"(fqo), "s"(wsr) : "memory"); }
      if constexpr (n == 55) fix2(B0, B1);
      if constexpr (n >= 56) glu_step(IC<2 * (n - 56) + 1>{});
      if constexpr (n == FQC) {
        asm volatile("s_waitcnt vmcnt(0)" : "+v"(fl0) :: "memory");
        _Pragma("nounroll") for (unsigned sp = 0; !ALL(fl0 == nonce) && sp < SPIN_CAP; ++sp)
          asm volatile("buffer_load_dword %0, %1, %2, 0 offen sc1\n\ts_waitcnt vmcnt(0)" : "+v"(fl0) : "v"(fqo), "s"(wsr) : "memory");
        kvv = (unsigned)(WS_QKV + head * QH + (wid >> 1) * 16 * QT + 2048 + (wid & 1) * 2048 + lane * 16);
        dma<0, L_Q, true>(wsr, WS_QKV + head * QH + (qg * 4 + (wid >> 1)) * QT + (wid & 1) * 1024 + lane * 16, lds0 + wid * 1024); dma_kv(IC<0>{}, kvv); }
      if constexpr (n > FQC && n < FQC + 8) dma_kv(IC<n - FQC>{}, kvv);
      if constexpr (n == 67) {
        const int vc = 2 * (grp ^ wn) + (p >> 1);
        kb = lds + (wid >> 1) * 4096 + lane * 16;
        vb0 = lds + (wid >> 1) * 4096 + 2048 + vc * 512 + ((((4 * h) ^ (4 * vc)) + q) << 4) + (p & 1) * 8;
        vb1 = lds + (wid >> 1) * 4096 + 2048 + vc * 512 + ((((4 * h + 8) ^ (4 * vc)) + q) << 4) + (p & 1) * 8;
        PIN(kb); PIN(vb0); PIN(vb1);
      }
      if constexpr (n == 68) { sa = MFMA(kf0, QA0, Z); sb = MFMA(kf0, QB0, Z); PIN(sa); PIN(sb); ka0 = ld128(kb); ka1 = ld128(kb + 1024); va0 = cat(trd(vb0), trd(vb1)); }
    });
    SBAR();
    C0 = MFMA(cfix, ((u32x4){mm, mm, mm, mm}), C0);
    float ma = sa[0], mb = sb[0];
    UNROLL for (int i = 1; i < 16; ++i) { ma = __builtin_fmaxf(ma, sa[i]); mb = __builtin_fmaxf(mb, sb[i]); }
    const unsigned pa = pk(1.f - max32(ma), 0.f), pb = pk(1.f - max32(mb), 0.f);
    const bool p0 = (head & 1) && h == 0, p1 = !(head & 1) && h == 1;
    QA0[0] = p0 ? pa : QA0[0]; QA1[2] = p1 ? pa : QA1[2]; QB0[0] = p0 ? pb : QB0[0]; QB1[2] = p1 ? pb : QB1[2];
    s0 = MFMA(ka0, QA0, Z); s0 = MFMA(ka1, QA1, s0);
    PIN(s0); PIN(QB0); PIN(QB1);
    SBAR();
    st128(glu8(C0), wsr, ng < 14 ? lane * 16 : OOB, WS_H + (mt * 112 + 64 + ng * 2 + wn) * 1024, SC1);
    SBAR();
    TS(8);
  }
  return phase23(x, wo, wd, ws, out, lds, nonce, QA0, QA1, QB0, QB1, ka0, ka1, va0, s0, kb, vb0, vb1, kvv, wid TSARG);
}

extern "C" void kernel_launch(void* const* d_in, const int* in_sizes, int n_in, void* d_out, int out_size, void* d_ws, size_t ws_size,
                              hipStream_t stream) {
  (void)hipFuncSetAttribute((const void*)fused_kernel, hipFuncAttributeMaxDynamicSharedMemorySize, LDS_BYTES);
  fused_kernel<<<256, 512, LDS_BYTES, stream>>>((const char*)d_in[0], (const char*)d_in[1], (const char*)d_in[2], (const char*)d_in[4], (const char*)d_in[5],
                                                (const char*)d_in[6], (char*)d_ws, (char*)d_out);
}
```

```cpp
#include <hip/hip_runtime.h>
#include <hip/hip_bf16.h>
#include <stdint.h>

typedef short bf16x8 __attribute__((ext_vector_type(8)));
typedef short s16x4 __attribute__((ext_vector_type(4)));
typedef float f32x16 __attribute__((ext_vector_type(16)));
typedef float f32x4 __attribute__((ext_vector_type(4)));
typedef float f32x2 __attribute__((ext_vector_type(2)));
typedef unsigned u32x4 __attribute__((ext_vector_type(4)));
typedef unsigned u32x2 __attribute__((ext_vector_type(2)));
typedef __bf16 bf2 __attribute__((ext_vector_type(2)));
typedef __amdgpu_buffer_rsrc_t srd_t;
#define LAS __attribute__((address_space(3)))
#define GAS __attribute__((address_space(1)))
typedef LAS char* lptr;
#define DI __device__ __forceinline__
#define AI __attribute__((always_inline))
#define MFMA(a, b, c) __builtin_amdgcn_mfma_f32_32x32x16_bf16(__builtin_bit_cast(bf16x8, (a)), __builtin_bit_cast(bf16x8, (b)), (c), 0, 0, 0)
#define PIN(x) asm volatile("" : "+v"(x))
#define SBAR() __builtin_amdgcn_sched_barrier(0)
#define RLX_AGENT __ATOMIC_RELAXED, __HIP_MEMORY_SCOPE_AGENT
#define EX(v) __builtin_amdgcn_exp2f(v)
#define F2U(x) __builtin_bit_cast(unsigned, (float)(x))
#define U2F(x) __builtin_bit_cast(float, (unsigned)(x))
#define ANY(p) (__builtin_amdgcn_ballot_w64(p) != 0)
#define ALL(p) (__builtin_amdgcn_ballot_w64(!(p)) == 0)
#define UNROLL _Pragma("unroll")
#define SRD(p, n) __builtin_amdgcn_make_buffer_rsrc((void*)(p), 0, (n), 0x00020000)

constexpr int WS_E = 0;
constexpr int WS_FQ = 4096;
constexpr int WS_FH = WS_FQ + 256 * 128;
constexpr int WS_FO = WS_FH + 256 * 128;
constexpr int WS_ST = WS_FO + 256 * 128;
constexpr int WS_QKV = 1 << 20;
constexpr int QT = 6144, QH = 64 * QT;
constexpr int WS_O = WS_QKV + 16 * QH;
constexpr int WS_H = WS_O + (2 << 20);
constexpr int WS_END = WS_H + 64 * 112 * 1024;
constexpr int OOB = 1 << 30;
constexpr int SC1 = 16;
constexpr unsigned SPIN_CAP = 1u << 20;

#define TS(i) do { } while (0)
#define TSR(i) do { } while (0)
#define TS_OUT() do { } while (0)

template <int I> struct IC { static constexpr int v = I; };
template <int B, int E, class F> DI void sfor(F&& f) { if constexpr (B < E) { f(IC<B>{}); sfor<B + 1, E>(f); } }
template <int N> DI void wait_bar() { asm volatile("s_waitcnt vmcnt(%0) lgkmcnt(0)\n\ts_barrier" :: "n"(N) : "memory"); }
template <int N> DI void vwait_bar() { asm volatile("s_waitcnt vmcnt(%0)\n\ts_barrier" :: "n"(N) : "memory"); }
DI void lbar() { asm volatile("s_waitcnt lgkmcnt(0)\n\ts_barrier" ::: "memory"); }
DI unsigned pk(float a, float b) { f32x2 v = {a, b}; return __builtin_bit_cast(unsigned, __builtin_convertvector(v, bf2)); }
DI float rbf(float a) { return U2F(pk(a, a) & 0xffff0000u); }
DI float dot2(unsigned a, unsigned b, float c) { return __builtin_amdgcn_fdot2_f32_bf16(__builtin_bit_cast(bf2, a), __builtin_bit_cast(bf2, b), c, false); }
DI u32x2 trd(lptr p) { return __builtin_bit_cast(u32x2, __builtin_amdgcn_ds_read_tr16_b64_v4i16((LAS s16x4*)p)); }
DI u32x4 cat(u32x2 a, u32x2 b) { return (u32x4){a[0], a[1], b[0], b[1]}; }
DI u32x4 ld128(lptr p) { return *(LAS u32x4*)p; }
DI unsigned bfi(unsigned m, unsigned a, unsigned b) { return (m & a) | (~m & b); }
template <int G, int L, bool DEV = false> DI void dma(srd_t srd, unsigned voff, unsigned lbase) {
  unsigned t;
  if constexpr (DEV)
    asm volatile("s_add_u32 m0, %3, %5\n\ts_mov_b32 %0, %4\n\tbuffer_load_dwordx4 %1, %2, %0 offen sc1 lds" : "=&s"(t) : "v"(voff), "s"(srd), "s"(lbase), "n"(G), "n"(L) : "memory", "scc");
  else
    asm volatile("s_add_u32 m0, %3, %5\n\ts_mov_b32 %0, %4\n\tbuffer_load_dwordx4 %1, %2, %0 offen lds" : "=&s"(t) : "v"(voff), "s"(srd), "s"(lbase), "n"(G), "n"(L) : "memory", "scc");
}
template <int L> DI void dma2(srd_t srd, unsigned voff0, unsigned voff1, unsigned lbase, unsigned ga, unsigned gb) {
  unsigned t;
  asm volatile("s_add_u32 m0, %4, %5\n\ts_mul_i32 %0, %6, %7\n\tbuffer_load_dwordx4 %1, %3, %0 offen lds\n\ts_add_u32 m0, m0, 1024\n\ts_nop 0\n\tbuffer_load_dwordx4 %2, %3, %0 offen lds"
               : "=&s"(t) : "v"(voff0), "v"(voff1), "s"(srd), "s"(lbase), "n"(L), "s"(ga), "s"(gb) : "memory", "scc");
}
template <int L> DI void dma1(srd_t srd, unsigned voff, unsigned lbase, unsigned ga, unsigned gb) {
  unsigned t;
  asm volatile("s_add_u32 m0, %3, %4\n\ts_mul_i32 %0, %5, %6\n\tbuffer_load_dwordx4 %1, %2, %0 offen lds" : "=&s"(t) : "v"(voff), "s"(srd), "s"(lbase), "n"(L), "s"(ga), "s"(gb) : "memory", "scc");
}
DI void touch(srd_t srd, unsigned voff, unsigned ldst) { asm volatile("s_mov_b32 m0, %2\n\ts_nop 0\n\tbuffer_load_dword %0, %1, 0 offen lds" :: "v"(voff), "s"(srd), "s"(ldst) : "memory"); }
DI void st128(u32x4 v, srd_t srd, int voff, int soff, int aux) { __builtin_amdgcn_raw_buffer_store_b128(v, srd, voff, soff, SC1); SBAR(); asm volatile("s_nop 1"); SBAR(); }
DI float swap32(float v, int h) {
  auto r = __builtin_amdgcn_permlane32_swap(F2U(v), F2U(v), false, false);
  return U2F(h ? r[0] : r[1]);
}
DI float max32(float v) {
  auto r = __builtin_amdgcn_permlane32_swap(F2U(v), F2U(v), false, false);
  return __builtin_fmaxf(U2F(r[0]), U2F(r[1]));
}
DI u32x4 widen(u32x2 a, u32x2 b) {
  auto r0 = __builtin_amdgcn_permlane32_swap(a[0], b[0], false, false); auto r1 = __builtin_amdgcn_permlane32_swap(a[1], b[1], false, false);
  return (u32x4){r0[0], r1[0], r0[1], r1[1]};
}
DI float wave_sum(float v) {
  v += U2F(__builtin_amdgcn_update_dpp(0u, F2U(v), 0x111, 0xf, 0xf, true));
  v += U2F(__builtin_amdgcn_update_dpp(0u, F2U(v), 0x112, 0xf, 0xf, true));
  v += U2F(__builtin_amdgcn_update_dpp(0u, F2U(v), 0x114, 0xf, 0xf, true));
  v += U2F(__builtin_amdgcn_update_dpp(0u, F2U(v), 0x118, 0xf, 0xf, true));
  v += U2F(__builtin_amdgcn_update_dpp(0u, F2U(v), 0x142, 0xa, 0xf, true));
  v += U2F(__builtin_amdgcn_update_dpp(0u, F2U(v), 0x143, 0xc, 0xf, true));
  return v;
}
DI unsigned flag_ld(const char* ws, int off) { return __hip_atomic_load((GAS unsigned*)(ws + off), RLX_AGENT); }
DI void flag_st(char* ws, int off, unsigned v) {
  if (__builtin_amdgcn_mbcnt_hi(~0u, __builtin_amdgcn_mbcnt_lo(~0u, 0u)) == 0) __hip_atomic_store((GAS unsigned*)(ws + off), v, RLX_AGENT); }

constexpr int RA = 2, NWF = RA + 1;
constexpr int PART = 14336;
constexpr int L_W = 65536;
constexpr int L_CS = 9 * 16384;
constexpr int L_PF = L_CS + 2048;
constexpr int L_Q = 65536;
constexpr int wpos(int m) { return m < 9 ? L_W + (m % 5) * 16384 : m == 9 ? 49152 : m == 10 ? L_W : m < 14 ? (m - 11) * 16384 : L_W + 16384 + (m - 14) * 8192; }
#define FQP 52
#define FQC 59
#define MAP 0
#define NG_OF(xcd, idx) ((xcd & 3) * 4 + (idx & 3))
#define MG_OF(xcd, idx) ((xcd >> 2) * 8 + (idx >> 2))
#define ROTX 0
constexpr int L_WD = 65536;
constexpr int LDS_BYTES = 163840;
constexpr unsigned ONES2 = 0x3f803f80u, PAD0 = 0x00003f80u;
static_assert(L_PF + 2048 <= LDS_BYTES, "LDS map");

constexpr int DN0 = 7;
constexpr int kv_at(int s, int p) { return p == 0 && s > 0 && s + 3 < 16 ? (s + 3) * 2 + 1 : p == 7 && s + 4 < 16 ? (s + 4) * 2 : -1; }
constexpr int wd_at(int s, int p) {
  if (s >= 1 && s <= 3) return p == 1 ? s * 4 - 4 : p == 3 ? s * 4 - 3 : p == 4 ? s * 4 - 2 : p == 5 ? s * 4 - 1 : -1;
  for (int g = 0; g < 2; ++g) { if (s == 10 + 4 * g && p == 7) return 12 + 4 * g; if (s == 11 + 4 * g) return p == 0 ? 13 + 4 * g : p == 2 ? 14 + 4 * g : p == 5 ? 15 + 4 * g : -1; }
  return -1;
}
constexpr int h_at(int s, int p) { const int z = (s - 5) * 3 + (p == 1 ? 1 : p == 3 ? 2 : 3); if (s == 15) return p == 1 ? 24 : p == 3 ? 25 : p == 4 ? 26 : p == 7 ? 27 : -1;
  return s == 4 && p == 7 ? 0 : s >= 5 && (p == 1 || p == 3 || p == 4) && z < 24 ? z : -1; }
constexpr bool pf_at(int s, int p) { return s == 4 && p == 1; }
constexpr int n_at(int s, int p) { return (kv_at(s, p) >= 0) + (wd_at(s, p) >= 0) + (h_at(s, p) >= 0) + pf_at(s, p); }
constexpr int cnt_after(int s0, int p0, int s) { int n = 0; for (int z = s0 * 8 + p0 + 1; z < s * 8 + 6; ++z) n += n_at(z / 8, z % 8); return n; }
constexpr int dslot(int sub) { return L_WD + (sub % 3) * 32768; }

struct QkvTab { unsigned v[44][2][4]; };
constexpr QkvTab make_tab() {
  QkvTab t = {};
  for (int tile = 0; tile < 44; ++tile) for (int h = 0; h < 2; ++h) {
    const int col0 = tile * 32, sec = col0 / 448; unsigned* e = t.v[tile][h];
    e[0] = e[1] = e[2] = OOB; e[3] = 0;
    if (tile < 42) for (int pp = 0; pp < 2; ++pp) {
      const int cu = (col0 - sec * 448) / 8 + 2 * pp + h, j = cu / 7, w = cu % 7, hd = 2 * j + (w > 3), ch = w > 3 ? w - 3 : w;
      e[pp] = WS_QKV + sec * 2048 + hd * QH + ch * 512 + (sec == 2 ? ch * 64 : 0);
      if (w == 3) { e[2] = WS_QKV + sec * 2048 + (hd + 1) * QH; e[3] |= 1u << pp; }
    }
  }
  return t;
}
__device__ const QkvTab qkv_tab = make_tab();

#define TSARG
#define TSPAR
#define LANE_IDS() int lane; asm volatile("v_mbcnt_lo_u32_b32 %0, -1, 0\n\tv_mbcnt_hi_u32_b32 %0, -1, %0" : "=v"(lane));        \
  const int r = lane & 31, h = lane >> 5, q = (lane & 15) >> 2, p = lane & 3, grp = (lane >> 4) & 1, rr = lane >> 3, cc = lane & 7
DI void phase23(const char* x, const char* wo, const char* wd, char* ws, char* out, lptr lds, unsigned nonce, u32x4 QA0, u32x4 QA1, u32x4 QB0, u32x4 QB1,
                u32x4 k0, u32x4 k1, u32x4 v0, f32x16 sa, lptr kb, lptr vb0, lptr vb1, unsigned kvv, int wid TSPAR) {
  const unsigned lds0 = (unsigned)(uintptr_t)lds;
  const int b = __builtin_amdgcn_workgroup_id_x(), xcd = b & 7, idx = b >> 3;
  const int ng = NG_OF(xcd, idx), mg = MG_OF(xcd, idx);
  const srd_t wsr = SRD(ws, WS_END);
  const f32x16 Z = {};
  const int head = 2 * xcd + (idx & 1), qg = idx >> 1, odd = head & 1;
  const int idx3 = idx < 28 ? idx : idx - 28, mq3 = (idx3 * 37) >> 8, mg3 = xcd * 4 + mq3, ng3 = idx3 - mq3 * 7;
  LANE_IDS();
  const unsigned lw2k = lds0 + wid * 2048;
  auto dma_kv = [&](auto zc) AI { constexpr int z = decltype(zc)::v; dma<(z >> 1) * QT + (z & 1) * 1024, ((z >> 1) & 3) * 16384 + (z & 1) * 1024, true>(wsr, kvv, lw2k); };
  const int w3m = wid & 1, w3k = wid >> 1;
  u32x4 hf[28];
  const int ft = w3k & 1, fs = w3k >> 1;
  unsigned vo_d = 0; const unsigned ld_d = lds0 + wid * 4096;
  auto set_vo_d = [&]() AI { vo_d = (unsigned)(((idx < 28 ? w3k * 448 + w3m * 32 : OOB / 896) + rr) * 896 + ((cc ^ (grp << 2)) << 4) + ng3 * 128); PIN(vo_d); };
  const srd_t sD = SRD(wd, 1792 * 896);
  auto dma_d = [&](auto zc) AI { constexpr int z = decltype(zc)::v; dma<((z >> 2) * 64 + (z & 3) * 8) * 896, dslot(z >> 2) + (z & 3) * 1024>(sD, vo_d, ld_d); };
  int db0 = 0, db1 = 0, dbo = L_WD;
  auto set_db = [&]() AI { db0 = L_WD + w3k * 8192 + (4 * h + q) * 128 + p * 8 + ((grp ^ ((q >> 1) << 1)) << 5); db1 = db0 ^ 64; PIN(db0); PIN(db1); };
  auto rebase = [&](int o) AI { db0 += o - dbo; db1 += o - dbo; dbo = o; PIN(db0); PIN(db1); };
  f32x16 F0 = Z, F1 = Z;
  u32x4 dw0, dw1;
  auto rd_d0 = [&](int d) AI { const int o = dslot(d >> 2) + (d & 3) * 2048 - dbo; dw0 = cat(trd(lds + db0 + o), trd(lds + db0 + o + 1024)); };
  auto rd_d1 = [&](int d) AI { const int o = dslot(d >> 2) + (d & 3) * 2048 - dbo; dw1 = cat(trd(lds + db1 + o), trd(lds + db1 + o + 1024)); };
  {
    const int wq = wid & 1, kq = wid >> 1;
    u32x4 v1, pa, pb;
    float e0, e1, e2, e3;
    f32x16 OA = Z, OB = Z, sb;
    unsigned junk, fh = ~nonce; const int fho = WS_FH + ((mg3 >> 1) * 16 + (lane & 15)) * 128;
    const int hld = WS_H + ((mg3 * 2 + w3m) * 112 + w3k * 28) * 1024, hlv = idx < 28 ? lane * 16 : OOB;
    auto send = [&](const f32x16& a, int hh, int j) AI {
      const lptr d = kb + (((wid ^ (2 * j)) * 6144 + (j - 1) * 2048) - kq * 4096);
      *(LAS f32x4*)d = (f32x4){a[8 * hh], a[8 * hh + 1], a[8 * hh + 2], a[8 * hh + 3]}; *(LAS f32x4*)(d + 1024) = (f32x4){a[8 * hh + 4], a[8 * hh + 5], a[8 * hh + 6], a[8 * hh + 7]}; };
    TS(10); TS(19);
#define EX2(S, i, a, c) do { a = EX(S[i]); c = EX(S[(i) + 1]); PIN(a); PIN(c); } while (0)
#define STEP(FIRST, SC, SN, QN0, QN1, OP, OC, L0, L1, L2, L3, X0, X1) do {                                                       \
      SBAR(); SN = MFMA(k0, QN0, Z); SBAR(); L0;                                                                          \
      if (!(FIRST)) { pb[2] = pk(e0, e1); pb[3] = pk(e2, e3); PIN(pb); } SBAR(); EX2(SC, 0, e0, e1); SBAR(); X0; SBAR(); EX2(SC, 2, e2, e3); \
      SBAR(); if (!(FIRST)) OP = MFMA(v1, pb, OP); SBAR(); L1;                                                                          \
      pa[0] = pk(e0, e1); pa[1] = pk(e2, e3); PIN(pa); SBAR(); EX2(SC, 4, e0, e1); EX2(SC, 6, e2, e3);                    \
      SBAR(); SN = MFMA(k1, QN1, SN); SBAR(); L2;                                                                         \
      pa[2] = pk(e0, e1); pa[3] = pk(e2, e3); PIN(pa); SBAR(); EX2(SC, 8, e0, e1); SBAR(); X1; SBAR(); EX2(SC, 10, e2, e3); \
      SBAR(); OC = MFMA(v0, pa, OC); SBAR(); L3;                                                                          \
      pb[0] = pk(e0, e1); pb[1] = pk(e2, e3); PIN(pb); SBAR(); EX2(SC, 12, e0, e1); EX2(SC, 14, e2, e3); SBAR(); } while (0)
    sfor<0, 16>([&](auto sc_) AI {
      constexpr int s = decltype(sc_)::v, so = (s & 3) * 16384, sn = ((s + 1) & 3) * 16384;
      auto side = [&](auto pc) AI {
        constexpr int P = decltype(pc)::v, kv = kv_at(s, P), wz = wd_at(s, P), hz = h_at(s, P);
        if constexpr (s == 0 && P == 3) set_vo_d();
        if constexpr (s == 0 && P == 5) set_db();
        if constexpr (kv >= 0) dma_kv(IC<kv>{});
        if constexpr (wz >= 0) dma_d(IC<wz>{});
        if constexpr (pf_at(s, P)) {
          const int row = wid < 2 ? mq3 * 448 + 320 + wid * 64 + lane : mq3 * 112 + (wid - 2) * 56 + (lane < 56 ? lane : 55);
          asm volatile("buffer_load_dword %0, %1, %2, 0 offen" : "=&v"(junk) : "v"(wid < 4 && idx < 28 ? row * 896 + ng3 * 128 : OOB), "s"(SRD(wid < 2 ? wd : wo, wid < 2 ? 1792 * 896 : 448 * 896)) : "memory");
        }
        if constexpr (hz >= 0) hf[hz] = __builtin_amdgcn_raw_buffer_load_b128(wsr, hlv + (hz & 3) * 1024, hld + (hz >> 2) * 4096, SC1);
      };
      auto mid = [&]() AI {
        if (s == 4 && wid == 0) {
          asm volatile("s_waitcnt vmcnt(%1)" : "+v"(fh) : "n"(cnt_after(3, 6, 4)) : "memory");
          _Pragma("nounroll") for (unsigned sp = 0; !ALL(fh == nonce) && sp < SPIN_CAP; ++sp) { fh = flag_ld(ws, fho); PIN(fh); }
        }
        if constexpr (s == 0) vwait_bar<5 + cnt_after(0, -1, 0)>();
        else if constexpr (s == 1) vwait_bar<cnt_after(0, -1, 1)>();
        else if constexpr (s == 14) vwait_bar<0>(); else if constexpr (s < 14) vwait_bar<cnt_after(s - 1, 0, s)>();
        if (s == 1 && wid == 0) flag_st(ws, WS_FH + (mg * 16 + ng) * 128, nonce);
        if (s == 3 && wid == 0) asm volatile("buffer_load_dword %0, %1, %2, 0 offen sc1" : "+v"(fh) : "v"(fho), "s"(wsr) : "memory");
        if constexpr (s == 6) asm volatile("" :: "v"(junk));
        if constexpr (s == 13) rebase(L_WD + 65536);
        if constexpr (s == 14) { UNROLL for (int k = 23; k >= 8; --k) PIN(hf[k]); }
        if (s == 1) TS(20); if (s == 3) TS(21); if (s == 5) TS(22); if (s == 7) TS(23); if (s == 9) TS(24); if (s == 11) TS(25); if (s == 13) TS(26);
        __builtin_amdgcn_s_setprio(3);
      };
      auto dn0 = [&](int d) AI { if (d >= 0) { F0 = MFMA(dw0, hf[d], F0); PIN(F0); } if (d >= -1) rd_d0(d + 1); };
      auto dn1 = [&](int d) AI { if (d >= 0) { F1 = MFMA(dw1, hf[d], F1); PIN(F1); } if (d >= -1) rd_d1(d + 1); };
      STEP(s == 0, sa, sb, QB0, QB1, OB, OA,
           k0 = ld128(kb + sn); side(IC<0>{}); __builtin_amdgcn_s_setprio(2),
           v1 = cat(trd(vb0 + so + 256), trd(vb1 + so + 256)); side(IC<1>{}),
           k1 = ld128(kb + sn + 1024); side(IC<2>{}); __builtin_amdgcn_s_setprio(1),
           side(IC<3>{}), dn0(s - DN0), if (s == 15) dn1(8));
      STEP(false, sb, sa, QA0, QA1, OA, OB,
           __builtin_amdgcn_s_setprio(0); side(IC<4>{}),
           side(IC<5>{}),
           mid(); if (s == 15) send(OA, 0, 1),
           v0 = cat(trd(vb0 + sn), trd(vb1 + sn)); side(IC<7>{}); if (s == 15) send(OA, 1, 3), if (s < 15) dn1(s - DN0); else dn0(9), if (s == 15) dn1(9));
    });
    pb[2] = pk(e0, e1); pb[3] = pk(e2, e3);
    OB = MFMA(v1, pb, OB);
    __builtin_amdgcn_s_setprio(0);
    TS(11);
    send(OB, 1, 2);
    *(LAS float*)(kb + (49152 + wq * 1024)) = odd ? OB[0] : OB[4];
    {
    LANE_IDS();
    const bool padl = h != odd;
    const int qt = qg * 4 + wq * 2 + ft, lw = (wid & 3) | (odd ? 0 : 4);
    const lptr pp = lds + wid * 6144 + lane * 16, lp = lds + lw * 6144 + (odd ? 0 : 1536) + r * 16;
    const lptr lq = lds + 49152 + (lw >> 1) * 4096 + wq * 1024 + (odd ? 0 : 512) + r * 16;
    f32x4 o0 = {OB[0], OB[1], OB[2], OB[3]}, o1 = {OB[4], OB[5], OB[6], OB[7]};
    lbar();
    const float l0 = *(LAS float*)lq, l1 = *(LAS float*)lp, l2 = *(LAS float*)(lp + 2048), l3 = *(LAS float*)(lp + 4096);
    f32x4 t[6];
    UNROLL for (int k = 0; k < 6; ++k) t[k] = *(LAS f32x4*)(pp + k * 1024);
    SBAR();
    float l = (l0 + l1) + (l2 + l3);
    UNROLL for (int k = 0; k < 3; ++k) { o0 += t[2 * k]; o1 += t[2 * k + 1]; }
    if (__builtin_expect(ANY(!(l <= 1.8446744e19f && l >= 5.4210109e-20f)), 0)) {
      const int qo = WS_QKV + head * QH + qt * QT + lane * 16;
      u32x4 q0 = __builtin_amdgcn_raw_buffer_load_b128(wsr, qo, 0, SC1), q1 = __builtin_amdgcn_raw_buffer_load_b128(wsr, qo + 1024, 0, SC1);
      f32x16 O = Z; float m = -__builtin_inff();
      _Pragma("nounroll") for (int t = 0; t < 64; ++t) {
        const int ko = WS_QKV + head * QH + t * QT + 2048;
        f32x16 s = MFMA(__builtin_amdgcn_raw_buffer_load_b128(wsr, ko + lane * 16, 0, SC1), q0, Z);
        s = MFMA(__builtin_amdgcn_raw_buffer_load_b128(wsr, ko + 1024 + lane * 16, 0, SC1), q1, s);
        float mn = m;
        UNROLL for (int i = 0; i < 16; ++i) mn = __builtin_fmaxf(mn, s[i]);
        mn = max32(mn);
        const float f = EX(m - mn); m = mn;
        UNROLL for (int i = 0; i < 16; ++i) O[i] *= f;
        UNROLL for (int st = 0; st < 2; ++st) {
          unsigned vv[8];
          UNROLL for (int j = 0; j < 8; ++j) vv[j] = __builtin_amdgcn_raw_buffer_load_b16(wsr, ko + 2048 + (r >> 3) * 512 + (((16 * st + 8 * (j >> 2) + 4 * h + (j & 3)) ^ (4 * (r >> 3))) << 4) + (r & 7) * 2, 0, SC1) & 0xffffu;
          O = MFMA(((u32x4){vv[0] | vv[1] << 16, vv[2] | vv[3] << 16, vv[4] | vv[5] << 16, vv[6] | vv[7] << 16}),
                   ((u32x4){pk(EX(s[8 * st] - m), EX(s[8 * st + 1] - m)), pk(EX(s[8 * st + 2] - m), EX(s[8 * st + 3] - m)),
                            pk(EX(s[8 * st + 4] - m), EX(s[8 * st + 5] - m)), pk(EX(s[8 * st + 6] - m), EX(s[8 * st + 7] - m))}), O);
        }
      }
      const float lc = odd ? O[0] : O[12], lx = swap32(lc, h); l = padl ? lc : lx;
      o0 = fs ? (f32x4){O[8], O[9], O[10], O[11]} : (f32x4){O[0], O[1], O[2], O[3]};
      o1 = fs ? (f32x4){O[12], O[13], O[14], O[15]} : (f32x4){O[4], O[5], O[6], O[7]};
    }
    const float ri = __builtin_amdgcn_rcpf(l);
    const float r0 = padl && odd && fs == 0 ? 0.f : ri, r1 = padl && !odd && fs == 1 ? 0.f : ri;
    st128((u32x4){pk(o0[0] * r0, o0[1] * r0), pk(o0[2] * r0, o0[3] * r0), pk(o1[0] * r1, o1[1] * r1), pk(o1[2] * r1, o1[3] * r1)},
                                           wsr, lane * 16, WS_O + (qt * 16 + head) * 2048 + fs * 1024, SC1);
    }
  }
  TS(12);
  if (idx >= 28) { wait_bar<0>();
    if (wid == 0) {
      flag_st(ws, WS_FO + (qg * 16 + head) * 128, nonce);
      if (b == 255) {
        LANE_IDS();
        _Pragma("nounroll") for (unsigned sp = 0; sp < SPIN_CAP; ++sp) {
          unsigned d = 0;
          UNROLL for (int i = 0; i < 4; ++i) d |= flag_ld(ws, WS_FQ + lane * 512 + i * 128) ^ nonce;
          if (ALL(d == 0)) break;
        }
        flag_st(ws, WS_E, nonce);
      }
    }
    return;
  }
  {
    lbar();
    const srd_t sO = SRD(wo, 448 * 896); const unsigned wov = vo_d + (wid * 56 - w3k * 448 - w3m * 32) * 896, wol = lds0 + wid * 7168;
    auto dma_o = [&](auto ic) AI { constexpr int i = decltype(ic)::v; dma<i * 8 * 896, i * 1024>(sO, wov, wol); };
    sfor<0, 4>(dma_o);
    u32x4 ew0, ew1;
    auto down2 = [&](int d, bool more) AI {
      const int o = dslot((d + 1) >> 2) + ((d + 1) & 3) * 2048 - dbo;
      ew0 = cat(trd(lds + db0 + o), trd(lds + db0 + o + 1024)); ew1 = cat(trd(lds + db1 + o), trd(lds + db1 + o + 1024));
      if (d == 10 || d == 22) rebase(L_WD);
      SBAR(); F0 = MFMA(dw0, hf[d], F0); F1 = MFMA(dw1, hf[d], F1); PIN(F0); PIN(F1); SBAR();
      if (more) { rd_d0(d + 2); rd_d1(d + 2); }
      SBAR(); F0 = MFMA(ew0, hf[d + 1], F0); F1 = MFMA(ew1, hf[d + 1], F1); PIN(F0); PIN(F1); SBAR();
    };
    static_assert(DN0 == 7, "units 0..9 ran beside the attention");
    down2(10, true); sfor<4, 7>(dma_o);
    vwait_bar<7>();
    if (wid == 0) flag_st(ws, WS_FO + (qg * 16 + head) * 128, nonce);
    TS(13);
    sfor<20, 22>(dma_d); down2(12, true); sfor<22, 24>(dma_d); down2(14, true);
    vwait_bar<4>();
    LANE_IDS();
    const int mt = mg3 * 2 + w3m, orot = ng3 & 1;
    unsigned fo = ~nonce; const int foo = WS_FO + ((mg3 >> 1) * 16 + (lane & 15)) * 128;
    sfor<24, 26>(dma_d); down2(16, true); sfor<26, 28>(dma_d);
    if (wid == 0) asm volatile("buffer_load_dword %0, %1, %2, 0 offen sc1" : "+v"(fo) : "v"(foo), "s"(wsr) : "memory");
    down2(18, false);
    u32x4 wo0[8], wo1[8];
    UNROLL for (int k = 0; k < 8; ++k) {
      const int wbo = w3k * (PART - 8192) - L_WD + ((k >> 2) ^ orot) * 7168, o = (((k >> 1) & 1) * 24 + (k & 1) * 16) * 128;
      const lptr wb0 = lds + db0 + wbo, wb1 = lds + db1 + wbo;
      wo0[k] = cat(trd(wb0 + o), trd(wb0 + o + 1024)); wo1[k] = cat(trd(wb1 + o), trd(wb1 + o + 1024));
    }
    rebase(L_WD + 65536);
    TS(14);
    if (wid == 0) {
      asm volatile("s_waitcnt vmcnt(0)" : "+v"(fo) :: "memory");
      _Pragma("nounroll") for (unsigned sp = 0; !ALL(fo == nonce) && sp < SPIN_CAP; ++sp) { fo = flag_ld(ws, foo); PIN(fo); }
    }
    wait_bar<0>();
    UNROLL for (int k = 27; k >= 24; --k) PIN(hf[k]);
    TS(15);
    u32x4 of[8];
    const int oso = WS_O + (mt * 16 + w3k * 4) * 2048 + orot * 4096;
    UNROLL for (int k = 0; k < 4; ++k) of[k] = __builtin_amdgcn_raw_buffer_load_b128(wsr, lane * 16 + k * 1024, oso, SC1);
    SBAR();
    rd_d0(20); rd_d1(20); down2(20, true); down2(22, true);
    UNROLL for (int k = 4; k < 8; ++k) of[k] = __builtin_amdgcn_raw_buffer_load_b128(wsr, lane * 16 + (k & 3) * 1024, oso ^ 4096, SC1);
    const unsigned xo = (unsigned)((mt * 32 + w3k * 8 + rr) * 896 + ng3 * 128 + cc * 16);
    const u32x4 xv = *(const u32x4*)(x + xo);
    SBAR();
    down2(24, true); down2(26, false);
    auto xch_send = [&](const f32x16& a0, const f32x16& a1, int base) AI {
      UNROLL for (int c = 0; c < 8; ++c) { const f32x16& a = c < 4 ? a0 : a1; const int o = (c & 3) * 4;
        *(LAS f32x4*)(lds + (base + wid * 8192 + c * 1024) + ((lane ^ (2 * c)) << 4)) = (f32x4){a[o], a[o + 1], a[o + 2], a[o + 3]}; } };
    int xro = w3m * 8192 + cc * 1024 + (((w3k * 8 + rr) ^ (2 * cc)) << 4); PIN(xro);
    xch_send(F0, F1, 0);
    f32x16 S0 = Z, S1 = Z;
    UNROLL for (int k = 0; k < 4; ++k) { S0 = MFMA(wo0[k], of[k], S0); S1 = MFMA(wo1[k], of[k], S1); }
    PIN(S0); PIN(S1);
    lbar();
    f32x4 g0 = *(LAS f32x4*)(lds + xro), g1 = *(LAS f32x4*)(lds + xro + 512);
    UNROLL for (int k = 1; k < 4; ++k) { g0 += *(LAS f32x4*)(lds + xro + k * 16384); g1 += *(LAS f32x4*)(lds + xro + k * 16384 + 512); }
    UNROLL for (int k = 4; k < 8; ++k) { S0 = MFMA(wo0[k], of[k], S0); S1 = MFMA(wo1[k], of[k], S1); }
    UNROLL for (int i = 0; i < 4; ++i) {
      g0[i] = g0[i] * __builtin_amdgcn_rcpf(1.f + EX(g0[i] * -1.4426950408889634f)); g1[i] = g1[i] * __builtin_amdgcn_rcpf(1.f + EX(g1[i] * -1.4426950408889634f)); }
    TS(16);
    xch_send(S0, S1, L_WD);
    f32x4 y0 = {U2F(xv[0] << 16), U2F(xv[0] & 0xffff0000u), U2F(xv[1] << 16), U2F(xv[1] & 0xffff0000u)}, y1 = {U2F(xv[2] << 16), U2F(xv[2] & 0xffff0000u), U2F(xv[3] << 16), U2F(xv[3] & 0xffff0000u)};
    xro += L_WD;
    PIN(g0); PIN(g1); PIN(y0); PIN(y1); PIN(xro);
    lbar();
    f32x4 t[8];
    UNROLL for (int k = 0; k < 8; ++k) t[k] = *(LAS f32x4*)(lds + xro + (k >> 1) * 16384 + (k & 1) * 512);
    SBAR();
    UNROLL for (int k = 0; k < 4; ++k) UNROLL for (int i = 0; i < 4; ++i) { y0[i] = __builtin_fmaf(t[2 * k][i], g0[i], y0[i]); y1[i] = __builtin_fmaf(t[2 * k + 1][i], g1[i], y1[i]); }
    __builtin_amdgcn_raw_buffer_store_b128((u32x4){pk(y0[0], y0[1]), pk(y0[2], y0[3]), pk(y1[0], y1[1]), pk(y1[2], y1[3])}, SRD(out, 2048 * 896), xo, 0, SC1);
    TS(17); TS_OUT();
  }
}

#if defined(__HIP_DEVICE_COMPILE__)
#define NOPK __attribute__((target("no-packed-fp32-ops")))
#else
#define NOPK
#endif
__global__ __launch_bounds__(512, 2) NOPK void fused_kernel(const char* x, const char* wqkv, const char* wo, const char* wg, const char* wu, const char* wd,
                                                       char* ws, char* out) {
  extern __shared__ __attribute__((aligned(16))) char smem[];
  const lptr lds = (lptr)smem;
  const unsigned lds0 = (unsigned)(uintptr_t)lds;
  const int tid = __builtin_amdgcn_workitem_id_x(), lane = tid & 63, wid = __builtin_amdgcn_readfirstlane(tid >> 6);
  const int rr = lane >> 3, cc = lane & 7;
  const int b = __builtin_amdgcn_workgroup_id_x(), xcd = b & 7, idx = b >> 3;
  const int ng = NG_OF(xcd, idx), mg = MG_OF(xcd, idx);
  unsigned tsv = 0; TSR(62); TS(0);
  const int rot0 = MAP == 2 ? (mg & 3) * 2 + ((mg >> 2) & 1) : (mg + ROTX * (mg >> 3)) & 7, rot = rot0 == 7 ? 3 : rot0;
  unsigned kc[7];
  UNROLL for (int c = 0; c < 7; ++c) kc[c] = (unsigned)((c + rot >= 7 ? c + rot - 7 : c + rot) * 64);
  const srd_t xr = SRD(x, 2048 * 896);
  const unsigned xv0 = (unsigned)((mg * 128 + wid * 16 + rr) * 896 + ((cc ^ rr) << 4));
  const unsigned xv1 = (unsigned)((mg * 128 + wid * 16 + 8 + (rr ^ 1)) * 896 + ((cc ^ rr ^ 1) << 4));
  const unsigned lw2k = lds0 + wid * 2048;
  auto dma_x = [&](auto cc_) AI { constexpr int c = decltype(cc_)::v; dma2<(c & 3) * 16384>(xr, xv0, xv1, lw2k, kc[c], 2); };
  const srd_t wsr = SRD(ws, WS_END);
  u32x4 srow; unsigned nonce;
  const int srw = mg * 128 + ng * 8 + wid;
  asm volatile("buffer_load_dwordx4 %0, %1, %2, %3 offen" : "=&v"(srow) : "v"(lane * 16), "s"(SRD(x, srw * 896 + 896)), "s"(srw * 896) : "memory");
  dma_x(IC<0>{});
  SBAR();
  const int r = lane & 31, h = lane >> 5, q = (lane & 15) >> 2, p = lane & 3, grp = (lane >> 4) & 1;
  const f32x16 Z = {};
  const int head = 2 * xcd + (idx & 1), qg = idx >> 1;
  auto dma_kv = [&](auto zc, unsigned kvv) AI { constexpr int z = decltype(zc)::v; dma<(z >> 1) * QT + (z & 1) * 1024, ((z >> 1) & 3) * 16384 + (z & 1) * 1024, true>(wsr, kvv, lw2k); };
  u32x4 QA0, QA1, QB0, QB1, kf0, kf1, ka0, ka1, va0; f32x16 s0; lptr kb, vb0, vb1; unsigned kvv = 0;
  {
    const int wm = wid & 3, wn = wid >> 2;
    const bool bq = ng <= 10;
    const unsigned st1 = bq ? 2688 : 3584;
    unsigned va = (16 * wm + rr) * st1 + (((cc << 4) ^ (grp << (bq ? 6 : 5))) ^ (bq ? 0 : wn << 6)) + (bq ? 2 * ng + wn : ng + 12) * 128, vb = va + 8 * st1;
    const char* wgu = wn ? wu : wg;
    const srd_t s2 = SRD(wgu, 448 * 3584), s1 = SRD(bq ? wqkv : wgu, 448 * st1);
    auto dma_w = [&](auto mc) AI {
      constexpr int m = decltype(mc)::v;
      if constexpr (m == 7) { va = (unsigned)((16 * wm + rr) * 3584 + ((cc ^ (grp << 1) ^ (wn << 2)) << 4)) + ng * 128; vb = va + 8 * 3584; PIN(va); PIN(vb); }
      if constexpr (m == 14) {
        va = (unsigned)((16 * wm + (lane >> 2)) * 3584 + (((lane & 3) ^ (wn << 1)) << 4)) + 2048 + ng * 64;
        if (ng >= 14) va = OOB;
        PIN(va);
      }
      if constexpr (m < 7) dma2<wpos(m)>(s1, va, vb, lw2k, kc[m], st1);
      else if constexpr (m < 14) dma2<wpos(m)>(s2, va, vb, lw2k, kc[m - 7], 3584);
      else dma1<wpos(m)>(s2, va, lds0 + wid * 1024, kc[m - 14], 3584);
    };
    dma_w(IC<0>{});
    unsigned epoch;
    asm volatile("buffer_load_dword %0, off, %1, 0 sc1" : "=&v"(epoch) : "s"(wsr) : "memory");
    dma_x(IC<1>{});
    {
      unsigned long long pc; asm volatile("s_getpc_b64 %0" : "=s"(pc));
      if (wid == 7) touch(SRD(pc & ~127ull, 28672), lane < 8 ? (idx * 8 + lane) * 128 : OOB, lds0 + L_PF);
    }
    SBAR();
    const int rowb = (8 * h + q) * 128 + p * 8;
    int b20 = grp * 8192 + rowb + (((2 * wn) ^ (q >> 1) ^ (grp << 1)) << 5);
    int b10 = bq ? wn * 8192 + rowb + ((grp ^ ((q >> 1) << 1)) << 5) : b20, b11 = b10 ^ (bq ? 64 : 32);
    int xb[4];
    UNROLL for (int i = 0; i < 4; ++i) { xb[i] = (4 * wm + (r >> 3)) * 1024 + ((r & 7) ^ ((r >> 3) & 1)) * 128 + (((2 * i + h) ^ (r & 7)) << 4); PIN(xb[i]); }
    PIN(b10); PIN(b11); PIN(b20);
    UNROLL for (int c = 2; c < 7; ++c) asm volatile("" : "+s"(kc[c]));
    SBAR();
    u32x4 xf[28];
    auto row_stats = [&]() AI {
      float a = dot2(srow[0], ONES2, 0.f), c = dot2(srow[0], srow[0], 0.f);
      UNROLL for (int i = 1; i < 4; ++i) { a = dot2(srow[i], ONES2, a); c = dot2(srow[i], srow[i], c); }
      a = wave_sum(a); c = wave_sum(c);
      const float mean = a * (1.f / 448.f), var = __builtin_fmaxf(c * (1.f / 448.f) - mean * mean, 0.f);
      unsigned g = pk(-mean, __builtin_amdgcn_sqrtf(rbf(rbf(var) + 1e-5f))); PIN(g); SBAR();
      asm volatile("s_waitcnt vmcnt(4)" : "+v"(epoch) :: "memory");
      nonce = __builtin_amdgcn_readfirstlane(epoch) * 0x9E3779B1u + 0x7F4A7C15u;
      if (lane == 63) __hip_atomic_store((GAS unsigned long long*)(ws + WS_ST + srw * 8), (unsigned long long)nonce << 32 | g, RLX_AGENT);
    };
    auto rd_x = [&](int k0, int k1) AI { UNROLL for (int ks = k0; ks < k1; ++ks) xf[ks] = ld128(lds + xb[ks & 3] + ((ks >> 2) & 3) * 16384); };
    u32x2 wf[NWF][4];
    int ra0 = 0, ra1 = 0;
    auto rd_w = [&](int n) AI {
      if (n < 56) {
        const int m = n >> 2, o = (n & 3) * 2048;
        if ((n & 3) == 0) { ra0 = (m < 7 ? b10 : b20) + wpos(m); ra1 = (m < 7 ? b11 : b20 ^ 32) + wpos(m); PIN(ra0); PIN(ra1); }
        wf[n % NWF][0] = trd(lds + ra0 + o); wf[n % NWF][1] = trd(lds + ra0 + o + 512); wf[n % NWF][2] = trd(lds + ra1 + o); wf[n % NWF][3] = trd(lds + ra1 + o + 512);
      } else {
        const int o = ((n - 56) >> 1) * 8192 + (n & 1) * 2048;
        if (n == 56) { ra0 = wpos(14) + grp * 4096 + (8 * h + q) * 64 + ((wn ^ grp) << 5) + p * 8; PIN(ra0); }
        wf[n % NWF][0] = trd(lds + ra0 + o); wf[n % NWF][1] = trd(lds + ra0 + o + 256); wf[n % NWF][2] = trd(lds + ra0 + o + 1024); wf[n % NWF][3] = trd(lds + ra0 + o + 1280);
      }
    };
    SBAR();
    dma_w(IC<1>{});
    asm volatile("s_waitcnt vmcnt(9)" : "+v"(srow) :: "memory");
    row_stats();
    SBAR();
    lbar();
    TS(1);
    UNROLL for (int n = 0; n < RA; ++n) rd_w(n);
    rd_x(0, 4);
    dma_x(IC<2>{});
    f32x16 A0 = Z, A1 = Z, B0 = Z, B1 = Z, C0 = Z;
    float cs0 = 0.f, cs1 = 0.f, rstd = 0.f, sig = 0.f, kg = 0.f; unsigned mm = 0; u32x2 sg = {0u, ~nonce};
    const int mt = mg * 4 + wm;
    const unsigned csel = ((wm & 1) ? 0x05040c0cu : 0x01000c0cu) + ((wm >> 1) ? 0x02020000u : 0u);
    auto colsum = [&](int n, int hh) AI {
      cs0 += U2F(__builtin_amdgcn_perm(wf[n % NWF][2 + hh][0], wf[n % NWF][hh][0], csel)); cs1 += U2F(__builtin_amdgcn_perm(wf[n % NWF][2 + hh][1], wf[n % NWF][hh][1], csel));
      PIN(cs0); PIN(cs1);
    };
    auto cs_out = [&]() AI {
      const float cs = cs0 + cs1, hi = rbf(cs); *(LAS unsigned*)(lds + L_CS + wn * 1024 + lane * 16 + ((wm & 1) * 2 + (wm >> 1)) * 4) = pk(hi, cs - hi); cs0 = 0.f; cs1 = 0.f; };
    auto fix2 = [&](f32x16& a0, f32x16& a1) AI {
      const u32x4 c = ld128(lds + L_CS + wn * 1024 + lane * 16);
      a0 = MFMA(c, ((u32x4){mm, mm, 0u, 0u}), a0); a1 = MFMA(c, ((u32x4){0u, 0u, mm, mm}), a1); PIN(a0); PIN(a1); };
    auto glu = [&](const f32x16& acc, int i) AI { return acc[i + 8] * __builtin_amdgcn_rcpf(__builtin_fmaf(EX(acc[i] * kg), sig, sig)); };
    auto glu8 = [&](const f32x16& acc) AI { return (u32x4){pk(glu(acc, 0), glu(acc, 1)), pk(glu(acc, 2), glu(acc, 3)), pk(glu(acc, 4), glu(acc, 5)), pk(glu(acc, 6), glu(acc, 7))}; };
    const int hs2 = WS_H + (mt * 112 + ng * 4 + wn * 2) * 1024;
    u32x4 tb0 = {0u, 0u, 0u, 0u}, tb1 = tb0;
    auto epi1 = [&](const f32x16& acc, int t) AI {
      if (bq) {
        const u32x4 e = t ? tb1 : tb0; const unsigned o[3] = {e[0] ^ (r << 4), e[1] ^ (r << 4), e[2] ^ (r << 4)};
        const unsigned m[2] = {(unsigned)__builtin_amdgcn_sbfe(e[3], 0, 1), (unsigned)__builtin_amdgcn_sbfe(e[3], 1, 1)};
        const float sc = 2 * ng + wn < 7 ? rstd * (0.18898223650461362f * 1.4426950408889634f) : rstd;
        u32x4 v[2];
        UNROLL for (int pp = 0; pp < 2; ++pp)
          v[pp] = widen((u32x2){pk(acc[8 * pp] * sc, acc[8 * pp + 1] * sc), pk(acc[8 * pp + 2] * sc, acc[8 * pp + 3] * sc)},
                        (u32x2){pk(acc[8 * pp + 4] * sc, acc[8 * pp + 5] * sc), pk(acc[8 * pp + 6] * sc, acc[8 * pp + 7] * sc)});
        u32x4 d0 = {v[0][0], v[0][1], bfi(m[0], PAD0, v[0][2]), ~m[0] & v[0][3]}, d1 = {v[1][0], v[1][1], bfi(m[1], PAD0, v[1][2]), ~m[1] & v[1][3]};
        u32x4 d2 = {PAD0, 0u, bfi(m[0], v[0][2], v[1][2]), bfi(m[0], v[0][3], v[1][3])};
        PIN(d0); PIN(d1); PIN(d2); SBAR();
        __builtin_amdgcn_raw_buffer_store_b128(d0, wsr, o[0], mt * QT, SC1); __builtin_amdgcn_raw_buffer_store_b128(d1, wsr, o[1], mt * QT, SC1);
        st128(d2, wsr, o[2], mt * QT, SC1);
      } else st128(glu8(acc), wsr, lane * 16 + t * 1024, hs2 + 12 * 4096, SC1);
    };
    u32x4 hp, hq; unsigned fl0 = ~nonce; f32x16 sa, sb; u32x4 cfix;
    int fqo = 0;
    auto fq_off = [&]() AI {
      const int c0 = (lane < 8 ? 448 + (wid & 1) * 448 : 0) + head * 28 - (head & 1) * 4 + (lane & 1) * 31, g = c0 >> 7, m2 = lane < 8 ? (wid >> 1) * 4 + (lane >> 1) : qg;
      fqo = WS_FQ + (m2 * 16 + g) * 128; };
    const int sgo = WS_ST + (mg * 128 + wm * 32 + r) * 8;

    sfor<0, 70>([&](auto nc) AI {
      constexpr int n = decltype(nc)::v, u = n & 7;
      constexpr int k0 = n < 28 ? n : n < 56 ? n - 28 : 2 * (n - 56), k1 = n < 56 ? k0 : k0 + 1;
      f32x16& D0 = n < 28 ? A0 : n < 56 ? B0 : C0; f32x16& D1 = n < 28 ? A1 : n < 56 ? B1 : C0;
      constexpr bool early = n == 54 || n == 55 || n >= 68;
      PIN(wf[n % NWF][3]);
      if (u == 7) __builtin_amdgcn_s_setprio(3); if (u == 1) __builtin_amdgcn_s_setprio(2); if (u == 3) __builtin_amdgcn_s_setprio(1); if (u == 5) __builtin_amdgcn_s_setprio(0);
      SBAR();
      D0 = MFMA(cat(wf[n % NWF][0], wf[n % NWF][1]), xf[k0], D0); PIN(D0);
      SBAR();
      if constexpr (u == 6 || n == 68 || n == 2) {
        if constexpr (n == 54 || n == 68) { UNROLL for (int e = 0; e < RA; ++e) { colsum(n + e, 0); colsum(n + e, 1); } cs_out(); }
        if (n == 2) wait_bar<7>(); else if (n <= 14) wait_bar<2>(); else if (n == 22) wait_bar<4>(); else if (n == 30) wait_bar<6>();
        else if (n == 38) { if (bq) wait_bar<10>(); else wait_bar<6>(); } else if (n == 46) wait_bar<4>(); else if (n == 54) wait_bar<5>();
        else if (n == 62) lbar(); else wait_bar<4>();
        if (n == 46 && wid == 0) flag_st(ws, WS_FQ + (mg * 16 + ng) * 128, nonce);
        if (n == 6) TS(2); if (n == 14) TS(3); if (n == 30) TS(4); if (n == 46) TS(5); if (n == 62) TS(6); if (n == 68) TS(7);
        SBAR();
      }
      constexpr bool d1f = u == 6 || n == 68 || n == 2;
      if constexpr (d1f) { D1 = MFMA(cat(wf[n % NWF][2], wf[n % NWF][3]), xf[k1], D1); PIN(D1); SBAR(); }
      if constexpr (n + RA < 70) rd_w(n + RA);
      if constexpr (n == 68) {
        const lptr qb = lds + L_Q + (wid & 1) * 4096 + ((wid >> 1) & 1) * 2048 + lane * 16, qa = lds + L_Q + (wid & 1) * 4096 + (((wid >> 1) & 1) ^ 1) * 2048 + lane * 16;
        kf0 = ld128(lds + lane * 16); QA0 = ld128(qa); QB0 = ld128(qb); kf1 = ld128(lds + 1024 + lane * 16); QA1 = ld128(qa + 1024); QB1 = ld128(qb + 1024);
        cfix = ld128(lds + L_CS + wn * 1024 + lane * 16);
      }
      if constexpr (n == 69) { sa = MFMA(kf1, QA1, sa); sb = MFMA(kf1, QB1, sb); PIN(sa); PIN(sb); }
      if constexpr (n == 2) rd_x(4, 8);
      if constexpr (u == 6 && n < 24) rd_x(n + 2, n + 6);
      if constexpr (u == 0 && n > 0 && n < 24) rd_x(n + 4, n + 8);
      if constexpr (!early) colsum(n, 0);
      SBAR();
      if constexpr (!d1f) { D1 = MFMA(cat(wf[n % NWF][2], wf[n % NWF][3]), xf[k1], D1); PIN(D1); }
      SBAR();
      if constexpr (!early) colsum(n, 1);
      if constexpr (n == 0) dma_w(IC<2>{}); if constexpr (n == 1) dma_x(IC<3>{}); if constexpr (n == 2) dma_w(IC<3>{}); if constexpr (n == 4) dma_w(IC<4>{});
      if constexpr (n == 6) dma_x(IC<4>{}); if constexpr (n == 7) dma_x(IC<5>{}); if constexpr (n == 8) dma_w(IC<5>{}); if constexpr (n == 10) dma_w(IC<6>{});
      if constexpr (n == 14) dma_x(IC<6>{}); if constexpr (n == 15) dma_w(IC<7>{}); if constexpr (n == 18) dma_w(IC<8>{}); if constexpr (n == 20) dma_w(IC<9>{});
      if constexpr (n == 23) dma_w(IC<10>{}); if constexpr (n == 26) dma_w(IC<11>{}); if constexpr (n == 29) dma_w(IC<12>{}); if constexpr (n == 32) dma_w(IC<13>{});
      if constexpr (n == 48) touch(SRD(b < 196 ? wd : wo, b < 196 ? 1792 * 896 : 448 * 896), lane < 8 ? (((b < 196 ? b : b - 196) * 8 + wid) * 8 + lane) * 128 : OOB, lds0 + L_PF + wid * 256);
      if constexpr (n == 17) asm volatile("buffer_load_dwordx2 %0, %1, %2, 0 offen sc1" : "+v"(sg) : "v"(sgo), "s"(wsr) : "memory");
      if constexpr (n == 16) asm volatile("global_load_dwordx4 %0, %2, %3\n\tglobal_load_dwordx4 %1, %2, %3 offset:32" : "+v"(tb0), "+v"(tb1) : "v"(h * 16), "s"(&qkv_tab.v[bq ? 4 * ng + 2 * wn : 0]) : "memory");
      if constexpr (n >= 39 && n < 53 && (n & 1)) dma_w(IC<14 + (n - 39) / 2>{});
      if constexpr (n == 27) cs_out();
      if constexpr (n == 23) {
        PIN(sg); PIN(tb0); PIN(tb1);
        _Pragma("nounroll") for (unsigned sp = 0; !ALL(sg[1] == nonce) && sp < SPIN_CAP; ++sp)
          asm volatile("buffer_load_dwordx2 %0, %1, %2, 0 offen sc1\n\ts_waitcnt vmcnt(0)" : "+v"(sg) : "v"(sgo), "s"(wsr) : "memory");
        sig = U2F(sg[0] & 0xffff0000u); rstd = __builtin_amdgcn_rcpf(sig); mm = __builtin_amdgcn_perm(sg[0], sg[0], 0x01000100u); kg = -1.4426950408889634f * rstd;
        PIN(sig); PIN(rstd); PIN(kg); PIN(mm);
      }
      if constexpr (n == 30) fix2(A0, A1);
      if constexpr (n == 31) epi1(A0, 0);
      if constexpr (n == 34) epi1(A1, 1);
      if constexpr (n == FQP) { fq_off(); asm volatile("buffer_load_dword %0, %1, %2, 0 offen sc1" : "+v"(fl0) : "v"(fqo), "s"(wsr) : "memory"); }
      if constexpr (n == 55) fix2(B0, B1);
      if constexpr (n == 57) { hp = glu8(B0); PIN(hp); }
      if constexpr (n == 64) { hq = glu8(B1); PIN(hq); }
      if constexpr (n == 69) { st128(hp, wsr, lane * 16, hs2, SC1); st128(hq, wsr, lane * 16 + 1024, hs2, SC1); }
      if constexpr (n == FQC) {
        asm volatile("s_waitcnt vmcnt(0)" : "+v"(fl0) :: "memory");
        _Pragma("nounroll") for (unsigned sp = 0; !ALL(fl0 == nonce) && sp < SPIN_CAP; ++sp)
          asm volatile("buffer_load_dword %0, %1, %2, 0 offen sc1\n\ts_waitcnt vmcnt(0)" : "+v"(fl0) : "v"(fqo), "s"(wsr) : "memory");
        kvv = (unsigned)(WS_QKV + head * QH + (wid >> 1) * 16 * QT + 2048 + (wid & 1) * 2048 + lane * 16);
        dma<0, L_Q, true>(wsr, WS_QKV + head * QH + (qg * 4 + (wid >> 1)) * QT + (wid & 1) * 1024 + lane * 16, lds0 + wid * 1024); dma_kv(IC<0>{}, kvv); }
      if constexpr (n > FQC && n < FQC + 8) dma_kv(IC<n - FQC>{}, kvv);
      if constexpr (n == 67) {
        const int vc = 2 * (grp ^ wn) + (p >> 1);
        kb = lds + (wid >> 1) * 4096 + lane * 16;
        vb0 = lds + (wid >> 1) * 4096 + 2048 + vc * 512 + ((((4 * h) ^ (4 * vc)) + q) << 4) + (p & 1) * 8;
        vb1 = lds + (wid >> 1) * 4096 + 2048 + vc * 512 + ((((4 * h + 8) ^ (4 * vc)) + q) << 4) + (p & 1) * 8;
        PIN(kb); PIN(vb0); PIN(vb1);
      }
      if constexpr (n == 68) { sa = MFMA(kf0, QA0, Z); sb = MFMA(kf0, QB0, Z); PIN(sa); PIN(sb); ka0 = ld128(kb); ka1 = ld128(kb + 1024); va0 = cat(trd(vb0), trd(vb1)); }
    });
    SBAR();
    C0 = MFMA(cfix, ((u32x4){mm, mm, mm, mm}), C0);
    float ma = sa[0], mb = sb[0];
    UNROLL for (int i = 1; i < 16; ++i) { ma = __builtin_fmaxf(ma, sa[i]); mb = __builtin_fmaxf(mb, sb[i]); }
    const unsigned pa = pk(1.f - max32(ma), 0.f), pb = pk(1.f - max32(mb), 0.f);
    const bool p0 = (head & 1) && h == 0, p1 = !(head & 1) && h == 1;
    QA0[0] = p0 ? pa : QA0[0]; QA1[2] = p1 ? pa : QA1[2]; QB0[0] = p0 ? pb : QB0[0]; QB1[2] = p1 ? pb : QB1[2];
    s0 = MFMA(ka0, QA0, Z); s0 = MFMA(ka1, QA1, s0);
    PIN(s0); PIN(QB0); PIN(QB1);
    SBAR();
    st128(glu8(C0), wsr, ng < 14 ? lane * 16 : OOB, WS_H + (mt * 112 + 64 + ng * 2 + wn) * 1024, SC1);
    SBAR();
    TS(8);
  }
  return phase23(x, wo, wd, ws, out, lds, nonce, QA0, QA1, QB0, QB1, ka0, ka1, va0, s0, kb, vb0, vb1, kvv, wid TSARG);
}

extern "C" void kernel_launch(void* const* d_in, const int* in_sizes, int n_in, void* d_out, int out_size, void* d_ws, size_t ws_size,
                              hipStream_t stream) {
  (void)hipFuncSetAttribute((const void*)fused_kernel, hipFuncAttributeMaxDynamicSharedMemorySize, LDS_BYTES);
  fused_kernel<<<256, 512, LDS_BYTES, stream>>>((const char*)d_in[0], (const char*)d_in[1], (const char*)d_in[2], (const char*)d_in[4], (const char*)d_in[5],
                                                (const char*)d_in[6], (char*)d_ws, (char*)d_out);
}
```

```cpp
#include <hip/hip_runtime.h>
#include <hip/hip_bf16.h>
#include <stdint.h>

typedef short bf16x8 __attribute__((ext_vector_type(8)));
typedef short s16x4 __attribute__((ext_vector_type(4)));
typedef float f32x16 __attribute__((ext_vector_type(16)));
typedef float f32x4 __attribute__((ext_vector_type(4)));
typedef float f32x2 __attribute__((ext_vector_type(2)));
typedef unsigned u32x4 __attribute__((ext_vector_type(4)));
typedef unsigned u32x2 __attribute__((ext_vector_type(2)));
typedef __bf16 bf2 __attribute__((ext_vector_type(2)));
typedef __amdgpu_buffer_rsrc_t srd_t;
#define LAS __attribute__((address_space(3)))
#define GAS __attribute__((address_space(1)))
typedef LAS char* lptr;
#define DI __device__ __forceinline__
#define AI __attribute__((always_inline))
#define MFMA(a, b, c) __builtin_amdgcn_mfma_f32_32x32x16_bf16(__builtin_bit_cast(bf16x8, (a)), __builtin_bit_cast(bf16x8, (b)), (c), 0, 0, 0)
#define PIN(x) asm volatile("" : "+v"(x))
#define SBAR() __builtin_amdgcn_sched_barrier(0)
#define RLX_AGENT __ATOMIC_RELAXED, __HIP_MEMORY_SCOPE_AGENT
#define EX(v) __builtin_amdgcn_exp2f(v)
#define F2U(x) __builtin_bit_cast(unsigned, (float)(x))
#define U2F(x) __builtin_bit_cast(float, (unsigned)(x))
#define ANY(p) (__builtin_amdgcn_ballot_w64(p) != 0)
#define ALL(p) (__builtin_amdgcn_ballot_w64(!(p)) == 0)
#define UNROLL _Pragma("unroll")
#define SRD(p, n) __builtin_amdgcn_make_buffer_rsrc((void*)(p), 0, (n), 0x00020000)

constexpr int WS_E = 0;
constexpr int WS_FQ = 4096;
constexpr int WS_FH = WS_FQ + 256 * 128;
constexpr int WS_FO = WS_FH + 256 * 128;
constexpr int WS_ST = WS_FO + 256 * 128;
constexpr int WS_QKV = 1 << 20;
constexpr int QT = 6144, QH = 64 * QT;
constexpr int WS_O = WS_QKV + 16 * QH;
constexpr int WS_H = WS_O + (2 << 20);
constexpr int WS_END = WS_H + 64 * 112 * 1024;
constexpr int OOB = 1 << 30;
constexpr int SC1 = 16;
constexpr unsigned SPIN_CAP = 1u << 20;

#define PADPT 1

#define DBG_TS
#define RTIME() ({ unsigned long long t_; asm volatile("s_memrealtime %0\n\ts_waitcnt lgkmcnt(0)" : "=s"(t_) :: "memory"); (unsigned)t_; })
#define TS(i) do { if ((i) == PADPT) tsv = RTIME(); } while (0)
#define TSR(i) do { } while (0)
#define TS_OUT() do { const unsigned tend = tsv + 6000; while ((int)(RTIME() - tend) < 0) __builtin_amdgcn_s_sleep(1); } while (0)

template <int I> struct IC { static constexpr int v = I; };
template <int B, int E, class F> DI void sfor(F&& f) { if constexpr (B < E) { f(IC<B>{}); sfor<B + 1, E>(f); } }
template <int N> DI void wait_bar() { asm volatile("s_waitcnt vmcnt(%0) lgkmcnt(0)\n\ts_barrier" :: "n"(N) : "memory"); }
template <int N> DI void vwait_bar() { asm volatile("s_waitcnt vmcnt(%0)\n\ts_barrier" :: "n"(N) : "memory"); }
DI void lbar() { asm volatile("s_waitcnt lgkmcnt(0)\n\ts_barrier" ::: "memory"); }
DI unsigned pk(float a, float b) { f32x2 v = {a, b}; return __builtin_bit_cast(unsigned, __builtin_convertvector(v, bf2)); }
DI float rbf(float a) { return U2F(pk(a, a) & 0xffff0000u); }
DI float dot2(unsigned a, unsigned b, float c) { return __builtin_amdgcn_fdot2_f32_bf16(__builtin_bit_cast(bf2, a), __builtin_bit_cast(bf2, b), c, false); }
DI u32x2 trd(lptr p) { return __builtin_bit_cast(u32x2, __builtin_amdgcn_ds_read_tr16_b64_v4i16((LAS s16x4*)p)); }
DI u32x4 cat(u32x2 a, u32x2 b) { return (u32x4){a[0], a[1], b[0], b[1]}; }
DI u32x4 ld128(lptr p) { return *(LAS u32x4*)p; }
DI unsigned bfi(unsigned m, unsigned a, unsigned b) { return (m & a) | (~m & b); }
template <int G, int L, bool DEV = false> DI void dma(srd_t srd, unsigned voff, unsigned lbase) {
  unsigned t;
  if constexpr (DEV)
    asm volatile("s_add_u32 m0, %3, %5\n\ts_mov_b32 %0, %4\n\tbuffer_load_dwordx4 %1, %2, %0 offen sc1 lds" : "=&s"(t) : "v"(voff), "s"(srd), "s"(lbase), "n"(G), "n"(L) : "memory", "scc");
  else
    asm volatile("s_add_u32 m0, %3, %5\n\ts_mov_b32 %0, %4\n\tbuffer_load_dwordx4 %1, %2, %0 offen lds" : "=&s"(t) : "v"(voff), "s"(srd), "s"(lbase), "n"(G), "n"(L) : "memory", "scc");
}
template <int L> DI void dma2(srd_t srd, unsigned voff0, unsigned voff1, unsigned lbase, unsigned ga, unsigned gb) {
  unsigned t;
  asm volatile("s_add_u32 m0, %4, %5\n\ts_mul_i32 %0, %6, %7\n\tbuffer_load_dwordx4 %1, %3, %0 offen lds\n\ts_add_u32 m0, m0, 1024\n\ts_nop 0\n\tbuffer_load_dwordx4 %2, %3, %0 offen lds"
               : "=&s"(t) : "v"(voff0), "v"(voff1), "s"(srd), "s"(lbase), "n"(L), "s"(ga), "s"(gb) : "memory", "scc");
}
template <int L> DI void dma1(srd_t srd, unsigned voff, unsigned lbase, unsigned ga, unsigned gb) {
  unsigned t;
  asm volatile("s_add_u32 m0, %3, %4\n\ts_mul_i32 %0, %5, %6\n\tbuffer_load_dwordx4 %1, %2, %0 offen lds" : "=&s"(t) : "v"(voff), "s"(srd), "s"(lbase), "n"(L), "s"(ga), "s"(gb) : "memory", "scc");
}
DI void touch(srd_t srd, unsigned voff, unsigned ldst) { asm volatile("s_mov_b32 m0, %2\n\ts_nop 0\n\tbuffer_load_dword %0, %1, 0 offen lds" :: "v"(voff), "s"(srd), "s"(ldst) : "memory"); }
DI void st128(u32x4 v, srd_t srd, int voff, int soff, int aux) { __builtin_amdgcn_raw_buffer_store_b128(v, srd, voff, soff, SC1); SBAR(); asm volatile("s_nop 1"); SBAR(); }
DI float swap32(float v, int h) {
  auto r = __builtin_amdgcn_permlane32_swap(F2U(v), F2U(v), false, false);
  return U2F(h ? r[0] : r[1]);
}
DI float max32(float v) {
  auto r = __builtin_amdgcn_permlane32_swap(F2U(v), F2U(v), false, false);
  return __builtin_fmaxf(U2F(r[0]), U2F(r[1]));
}
DI u32x4 widen(u32x2 a, u32x2 b) {
  auto r0 = __builtin_amdgcn_permlane32_swap(a[0], b[0], false, false); auto r1 = __builtin_amdgcn_permlane32_swap(a[1], b[1], false, false);
  return (u32x4){r0[0], r1[0], r0[1], r1[1]};
}
DI float wave_sum(float v) {
  v += U2F(__builtin_amdgcn_update_dpp(0u, F2U(v), 0x111, 0xf, 0xf, true));
  v += U2F(__builtin_amdgcn_update_dpp(0u, F2U(v), 0x112, 0xf, 0xf, true));
  v += U2F(__builtin_amdgcn_update_dpp(0u, F2U(v), 0x114, 0xf, 0xf, true));
  v += U2F(__builtin_amdgcn_update_dpp(0u, F2U(v), 0x118, 0xf, 0xf, true));
  v += U2F(__builtin_amdgcn_update_dpp(0u, F2U(v), 0x142, 0xa, 0xf, true));
  v += U2F(__builtin_amdgcn_update_dpp(0u, F2U(v), 0x143, 0xc, 0xf, true));
  return v;
}
DI unsigned flag_ld(const char* ws, int off) { return __hip_atomic_load((GAS unsigned*)(ws + off), RLX_AGENT); }
DI void flag_st(char* ws, int off, unsigned v) {
  if (__builtin_amdgcn_mbcnt_hi(~0u, __builtin_amdgcn_mbcnt_lo(~0u, 0u)) == 0) __hip_atomic_store((GAS unsigned*)(ws + off), v, RLX_AGENT); }

constexpr int RA = 2, NWF = RA + 1;
constexpr int PART = 14336;
constexpr int L_W = 65536;
constexpr int L_CS = 9 * 16384;
constexpr int L_PF = L_CS + 2048;
constexpr int L_Q = 65536;
constexpr int wpos(int m) { return m < 9 ? L_W + (m % 5) * 16384 : m == 9 ? 49152 : m == 10 ? L_W : m < 14 ? (m - 11) * 16384 : L_W + 16384 + (m - 14) * 8192; }
#define FQP 52
#define FQC 59
#define MAP 0
#define NG_OF(xcd, idx) ((xcd & 3) * 4 + (idx & 3))
#define MG_OF(xcd, idx) ((xcd >> 2) * 8 + (idx >> 2))
#define ROTX 0
constexpr int L_WD = 65536;
constexpr int LDS_BYTES = 163840;
constexpr unsigned ONES2 = 0x3f803f80u, PAD0 = 0x00003f80u;
static_assert(L_PF + 2048 <= LDS_BYTES, "LDS map");

constexpr int DN0 = 7;
constexpr int kv_at(int s, int p) { return p == 0 && s > 0 && s + 3 < 16 ? (s + 3) * 2 + 1 : p == 7 && s + 4 < 16 ? (s + 4) * 2 : -1; }
constexpr int wd_at(int s, int p) {
  if (s >= 1 && s <= 3) return p == 1 ? s * 4 - 4 : p == 3 ? s * 4 - 3 : p == 4 ? s * 4 - 2 : p == 5 ? s * 4 - 1 : -1;
  for (int g = 0; g < 2; ++g) { if (s == 10 + 4 * g && p == 7) return 12 + 4 * g; if (s == 11 + 4 * g) return p == 0 ? 13 + 4 * g : p == 2 ? 14 + 4 * g : p == 5 ? 15 + 4 * g : -1; }
  return -1;
}
constexpr int h_at(int s, int p) { const int z = (s - 5) * 3 + (p == 1 ? 1 : p == 3 ? 2 : 3); if (s == 15) return p == 1 ? 24 : p == 3 ? 25 : p == 4 ? 26 : p == 7 ? 27 : -1;
  return s == 4 && p == 7 ? 0 : s >= 5 && (p == 1 || p == 3 || p == 4) && z < 24 ? z : -1; }
constexpr bool pf_at(int s, int p) { return s == 4 && p == 1; }
constexpr int n_at(int s, int p) { return (kv_at(s, p) >= 0) + (wd_at(s, p) >= 0) + (h_at(s, p) >= 0) + pf_at(s, p); }
constexpr int cnt_after(int s0, int p0, int s) { int n = 0; for (int z = s0 * 8 + p0 + 1; z < s * 8 + 6; ++z) n += n_at(z / 8, z % 8); return n; }
constexpr int dslot(int sub) { return L_WD + (sub % 3) * 32768; }

struct QkvTab { unsigned v[44][2][4]; };
constexpr QkvTab make_tab() {
  QkvTab t = {};
  for (int tile = 0; tile < 44; ++tile) for (int h = 0; h < 2; ++h) {
    const int col0 = tile * 32, sec = col0 / 448; unsigned* e = t.v[tile][h];
    e[0] = e[1] = e[2] = OOB; e[3] = 0;
    if (tile < 42) for (int pp = 0; pp < 2; ++pp) {
      const int cu = (col0 - sec * 448) / 8 + 2 * pp + h, j = cu / 7, w = cu % 7, hd = 2 * j + (w > 3), ch = w > 3 ? w - 3 : w;
      e[pp] = WS_QKV + sec * 2048 + hd * QH + ch * 512 + (sec == 2 ? ch * 64 : 0);
      if (w == 3) { e[2] = WS_QKV + sec * 2048 + (hd + 1) * QH; e[3] |= 1u << pp; }
    }
  }
  return t;
}
__device__ const QkvTab qkv_tab = make_tab();

#define TSARG , tsv
#define TSPAR , unsigned tsv
#define LANE_IDS() int lane; asm volatile("v_mbcnt_lo_u32_b32 %0, -1, 0\n\tv_mbcnt_hi_u32_b32 %0, -1, %0" : "=v"(lane));        \
  const int r = lane & 31, h = lane >> 5, q = (lane & 15) >> 2, p = lane & 3, grp = (lane >> 4) & 1, rr = lane >> 3, cc = lane & 7
DI void phase23(const char* x, const char* wo, const char* wd, char* ws, char* out, lptr lds, unsigned nonce, u32x4 QA0, u32x4 QA1, u32x4 QB0, u32x4 QB1,
                u32x4 k0, u32x4 k1, u32x4 v0, f32x16 sa, lptr kb, lptr vb0, lptr vb1, unsigned kvv, int wid TSPAR) {
  const unsigned lds0 = (unsigned)(uintptr_t)lds;
  const int b = __builtin_amdgcn_workgroup_id_x(), xcd = b & 7, idx = b >> 3;
  const int ng = NG_OF(xcd, idx), mg = MG_OF(xcd, idx);
  const srd_t wsr = SRD(ws, WS_END);
  const f32x16 Z = {};
  const int head = 2 * xcd + (idx & 1), qg = idx >> 1, odd = head & 1;
  const int idx3 = idx < 28 ? idx : idx - 28, mq3 = (idx3 * 37) >> 8, mg3 = xcd * 4 + mq3, ng3 = idx3 - mq3 * 7;
  LANE_IDS();
  const unsigned lw2k = lds0 + wid * 2048;
  auto dma_kv = [&](auto zc) AI { constexpr int z = decltype(zc)::v; dma<(z >> 1) * QT + (z & 1) * 1024, ((z >> 1) & 3) * 16384 + (z & 1) * 1024, true>(wsr, kvv, lw2k); };
  const int w3m = wid & 1, w3k = wid >> 1;
  u32x4 hf[28];
  const int ft = w3k & 1, fs = w3k >> 1;
  unsigned vo_d = 0; const unsigned ld_d = lds0 + wid * 4096;
  auto set_vo_d = [&]() AI { vo_d = (unsigned)(((idx < 28 ? w3k * 448 + w3m * 32 : OOB / 896) + rr) * 896 + ((cc ^ (grp << 2)) << 4) + ng3 * 128); PIN(vo_d); };
  const srd_t sD = SRD(wd, 1792 * 896);
  auto dma_d = [&](auto zc) AI { constexpr int z = decltype(zc)::v; dma<((z >> 2) * 64 + (z & 3) * 8) * 896, dslot(z >> 2) + (z & 3) * 1024>(sD, vo_d, ld_d); };
  int db0 = 0, db1 = 0, dbo = L_WD;
  auto set_db = [&]() AI { db0 = L_WD + w3k * 8192 + (4 * h + q) * 128 + p * 8 + ((grp ^ ((q >> 1) << 1)) << 5); db1 = db0 ^ 64; PIN(db0); PIN(db1); };
  auto rebase = [&](int o) AI { db0 += o - dbo; db1 += o - dbo; dbo = o; PIN(db0); PIN(db1); };
  f32x16 F0 = Z, F1 = Z;
  u32x4 dw0, dw1;
  auto rd_d0 = [&](int d) AI { const int o = dslot(d >> 2) + (d & 3) * 2048 - dbo; dw0 = cat(trd(lds + db0 + o), trd(lds + db0 + o + 1024)); };
  auto rd_d1 = [&](int d) AI { const int o = dslot(d >> 2) + (d & 3) * 2048 - dbo; dw1 = cat(trd(lds + db1 + o), trd(lds + db1 + o + 1024)); };
  {
    const int wq = wid & 1, kq = wid >> 1;
    u32x4 v1, pa, pb;
    float e0, e1, e2, e3;
    f32x16 OA = Z, OB = Z, sb;
    unsigned junk, fh = ~nonce; const int fho = WS_FH + ((mg3 >> 1) * 16 + (lane & 15)) * 128;
    const int hld = WS_H + ((mg3 * 2 + w3m) * 112 + w3k * 28) * 1024, hlv = idx < 28 ? lane * 16 : OOB;
    auto send = [&](const f32x16& a, int hh, int j) AI {
      const lptr d = kb + (((wid ^ (2 * j)) * 6144 + (j - 1) * 2048) - kq * 4096);
      *(LAS f32x4*)d = (f32x4){a[8 * hh], a[8 * hh + 1], a[8 * hh + 2], a[8 * hh + 3]}; *(LAS f32x4*)(d + 1024) = (f32x4){a[8 * hh + 4], a[8 * hh + 5], a[8 * hh + 6], a[8 * hh + 7]}; };
    TS(10); TS(19);
#define EX2(S, i, a, c) do { a = EX(S[i]); c = EX(S[(i) + 1]); PIN(a); PIN(c); } while (0)
#define STEP(FIRST, SC, SN, QN0, QN1, OP, OC, L0, L1, L2, L3, X0, X1) do {                                                       \
      SBAR(); SN = MFMA(k0, QN0, Z); SBAR(); L0;                                                                          \
      if (!(FIRST)) { pb[2] = pk(e0, e1); pb[3] = pk(e2, e3); PIN(pb); } SBAR(); EX2(SC, 0, e0, e1); SBAR(); X0; SBAR(); EX2(SC, 2, e2, e3); \
      SBAR(); if (!(FIRST)) OP = MFMA(v1, pb, OP); SBAR(); L1;                                                                          \
      pa[0] = pk(e0, e1); pa[1] = pk(e2, e3); PIN(pa); SBAR(); EX2(SC, 4, e0, e1); EX2(SC, 6, e2, e3);                    \
      SBAR(); SN = MFMA(k1, QN1, SN); SBAR(); L2;                                                                         \
      pa[2] = pk(e0, e1); pa[3] = pk(e2, e3); PIN(pa); SBAR(); EX2(SC, 8, e0, e1); SBAR(); X1; SBAR(); EX2(SC, 10, e2, e3); \
      SBAR(); OC = MFMA(v0, pa, OC); SBAR(); L3;                                                                          \
      pb[0] = pk(e0, e1); pb[1] = pk(e2, e3); PIN(pb); SBAR(); EX2(SC, 12, e0, e1); EX2(SC, 14, e2, e3); SBAR(); } while (0)
    sfor<0, 16>([&](auto sc_) AI {
      constexpr int s = decltype(sc_)::v, so = (s & 3) * 16384, sn = ((s + 1) & 3) * 16384;
      auto side = [&](auto pc) AI {
        constexpr int P = decltype(pc)::v, kv = kv_at(s, P), wz = wd_at(s, P), hz = h_at(s, P);
        if constexpr (s == 0 && P == 3) set_vo_d();
        if constexpr (s == 0 && P == 5) set_db();
        if constexpr (kv >= 0) dma_kv(IC<kv>{});
        if constexpr (wz >= 0) dma_d(IC<wz>{});
        if constexpr (pf_at(s, P)) {
          const int row = wid < 2 ? mq3 * 448 + 320 + wid * 64 + lane : mq3 * 112 + (wid - 2) * 56 + (lane < 56 ? lane : 55);
          asm volatile("buffer_load_dword %0, %1, %2, 0 offen" : "=&v"(junk) : "v"(wid < 4 && idx < 28 ? row * 896 + ng3 * 128 : OOB), "s"(SRD(wid < 2 ? wd : wo, wid < 2 ? 1792 * 896 : 448 * 896)) : "memory");
        }
        if constexpr (hz >= 0) hf[hz] = __builtin_amdgcn_raw_buffer_load_b128(wsr, hlv + (hz & 3) * 1024, hld + (hz >> 2) * 4096, SC1);
      };
      auto mid = [&]() AI {
        if (s == 4 && wid == 0) {
          asm volatile("s_waitcnt vmcnt(%1)" : "+v"(fh) : "n"(cnt_after(3, 6, 4)) : "memory");
          _Pragma("nounroll") for (unsigned sp = 0; !ALL(fh == nonce) && sp < SPIN_CAP; ++sp) { fh = flag_ld(ws, fho); PIN(fh); }
        }
        if constexpr (s == 0) vwait_bar<5 + cnt_after(0, -1, 0)>();
        else if constexpr (s == 1) vwait_bar<cnt_after(0, -1, 1)>();
        else if constexpr (s == 14) vwait_bar<0>(); else if constexpr (s < 14) vwait_bar<cnt_after(s - 1, 0, s)>();
        if (s == 1 && wid == 0) flag_st(ws, WS_FH + (mg * 16 + ng) * 128, nonce);
        if (s == 3 && wid == 0) asm volatile("buffer_load_dword %0, %1, %2, 0 offen sc1" : "+v"(fh) : "v"(fho), "s"(wsr) : "memory");
        if constexpr (s == 6) asm volatile("" :: "v"(junk));
        if constexpr (s == 13) rebase(L_WD + 65536);
        if constexpr (s == 14) { UNROLL for (int k = 23; k >= 8; --k) PIN(hf[k]); }
        if (s == 1) TS(20); if (s == 3) TS(21); if (s == 5) TS(22); if (s == 7) TS(23); if (s == 9) TS(24); if (s == 11) TS(25); if (s == 13) TS(26);
        __builtin_amdgcn_s_setprio(3);
      };
      auto dn0 = [&](int d) AI { if (d >= 0) { F0 = MFMA(dw0, hf[d], F0); PIN(F0); } if (d >= -1) rd_d0(d + 1); };
      auto dn1 = [&](int d) AI { if (d >= 0) { F1 = MFMA(dw1, hf[d], F1); PIN(F1); } if (d >= -1) rd_d1(d + 1); };
      STEP(s == 0, sa, sb, QB0, QB1, OB, OA,
           k0 = ld128(kb + sn); side(IC<0>{}); __builtin_amdgcn_s_setprio(2),
           v1 = cat(trd(vb0 + so + 256), trd(vb1 + so + 256)); side(IC<1>{}),
           k1 = ld128(kb + sn + 1024); side(IC<2>{}); __builtin_amdgcn_s_setprio(1),
           side(IC<3>{}), dn0(s - DN0), if (s == 15) dn1(8));
      STEP(false, sb, sa, QA0, QA1, OA, OB,
           __builtin_amdgcn_s_setprio(0); side(IC<4>{}),
           side(IC<5>{}),
           mid(); if (s == 15) send(OA, 0, 1),
           v0 = cat(trd(vb0 + sn), trd(vb1 + sn)); side(IC<7>{}); if (s == 15) send(OA, 1, 3), if (s < 15) dn1(s - DN0); else dn0(9), if (s == 15) dn1(9));
    });
    pb[2] = pk(e0, e1); pb[3] = pk(e2, e3);
    OB = MFMA(v1, pb, OB);
    __builtin_amdgcn_s_setprio(0);
    TS(11);
    send(OB, 1, 2);
    *(LAS float*)(kb + (49152 + wq * 1024)) = odd ? OB[0] : OB[4];
    {
    LANE_IDS();
    const bool padl = h != odd;
    const int qt = qg * 4 + wq * 2 + ft, lw = (wid & 3) | (odd ? 0 : 4);
    const lptr pp = lds + wid * 6144 + lane * 16, lp = lds + lw * 6144 + (odd ? 0 : 1536) + r * 16;
    const lptr lq = lds + 49152 + (lw >> 1) * 4096 + wq * 1024 + (odd ? 0 : 512) + r * 16;
    f32x4 o0 = {OB[0], OB[1], OB[2], OB[3]}, o1 = {OB[4], OB[5], OB[6], OB[7]};
    lbar();
    const float l0 = *(LAS float*)lq, l1 = *(LAS float*)lp, l2 = *(LAS float*)(lp + 2048), l3 = *(LAS float*)(lp + 4096);
    f32x4 t[6];
    UNROLL for (int k = 0; k < 6; ++k) t[k] = *(LAS f32x4*)(pp + k * 1024);
    SBAR();
    float l = (l0 + l1) + (l2 + l3);
    UNROLL for (int k = 0; k < 3; ++k) { o0 += t[2 * k]; o1 += t[2 * k + 1]; }
    if (__builtin_expect(ANY(!(l <= 1.8446744e19f && l >= 5.4210109e-20f)), 0)) {
      const int qo = WS_QKV + head * QH + qt * QT + lane * 16;
      u32x4 q0 = __builtin_amdgcn_raw_buffer_load_b128(wsr, qo, 0, SC1), q1 = __builtin_amdgcn_raw_buffer_load_b128(wsr, qo + 1024, 0, SC1);
      f32x16 O = Z; float m = -__builtin_inff();
      _Pragma("nounroll") for (int t = 0; t < 64; ++t) {
        const int ko = WS_QKV + head * QH + t * QT + 2048;
        f32x16 s = MFMA(__builtin_amdgcn_raw_buffer_load_b128(wsr, ko + lane * 16, 0, SC1), q0, Z);
        s = MFMA(__builtin_amdgcn_raw_buffer_load_b128(wsr, ko + 1024 + lane * 16, 0, SC1), q1, s);
        float mn = m;
        UNROLL for (int i = 0; i < 16; ++i) mn = __builtin_fmaxf(mn, s[i]);
        mn = max32(mn);
        const float f = EX(m - mn); m = mn;
        UNROLL for (int i = 0; i < 16; ++i) O[i] *= f;
        UNROLL for (int st = 0; st < 2; ++st) {
          unsigned vv[8];
          UNROLL for (int j = 0; j < 8; ++j) vv[j] = __builtin_amdgcn_raw_buffer_load_b16(wsr, ko + 2048 + (r >> 3) * 512 + (((16 * st + 8 * (j >> 2) + 4 * h + (j & 3)) ^ (4 * (r >> 3))) << 4) + (r & 7) * 2, 0, SC1) & 0xffffu;
          O = MFMA(((u32x4){vv[0] | vv[1] << 16, vv[2] | vv[3] << 16, vv[4] | vv[5] << 16, vv[6] | vv[7] << 16}),
                   ((u32x4){pk(EX(s[8 * st] - m), EX(s[8 * st + 1] - m)), pk(EX(s[8 * st + 2] - m), EX(s[8 * st + 3] - m)),
                            pk(EX(s[8 * st + 4] - m), EX(s[8 * st + 5] - m)), pk(EX(s[8 * st + 6] - m), EX(s[8 * st + 7] - m))}), O);
        }
      }
      const float lc = odd ? O[0] : O[12], lx = swap32(lc, h); l = padl ? lc : lx;
      o0 = fs ? (f32x4){O[8], O[9], O[10], O[11]} : (f32x4){O[0], O[1], O[2], O[3]};
      o1 = fs ? (f32x4){O[12], O[13], O[14], O[15]} : (f32x4){O[4], O[5], O[6], O[7]};
    }
    const float ri = __builtin_amdgcn_rcpf(l);
    const float r0 = padl && odd && fs == 0 ? 0.f : ri, r1 = padl && !odd && fs == 1 ? 0.f : ri;
    st128((u32x4){pk(o0[0] * r0, o0[1] * r0), pk(o0[2] * r0, o0[3] * r0), pk(o1[0] * r1, o1[1] * r1), pk(o1[2] * r1, o1[3] * r1)},
                                           wsr, lane * 16, WS_O + (qt * 16 + head) * 2048 + fs * 1024, SC1);
    }
  }
  TS(12);
  if (idx >= 28) { wait_bar<0>();
    if (wid == 0) {
      flag_st(ws, WS_FO + (qg * 16 + head) * 128, nonce);
      if (b == 255) {
        LANE_IDS();
        _Pragma("nounroll") for (unsigned sp = 0; sp < SPIN_CAP; ++sp) {
          unsigned d = 0;
          UNROLL for (int i = 0; i < 4; ++i) d |= flag_ld(ws, WS_FQ + lane * 512 + i * 128) ^ nonce;
          if (ALL(d == 0)) break;
        }
        flag_st(ws, WS_E, nonce);
      }
    }
    return;
  }
  {
    lbar();
    const srd_t sO = SRD(wo, 448 * 896); const unsigned wov = vo_d + (wid * 56 - w3k * 448 - w3m * 32) * 896, wol = lds0 + wid * 7168;
    auto dma_o = [&](auto ic) AI { constexpr int i = decltype(ic)::v; dma<i * 8 * 896, i * 1024>(sO, wov, wol); };
    sfor<0, 4>(dma_o);
    u32x4 ew0, ew1;
    auto down2 = [&](int d, bool more) AI {
      const int o = dslot((d + 1) >> 2) + ((d + 1) & 3) * 2048 - dbo;
      ew0 = cat(trd(lds + db0 + o), trd(lds + db0 + o + 1024)); ew1 = cat(trd(lds + db1 + o), trd(lds + db1 + o + 1024));
      if (d == 10 || d == 22) rebase(L_WD);
      SBAR(); F0 = MFMA(dw0, hf[d], F0); F1 = MFMA(dw1, hf[d], F1); PIN(F0); PIN(F1); SBAR();
      if (more) { rd_d0(d + 2); rd_d1(d + 2); }
      SBAR(); F0 = MFMA(ew0, hf[d + 1], F0); F1 = MFMA(ew1, hf[d + 1], F1); PIN(F0); PIN(F1); SBAR();
    };
    static_assert(DN0 == 7, "units 0..9 ran beside the attention");
    down2(10, true); sfor<4, 7>(dma_o);
    vwait_bar<7>();
    if (wid == 0) flag_st(ws, WS_FO + (qg * 16 + head) * 128, nonce);
    TS(13);
    sfor<20, 22>(dma_d); down2(12, true); sfor<22, 24>(dma_d); down2(14, true);
    vwait_bar<4>();
    LANE_IDS();
    const int mt = mg3 * 2 + w3m, orot = ng3 & 1;
    unsigned fo = ~nonce; const int foo = WS_FO + ((mg3 >> 1) * 16 + (lane & 15)) * 128;
    sfor<24, 26>(dma_d); down2(16, true); sfor<26, 28>(dma_d);
    if (wid == 0) asm volatile("buffer_load_dword %0, %1, %2, 0 offen sc1" : "+v"(fo) : "v"(foo), "s"(wsr) : "memory");
    down2(18, false);
    u32x4 wo0[8], wo1[8];
    UNROLL for (int k = 0; k < 8; ++k) {
      const int wbo = w3k * (PART - 8192) - L_WD + ((k >> 2) ^ orot) * 7168, o = (((k >> 1) & 1) * 24 + (k & 1) * 16) * 128;
      const lptr wb0 = lds + db0 + wbo, wb1 = lds + db1 + wbo;
      wo0[k] = cat(trd(wb0 + o), trd(wb0 + o + 1024)); wo1[k] = cat(trd(wb1 + o), trd(wb1 + o + 1024));
    }
    rebase(L_WD + 65536);
    TS(14);
    if (wid == 0) {
      asm volatile("s_waitcnt vmcnt(0)" : "+v"(fo) :: "memory");
      _Pragma("nounroll") for (unsigned sp = 0; !ALL(fo == nonce) && sp < SPIN_CAP; ++sp) { fo = flag_ld(ws, foo); PIN(fo); }
    }
    wait_bar<0>();
    UNROLL for (int k = 27; k >= 24; --k) PIN(hf[k]);
    TS(15);
    u32x4 of[8];
    const int oso = WS_O + (mt * 16 + w3k * 4) * 2048 + orot * 4096;
    UNROLL for (int k = 0; k < 4; ++k) of[k] = __builtin_amdgcn_raw_buffer_load_b128(wsr, lane * 16 + k * 1024, oso, SC1);
    SBAR();
    rd_d0(20); rd_d1(20); down2(20, true); down2(22, true);
    UNROLL for (int k = 4; k < 8; ++k) of[k] = __builtin_amdgcn_raw_buffer_load_b128(wsr, lane * 16 + (k & 3) * 1024, oso ^ 4096, SC1);
    const unsigned xo = (unsigned)((mt * 32 + w3k * 8 + rr) * 896 + ng3 * 128 + cc * 16);
    const u32x4 xv = *(const u32x4*)(x + xo);
    SBAR();
    down2(24, true); down2(26, false);
    auto xch_send = [&](const f32x16& a0, const f32x16& a1, int base) AI {
      UNROLL for (int c = 0; c < 8; ++c) { const f32x16& a = c < 4 ? a0 : a1; const int o = (c & 3) * 4;
        *(LAS f32x4*)(lds + (base + wid * 8192 + c * 1024) + ((lane ^ (2 * c)) << 4)) = (f32x4){a[o], a[o + 1], a[o + 2], a[o + 3]}; } };
    int xro = w3m * 8192 + cc * 1024 + (((w3k * 8 + rr) ^ (2 * cc)) << 4); PIN(xro);
    xch_send(F0, F1, 0);
    f32x16 S0 = Z, S1 = Z;
    UNROLL for (int k = 0; k < 4; ++k) { S0 = MFMA(wo0[k], of[k], S0); S1 = MFMA(wo1[k], of[k], S1); }
    PIN(S0); PIN(S1);
    lbar();
    f32x4 g0 = *(LAS f32x4*)(lds + xro), g1 = *(LAS f32x4*)(lds + xro + 512);
    UNROLL for (int k = 1; k < 4; ++k) { g0 += *(LAS f32x4*)(lds + xro + k * 16384); g1 += *(LAS f32x4*)(lds + xro + k * 16384 + 512); }
    UNROLL for (int k = 4; k < 8; ++k) { S0 = MFMA(wo0[k], of[k], S0); S1 = MFMA(wo1[k], of[k], S1); }
    UNROLL for (int i = 0; i < 4; ++i) {
      g0[i] = g0[i] * __builtin_amdgcn_rcpf(1.f + EX(g0[i] * -1.4426950408889634f)); g1[i] = g1[i] * __builtin_amdgcn_rcpf(1.f + EX(g1[i] * -1.4426950408889634f)); }
    TS(16);
    xch_send(S0, S1, L_WD);
    f32x4 y0 = {U2F(xv[0] << 16), U2F(xv[0] & 0xffff0000u), U2F(xv[1] << 16), U2F(xv[1] & 0xffff0000u)}, y1 = {U2F(xv[2] << 16), U2F(xv[2] & 0xffff0000u), U2F(xv[3] << 16), U2F(xv[3] & 0xffff0000u)};
    xro += L_WD;
    PIN(g0); PIN(g1); PIN(y0); PIN(y1); PIN(xro);
    lbar();
    f32x4 t[8];
    UNROLL for (int k = 0; k < 8; ++k) t[k] = *(LAS f32x4*)(lds + xro + (k >> 1) * 16384 + (k & 1) * 512);
    SBAR();
    UNROLL for (int k = 0; k < 4; ++k) UNROLL for (int i = 0; i < 4; ++i) { y0[i] = __builtin_fmaf(t[2 * k][i], g0[i], y0[i]); y1[i] = __builtin_fmaf(t[2 * k + 1][i], g1[i], y1[i]); }
    __builtin_amdgcn_raw_buffer_store_b128((u32x4){pk(y0[0], y0[1]), pk(y0[2], y0[3]), pk(y1[0], y1[1]), pk(y1[2], y1[3])}, SRD(out, 2048 * 896), xo, 0, SC1);
    TS(17); TS_OUT();
  }
}

#if defined(__HIP_DEVICE_COMPILE__)
#define NOPK __attribute__((target("no-packed-fp32-ops")))
#else
#define NOPK
#endif
__global__ __launch_bounds__(512, 2) NOPK void fused_kernel(const char* x, const char* wqkv, const char* wo, const char* wg, const char* wu, const char* wd,
                                                       char* ws, char* out) {
  extern __shared__ __attribute__((aligned(16))) char smem[];
  const lptr lds = (lptr)smem;
  const unsigned lds0 = (unsigned)(uintptr_t)lds;
  const int tid = __builtin_amdgcn_workitem_id_x(), lane = tid & 63, wid = __builtin_amdgcn_readfirstlane(tid >> 6);
  const int rr = lane >> 3, cc = lane & 7;
  const int b = __builtin_amdgcn_workgroup_id_x(), xcd = b & 7, idx = b >> 3;
  const int ng = NG_OF(xcd, idx), mg = MG_OF(xcd, idx);
  unsigned tsv = 0; TSR(62); TS(0);
  const int rot0 = MAP == 2 ? (mg & 3) * 2 + ((mg >> 2) & 1) : (mg + ROTX * (mg >> 3)) & 7, rot = rot0 == 7 ? 3 : rot0;
  unsigned kc[7];
  UNROLL for (int c = 0; c < 7; ++c) kc[c] = (unsigned)((c + rot >= 7 ? c + rot - 7 : c + rot) * 64);
  const srd_t xr = SRD(x, 2048 * 896);
  const unsigned xv0 = (unsigned)((mg * 128 + wid * 16 + rr) * 896 + ((cc ^ rr) << 4));
  const unsigned xv1 = (unsigned)((mg * 128 + wid * 16 + 8 + (rr ^ 1)) * 896 + ((cc ^ rr ^ 1) << 4));
  const unsigned lw2k = lds0 + wid * 2048;
  auto dma_x = [&](auto cc_) AI { constexpr int c = decltype(cc_)::v; dma2<(c & 3) * 16384>(xr, xv0, xv1, lw2k, kc[c], 2); };
  const srd_t wsr = SRD(ws, WS_END);
  u32x4 srow; unsigned nonce;
  const int srw = mg * 128 + ng * 8 + wid;
  asm volatile("buffer_load_dwordx4 %0, %1, %2, %3 offen" : "=&v"(srow) : "v"(lane * 16), "s"(SRD(x, srw * 896 + 896)), "s"(srw * 896) : "memory");
  dma_x(IC<0>{});
  SBAR();
  const int r = lane & 31, h = lane >> 5, q = (lane & 15) >> 2, p = lane & 3, grp = (lane >> 4) & 1;
  const f32x16 Z = {};
  const int head = 2 * xcd + (idx & 1), qg = idx >> 1;
  auto dma_kv = [&](auto zc, unsigned kvv) AI { constexpr int z = decltype(zc)::v; dma<(z >> 1) * QT + (z & 1) * 1024, ((z >> 1) & 3) * 16384 + (z & 1) * 1024, true>(wsr, kvv, lw2k); };
  u32x4 QA0, QA1, QB0, QB1, kf0, kf1, ka0, ka1, va0; f32x16 s0; lptr kb, vb0, vb1; unsigned kvv = 0;
  {
    const int wm = wid & 3, wn = wid >> 2;
    const bool bq = ng <= 10;
    const unsigned st1 = bq ? 2688 : 3584;
    unsigned va = (16 * wm + rr) * st1 + (((cc << 4) ^ (grp << (bq ? 6 : 5))) ^ (bq ? 0 : wn << 6)) + (bq ? 2 * ng + wn : ng + 12) * 128, vb = va + 8 * st1;
    const char* wgu = wn ? wu : wg;
    const srd_t s2 = SRD(wgu, 448 * 3584), s1 = SRD(bq ? wqkv : wgu, 448 * st1);
    auto dma_w = [&](auto mc) AI {
      constexpr int m = decltype(mc)::v;
      if constexpr (m == 7) { va = (unsigned)((16 * wm + rr) * 3584 + ((cc ^ (grp << 1) ^ (wn << 2)) << 4)) + ng * 128; vb = va + 8 * 3584; PIN(va); PIN(vb); }
      if constexpr (m == 14) {
        va = (unsigned)((16 * wm + (lane >> 2)) * 3584 + (((lane & 3) ^ (wn << 1)) << 4)) + 2048 + ng * 64;
        if (ng >= 14) va = OOB;
        PIN(va);
      }
      if constexpr (m < 7) dma2<wpos(m)>(s1, va, vb, lw2k, kc[m], st1);
      else if constexpr (m < 14) dma2<wpos(m)>(s2, va, vb, lw2k, kc[m - 7], 3584);
      else dma1<wpos(m)>(s2, va, lds0 + wid * 1024, kc[m - 14], 3584);
    };
    dma_w(IC<0>{});
    unsigned epoch;
    asm volatile("buffer_load_dword %0, off, %1, 0 sc1" : "=&v"(epoch) : "s"(wsr) : "memory");
    dma_x(IC<1>{});
    {
      unsigned long long pc; asm volatile("s_getpc_b64 %0" : "=s"(pc));
      if (wid == 7) touch(SRD(pc & ~127ull, 28672), lane < 8 ? (idx * 8 + lane) * 128 : OOB, lds0 + L_PF);
    }
    SBAR();
    const int rowb = (8 * h + q) * 128 + p * 8;
    int b20 = grp * 8192 + rowb + (((2 * wn) ^ (q >> 1) ^ (grp << 1)) << 5);
    int b10 = bq ? wn * 8192 + rowb + ((grp ^ ((q >> 1) << 1)) << 5) : b20, b11 = b10 ^ (bq ? 64 : 32);
    int xb[4];
    UNROLL for (int i = 0; i < 4; ++i) { xb[i] = (4 * wm + (r >> 3)) * 1024 + ((r & 7) ^ ((r >> 3) & 1)) * 128 + (((2 * i + h) ^ (r & 7)) << 4); PIN(xb[i]); }
    PIN(b10); PIN(b11); PIN(b20);
    UNROLL for (int c = 2; c < 7; ++c) asm volatile("" : "+s"(kc[c]));
    SBAR();
    u32x4 xf[28];
    auto row_stats = [&]() AI {
      float a = dot2(srow[0], ONES2, 0.f), c = dot2(srow[0], srow[0], 0.f);
      UNROLL for (int i = 1; i < 4; ++i) { a = dot2(srow[i], ONES2, a); c = dot2(srow[i], srow[i], c); }
      a = wave_sum(a); c = wave_sum(c);
      const float mean = a * (1.f / 448.f), var = __builtin_fmaxf(c * (1.f / 448.f) - mean * mean, 0.f);
      unsigned g = pk(-mean, __builtin_amdgcn_sqrtf(rbf(rbf(var) + 1e-5f))); PIN(g); SBAR();
      asm volatile("s_waitcnt vmcnt(4)" : "+v"(epoch) :: "memory");
      nonce = __builtin_amdgcn_readfirstlane(epoch) * 0x9E3779B1u + 0x7F4A7C15u;
      if (lane == 63) __hip_atomic_store((GAS unsigned long long*)(ws + WS_ST + srw * 8), (unsigned long long)nonce << 32 | g, RLX_AGENT);
    };
    auto rd_x = [&](int k0, int k1) AI { UNROLL for (int ks = k0; ks < k1; ++ks) xf[ks] = ld128(lds + xb[ks & 3] + ((ks >> 2) & 3) * 16384); };
    u32x2 wf[NWF][4];
    int ra0 = 0, ra1 = 0;
    auto rd_w = [&](int n) AI {
      if (n < 56) {
        const int m = n >> 2, o = (n & 3) * 2048;
        if ((n & 3) == 0) { ra0 = (m < 7 ? b10 : b20) + wpos(m); ra1 = (m < 7 ? b11 : b20 ^ 32) + wpos(m); PIN(ra0); PIN(ra1); }
        wf[n % NWF][0] = trd(lds + ra0 + o); wf[n % NWF][1] = trd(lds + ra0 + o + 512); wf[n % NWF][2] = trd(lds + ra1 + o); wf[n % NWF][3] = trd(lds + ra1 + o + 512);
      } else {
        const int o = ((n - 56) >> 1) * 8192 + (n & 1) * 2048;
        if (n == 56) { ra0 = wpos(14) + grp * 4096 + (8 * h + q) * 64 + ((wn ^ grp) << 5) + p * 8; PIN(ra0); }
        wf[n % NWF][0] = trd(lds + ra0 + o); wf[n % NWF][1] = trd(lds + ra0 + o + 256); wf[n % NWF][2] = trd(lds + ra0 + o + 1024); wf[n % NWF][3] = trd(lds + ra0 + o + 1280);
      }
    };
    SBAR();
    dma_w(IC<1>{});
    asm volatile("s_waitcnt vmcnt(9)" : "+v"(srow) :: "memory");
    row_stats();
    SBAR();
    lbar();
    TS(1);
    UNROLL for (int n = 0; n < RA; ++n) rd_w(n);
    rd_x(0, 4);
    dma_x(IC<2>{});
    f32x16 A0 = Z, A1 = Z, B0 = Z, B1 = Z, C0 = Z;
    float cs0 = 0.f, cs1 = 0.f, rstd = 0.f, sig = 0.f, kg = 0.f; unsigned mm = 0; u32x2 sg = {0u, ~nonce};
    const int mt = mg * 4 + wm;
    const unsigned csel = ((wm & 1) ? 0x05040c0cu : 0x01000c0cu) + ((wm >> 1) ? 0x02020000u : 0u);
    auto colsum = [&](int n, int hh) AI {
      cs0 += U2F(__builtin_amdgcn_perm(wf[n % NWF][2 + hh][0], wf[n % NWF][hh][0], csel)); cs1 += U2F(__builtin_amdgcn_perm(wf[n % NWF][2 + hh][1], wf[n % NWF][hh][1], csel));
      PIN(cs0); PIN(cs1);
    };
    auto cs_out = [&]() AI {
      const float cs = cs0 + cs1, hi = rbf(cs); *(LAS unsigned*)(lds + L_CS + wn * 1024 + lane * 16 + ((wm & 1) * 2 + (wm >> 1)) * 4) = pk(hi, cs - hi); cs0 = 0.f; cs1 = 0.f; };
    auto fix2 = [&](f32x16& a0, f32x16& a1) AI {
      const u32x4 c = ld128(lds + L_CS + wn * 1024 + lane * 16);
      a0 = MFMA(c, ((u32x4){mm, mm, 0u, 0u}), a0); a1 = MFMA(c, ((u32x4){0u, 0u, mm, mm}), a1); PIN(a0); PIN(a1); };
    auto glu = [&](const f32x16& acc, int i) AI { return acc[i + 8] * __builtin_amdgcn_rcpf(__builtin_fmaf(EX(acc[i] * kg), sig, sig)); };
    auto glu8 = [&](const f32x16& acc) AI { return (u32x4){pk(glu(acc, 0), glu(acc, 1)), pk(glu(acc, 2), glu(acc, 3)), pk(glu(acc, 4), glu(acc, 5)), pk(glu(acc, 6), glu(acc, 7))}; };
    const int hs2 = WS_H + (mt * 112 + ng * 4 + wn * 2) * 1024;
    u32x4 tb0 = {0u, 0u, 0u, 0u}, tb1 = tb0;
    auto epi1 = [&](const f32x16& acc, int t) AI {
      if (bq) {
        const u32x4 e = t ? tb1 : tb0; const unsigned o[3] = {e[0] ^ (r << 4), e[1] ^ (r << 4), e[2] ^ (r << 4)};
        const unsigned m[2] = {(unsigned)__builtin_amdgcn_sbfe(e[3], 0, 1), (unsigned)__builtin_amdgcn_sbfe(e[3], 1, 1)};
        const float sc = 2 * ng + wn < 7 ? rstd * (0.18898223650461362f * 1.4426950408889634f) : rstd;
        u32x4 v[2];
        UNROLL for (int pp = 0; pp < 2; ++pp)
          v[pp] = widen((u32x2){pk(acc[8 * pp] * sc, acc[8 * pp + 1] * sc), pk(acc[8 * pp + 2] * sc, acc[8 * pp + 3] * sc)},
                        (u32x2){pk(acc[8 * pp + 4] * sc, acc[8 * pp + 5] * sc), pk(acc[8 * pp + 6] * sc, acc[8 * pp + 7] * sc)});
        u32x4 d0 = {v[0][0], v[0][1], bfi(m[0], PAD0, v[0][2]), ~m[0] & v[0][3]}, d1 = {v[1][0], v[1][1], bfi(m[1], PAD0, v[1][2]), ~m[1] & v[1][3]};
        u32x4 d2 = {PAD0, 0u, bfi(m[0], v[0][2], v[1][2]), bfi(m[0], v[0][3], v[1][3])};
        PIN(d0); PIN(d1); PIN(d2); SBAR();
        __builtin_amdgcn_raw_buffer_store_b128(d0, wsr, o[0], mt * QT, SC1); __builtin_amdgcn_raw_buffer_store_b128(d1, wsr, o[1], mt * QT, SC1);
        st128(d2, wsr, o[2], mt * QT, SC1);
      } else st128(glu8(acc), wsr, lane * 16 + t * 1024, hs2 + 12 * 4096, SC1);
    };
    u32x4 hp, hq; unsigned fl0 = ~nonce; f32x16 sa, sb; u32x4 cfix;
    int fqo = 0;
    auto fq_off = [&]() AI {
      const int c0 = (lane < 8 ? 448 + (wid & 1) * 448 : 0) + head * 28 - (head & 1) * 4 + (lane & 1) * 31, g = c0 >> 7, m2 = lane < 8 ? (wid >> 1) * 4 + (lane >> 1) : qg;
      fqo = WS_FQ + (m2 * 16 + g) * 128; };
    const int sgo = WS_ST + (mg * 128 + wm * 32 + r) * 8;

    sfor<0, 70>([&](auto nc) AI {
      constexpr int n = decltype(nc)::v, u = n & 7;
      constexpr int k0 = n < 28 ? n : n < 56 ? n - 28 : 2 * (n - 56), k1 = n < 56 ? k0 : k0 + 1;
      f32x16& D0 = n < 28 ? A0 : n < 56 ? B0 : C0; f32x16& D1 = n < 28 ? A1 : n < 56 ? B1 : C0;
      constexpr bool early = n == 54 || n == 55 || n >= 68;
      PIN(wf[n % NWF][3]);
      if (u == 7) __builtin_amdgcn_s_setprio(3); if (u == 1) __builtin_amdgcn_s_setprio(2); if (u == 3) __builtin_amdgcn_s_setprio(1); if (u == 5) __builtin_amdgcn_s_setprio(0);
      SBAR();
      D0 = MFMA(cat(wf[n % NWF][0], wf[n % NWF][1]), xf[k0], D0); PIN(D0);
      SBAR();
      if constexpr (u == 6 || n == 68 || n == 2) {
        if constexpr (n == 54 || n == 68) { UNROLL for (int e = 0; e < RA; ++e) { colsum(n + e, 0); colsum(n + e, 1); } cs_out(); }
        if (n == 2) wait_bar<7>(); else if (n <= 14) wait_bar<2>(); else if (n == 22) wait_bar<4>(); else if (n == 30) wait_bar<6>();
        else if (n == 38) { if (bq) wait_bar<10>(); else wait_bar<6>(); } else if (n == 46) wait_bar<4>(); else if (n == 54) wait_bar<5>();
        else if (n == 62) lbar(); else wait_bar<4>();
        if (n == 46 && wid == 0) flag_st(ws, WS_FQ + (mg * 16 + ng) * 128, nonce);
        if (n == 6) TS(2); if (n == 14) TS(3); if (n == 30) TS(4); if (n == 46) TS(5); if (n == 62) TS(6); if (n == 68) TS(7);
        SBAR();
      }
      constexpr bool d1f = u == 6 || n == 68 || n == 2;
      if constexpr (d1f) { D1 = MFMA(cat(wf[n % NWF][2], wf[n % NWF][3]), xf[k1], D1); PIN(D1); SBAR(); }
      if constexpr (n + RA < 70) rd_w(n + RA);
      if constexpr (n == 68) {
        const lptr qb = lds + L_Q + (wid & 1) * 4096 + ((wid >> 1) & 1) * 2048 + lane * 16, qa = lds + L_Q + (wid & 1) * 4096 + (((wid >> 1) & 1) ^ 1) * 2048 + lane * 16;
        kf0 = ld128(lds + lane * 16); QA0 = ld128(qa); QB0 = ld128(qb); kf1 = ld128(lds + 1024 + lane * 16); QA1 = ld128(qa + 1024); QB1 = ld128(qb + 1024);
        cfix = ld128(lds + L_CS + wn * 1024 + lane * 16);
      }
      if constexpr (n == 69) { sa = MFMA(kf1, QA1, sa); sb = MFMA(kf1, QB1, sb); PIN(sa); PIN(sb); }
      if constexpr (n == 2) rd_x(4, 8);
      if constexpr (u == 6 && n < 24) rd_x(n + 2, n + 6);
      if constexpr (u == 0 && n > 0 && n < 24) rd_x(n + 4, n + 8);
      if constexpr (!early) colsum(n, 0);
      SBAR();
      if constexpr (!d1f) { D1 = MFMA(cat(wf[n % NWF][2], wf[n % NWF][3]), xf[k1], D1); PIN(D1); }
      SBAR();
      if constexpr (!early) colsum(n, 1);
      if constexpr (n == 0) dma_w(IC<2>{}); if constexpr (n == 1) dma_x(IC<3>{}); if constexpr (n == 2) dma_w(IC<3>{}); if constexpr (n == 4) dma_w(IC<4>{});
      if constexpr (n == 6) dma_x(IC<4>{}); if constexpr (n == 7) dma_x(IC<5>{}); if constexpr (n == 8) dma_w(IC<5>{}); if constexpr (n == 10) dma_w(IC<6>{});
      if constexpr (n == 14) dma_x(IC<6>{}); if constexpr (n == 15) dma_w(IC<7>{}); if constexpr (n == 18) dma_w(IC<8>{}); if constexpr (n == 20) dma_w(IC<9>{});
      if constexpr (n == 23) dma_w(IC<10>{}); if constexpr (n == 26) dma_w(IC<11>{}); if constexpr (n == 29) dma_w(IC<12>{}); if constexpr (n == 32) dma_w(IC<13>{});
      if constexpr (n == 48) touch(SRD(b < 196 ? wd : wo, b < 196 ? 1792 * 896 : 448 * 896), lane < 8 ? (((b < 196 ? b : b - 196) * 8 + wid) * 8 + lane) * 128 : OOB, lds0 + L_PF + wid * 256);
      if constexpr (n == 17) asm volatile("buffer_load_dwordx2 %0, %1, %2, 0 offen sc1" : "+v"(sg) : "v"(sgo), "s"(wsr) : "memory");
      if constexpr (n == 16) asm volatile("global_load_dwordx4 %0, %2, %3\n\tglobal_load_dwordx4 %1, %2, %3 offset:32" : "+v"(tb0), "+v"(tb1) : "v"(h * 16), "s"(&qkv_tab.v[bq ? 4 * ng + 2 * wn : 0]) : "memory");
      if constexpr (n >= 39 && n < 53 && (n & 1)) dma_w(IC<14 + (n - 39) / 2>{});
      if constexpr (n == 27) cs_out();
      if constexpr (n == 23) {
        PIN(sg); PIN(tb0); PIN(tb1);
        _Pragma("nounroll") for (unsigned sp = 0; !ALL(sg[1] == nonce) && sp < SPIN_CAP; ++sp)
          asm volatile("buffer_load_dwordx2 %0, %1, %2, 0 offen sc1\n\ts_waitcnt vmcnt(0)" : "+v"(sg) : "v"(sgo), "s"(wsr) : "memory");
        sig = U2F(sg[0] & 0xffff0000u); rstd = __builtin_amdgcn_rcpf(sig); mm = __builtin_amdgcn_perm(sg[0], sg[0], 0x01000100u); kg = -1.4426950408889634f * rstd;
        PIN(sig); PIN(rstd); PIN(kg); PIN(mm);
      }
      if constexpr (n == 30) fix2(A0, A1);
      if constexpr (n == 31) epi1(A0, 0);
      if constexpr (n == 34) epi1(A1, 1);
      if constexpr (n == FQP) { fq_off(); asm volatile("buffer_load_dword %0, %1, %2, 0 offen sc1" : "+v"(fl0) : "v"(fqo), "s"(wsr) : "memory"); }
      if constexpr (n == 55) fix2(B0, B1);
      if constexpr (n == 57) { hp = glu8(B0); PIN(hp); }
      if constexpr (n == 64) { hq = glu8(B1); PIN(hq); }
      if constexpr (n == 69) { st128(hp, wsr, lane * 16, hs2, SC1); st128(hq, wsr, lane * 16 + 1024, hs2, SC1); }
      if constexpr (n == FQC) {
        asm volatile("s_waitcnt vmcnt(0)" : "+v"(fl0) :: "memory");
        _Pragma("nounroll") for (unsigned sp = 0; !ALL(fl0 == nonce) && sp < SPIN_CAP; ++sp)
          asm volatile("buffer_load_dword %0, %1, %2, 0 offen sc1\n\ts_waitcnt vmcnt(0)" : "+v"(fl0) : "v"(fqo), "s"(wsr) : "memory");
        kvv = (unsigned)(WS_QKV + head * QH + (wid >> 1) * 16 * QT + 2048 + (wid & 1) * 2048 + lane * 16);
        dma<0, L_Q, true>(wsr, WS_QKV + head * QH + (qg * 4 + (wid >> 1)) * QT + (wid & 1) * 1024 + lane * 16, lds0 + wid * 1024); dma_kv(IC<0>{}, kvv); }
      if constexpr (n > FQC && n < FQC + 8) dma_kv(IC<n - FQC>{}, kvv);
      if constexpr (n == 67) {
        const int vc = 2 * (grp ^ wn) + (p >> 1);
        kb = lds + (wid >> 1) * 4096 + lane * 16;
        vb0 = lds + (wid >> 1) * 4096 + 2048 + vc * 512 + ((((4 * h) ^ (4 * vc)) + q) << 4) + (p & 1) * 8;
        vb1 = lds + (wid >> 1) * 4096 + 2048 + vc * 512 + ((((4 * h + 8) ^ (4 * vc)) + q) << 4) + (p & 1) * 8;
        PIN(kb); PIN(vb0); PIN(vb1);
      }
      if constexpr (n == 68) { sa = MFMA(kf0, QA0, Z); sb = MFMA(kf0, QB0, Z); PIN(sa); PIN(sb); ka0 = ld128(kb); ka1 = ld128(kb + 1024); va0 = cat(trd(vb0), trd(vb1)); }
    });
    SBAR();
    C0 = MFMA(cfix, ((u32x4){mm, mm, mm, mm}), C0);
    float ma = sa[0], mb = sb[0];
    UNROLL for (int i = 1; i < 16; ++i) { ma = __builtin_fmaxf(ma, sa[i]); mb = __builtin_fmaxf(mb, sb[i]); }
    const unsigned pa = pk(1.f - max32(ma), 0.f), pb = pk(1.f - max32(mb), 0.f);
    const bool p0 = (head & 1) && h == 0, p1 = !(head & 1) && h == 1;
    QA0[0] = p0 ? pa : QA0[0]; QA1[2] = p1 ? pa : QA1[2]; QB0[0] = p0 ? pb : QB0[0]; QB1[2] = p1 ? pb : QB1[2];
    s0 = MFMA(ka0, QA0, Z); s0 = MFMA(ka1, QA1, s0);
    PIN(s0); PIN(QB0); PIN(QB1);
    SBAR();
    st128(glu8(C0), wsr, ng < 14 ? lane * 16 : OOB, WS_H + (mt * 112 + 64 + ng * 2 + wn) * 1024, SC1);
    SBAR();
    TS(8);
  }
  return phase23(x, wo, wd, ws, out, lds, nonce, QA0, QA1, QB0, QB1, ka0, ka1, va0, s0, kb, vb0, vb1, kvv, wid TSARG);
}

extern "C" void kernel_launch(void* const* d_in, const int* in_sizes, int n_in, void* d_out, int out_size, void* d_ws, size_t ws_size,
                              hipStream_t stream) {
  (void)hipFuncSetAttribute((const void*)fused_kernel, hipFuncAttributeMaxDynamicSharedMemorySize, LDS_BYTES);
  fused_kernel<<<256, 512, LDS_BYTES, stream>>>((const char*)d_in[0], (const char*)d_in[1], (const char*)d_in[2], (const char*)d_in[4], (const char*)d_in[5],
                                                (const char*)d_in[6], (char*)d_ws, (char*)d_out);
}

#undef DBG_TS
```

```cpp
#include <hip/hip_runtime.h>
#include <hip/hip_bf16.h>
#include <stdint.h>

typedef short bf16x8 __attribute__((ext_vector_type(8)));
typedef short s16x4 __attribute__((ext_vector_type(4)));
typedef float f32x16 __attribute__((ext_vector_type(16)));
typedef float f32x4 __attribute__((ext_vector_type(4)));
typedef float f32x2 __attribute__((ext_vector_type(2)));
typedef unsigned u32x4 __attribute__((ext_vector_type(4)));
typedef unsigned u32x2 __attribute__((ext_vector_type(2)));
typedef __bf16 bf2 __attribute__((ext_vector_type(2)));
typedef __amdgpu_buffer_rsrc_t srd_t;
#define LAS __attribute__((address_space(3)))
#define GAS __attribute__((address_space(1)))
typedef LAS char* lptr;
#define DI __device__ __forceinline__
#define AI __attribute__((always_inline))
#define MFMA(a, b, c) __builtin_amdgcn_mfma_f32_32x32x16_bf16(__builtin_bit_cast(bf16x8, (a)), __builtin_bit_cast(bf16x8, (b)), (c), 0, 0, 0)
#define PIN(x) asm volatile("" : "+v"(x))
#define SBAR() __builtin_amdgcn_sched_barrier(0)
#define RLX_AGENT __ATOMIC_RELAXED, __HIP_MEMORY_SCOPE_AGENT
#define EX(v) __builtin_amdgcn_exp2f(v)
#define F2U(x) __builtin_bit_cast(unsigned, (float)(x))
#define U2F(x) __builtin_bit_cast(float, (unsigned)(x))
#define ANY(p) (__builtin_amdgcn_ballot_w64(p) != 0)
#define ALL(p) (__builtin_amdgcn_ballot_w64(!(p)) == 0)
#define UNROLL _Pragma("unroll")
#define SRD(p, n) __builtin_amdgcn_make_buffer_rsrc((void*)(p), 0, (n), 0x00020000)

constexpr int WS_E = 0;
constexpr int WS_FQ = 4096;
constexpr int WS_FH = WS_FQ + 256 * 128;
constexpr int WS_FO = WS_FH + 256 * 128;
constexpr int WS_ST = WS_FO + 256 * 128;
constexpr int WS_QKV = 1 << 20;
constexpr int QT = 6144, QH = 64 * QT;
constexpr int WS_O = WS_QKV + 16 * QH;
constexpr int WS_H = WS_O + (2 << 20);
constexpr int WS_END = WS_H + 64 * 112 * 1024;
constexpr int OOB = 1 << 30;
constexpr int SC1 = 16;
constexpr unsigned SPIN_CAP = 1u << 20;

template <int I> struct IC { static constexpr int v = I; };
template <int B, int E, class F> DI void sfor(F&& f) { if constexpr (B < E) { f(IC<B>{}); sfor<B + 1, E>(f); } }
template <int N> DI void wait_bar() { asm volatile("s_waitcnt vmcnt(%0) lgkmcnt(0)\n\ts_barrier" :: "n"(N) : "memory"); }
template <int N> DI void vwait_bar() { asm volatile("s_waitcnt vmcnt(%0)\n\ts_barrier" :: "n"(N) : "memory"); }
DI void lbar() { asm volatile("s_waitcnt lgkmcnt(0)\n\ts_barrier" ::: "memory"); }
DI unsigned pk(float a, float b) { f32x2 v = {a, b}; return __builtin_bit_cast(unsigned, __builtin_convertvector(v, bf2)); }
DI float rbf(float a) { return U2F(pk(a, a) & 0xffff0000u); }
DI float dot2(unsigned a, unsigned b, float c) { return __builtin_amdgcn_fdot2_f32_bf16(__builtin_bit_cast(bf2, a), __builtin_bit_cast(bf2, b), c, false); }
DI u32x2 trd(lptr p) { return __builtin_bit_cast(u32x2, __builtin_amdgcn_ds_read_tr16_b64_v4i16((LAS s16x4*)p)); }
DI u32x4 cat(u32x2 a, u32x2 b) { return (u32x4){a[0], a[1], b[0], b[1]}; }
DI u32x4 ld128(lptr p) { return *(LAS u32x4*)p; }
DI unsigned bfi(unsigned m, unsigned a, unsigned b) { return (m & a) | (~m & b); }
template <int G, int L, bool DEV = false> DI void dma(srd_t srd, unsigned voff, unsigned lbase) {
  unsigned t;
  if constexpr (DEV)
    asm volatile("s_add_u32 m0, %3, %5\n\ts_mov_b32 %0, %4\n\tbuffer_load_dwordx4 %1, %2, %0 offen sc1 lds" : "=&s"(t) : "v"(voff), "s"(srd), "s"(lbase), "n"(G), "n"(L) : "memory", "scc");
  else
    asm volatile("s_add_u32 m0, %3, %5\n\ts_mov_b32 %0, %4\n\tbuffer_load_dwordx4 %1, %2, %0 offen lds" : "=&s"(t) : "v"(voff), "s"(srd), "s"(lbase), "n"(G), "n"(L) : "memory", "scc");
}
template <int L> DI void dma2(srd_t srd, unsigned voff0, unsigned voff1, unsigned lbase, unsigned ga, unsigned gb) {
  unsigned t;
  asm volatile("s_add_u32 m0, %4, %5\n\ts_mul_i32 %0, %6, %7\n\tbuffer_load_dwordx4 %1, %3, %0 offen lds\n\ts_add_u32 m0, m0, 1024\n\ts_nop 0\n\tbuffer_load_dwordx4 %2, %3, %0 offen lds"
               : "=&s"(t) : "v"(voff0), "v"(voff1), "s"(srd), "s"(lbase), "n"(L), "s"(ga), "s"(gb) : "memory", "scc");
}
template <int L> DI void dma1(srd_t srd, unsigned voff, unsigned lbase, unsigned ga, unsigned gb) {
  unsigned t;
  asm volatile("s_add_u32 m0, %3, %4\n\ts_mul_i32 %0, %5, %6\n\tbuffer_load_dwordx4 %1, %2, %0 offen lds" : "=&s"(t) : "v"(voff), "s"(srd), "s"(lbase), "n"(L), "s"(ga), "s"(gb) : "memory", "scc");
}
DI void touch(srd_t srd, unsigned voff, unsigned ldst) { asm volatile("s_mov_b32 m0, %2\n\ts_nop 0\n\tbuffer_load_dword %0, %1, 0 offen lds" :: "v"(voff), "s"(srd), "s"(ldst) : "memory"); }
DI void st128(u32x4 v, srd_t srd, int voff, int soff, int) { __builtin_amdgcn_raw_buffer_store_b128(v, srd, voff, soff, SC1); SBAR(); asm volatile("s_nop 1"); SBAR(); }
DI float swap32(float v, int h) {
  auto r = __builtin_amdgcn_permlane32_swap(F2U(v), F2U(v), false, false);
  return U2F(h ? r[0] : r[1]);
}
DI float max32(float v) {
  auto r = __builtin_amdgcn_permlane32_swap(F2U(v), F2U(v), false, false);
  return __builtin_fmaxf(U2F(r[0]), U2F(r[1]));
}
DI u32x4 widen(u32x2 a, u32x2 b) {
  auto r0 = __builtin_amdgcn_permlane32_swap(a[0], b[0], false, false); auto r1 = __builtin_amdgcn_permlane32_swap(a[1], b[1], false, false);
  return (u32x4){r0[0], r1[0], r0[1], r1[1]};
}
DI float wave_sum(float v) {
  v += U2F(__builtin_amdgcn_update_dpp(0u, F2U(v), 0x111, 0xf, 0xf, true));
  v += U2F(__builtin_amdgcn_update_dpp(0u, F2U(v), 0x112, 0xf, 0xf, true));
  v += U2F(__builtin_amdgcn_update_dpp(0u, F2U(v), 0x114, 0xf, 0xf, true));
  v += U2F(__builtin_amdgcn_update_dpp(0u, F2U(v), 0x118, 0xf, 0xf, true));
  v += U2F(__builtin_amdgcn_update_dpp(0u, F2U(v), 0x142, 0xa, 0xf, true));
  v += U2F(__builtin_amdgcn_update_dpp(0u, F2U(v), 0x143, 0xc, 0xf, true));
  return v;
}
DI unsigned flag_ld(const char* ws, int off) { return __hip_atomic_load((GAS unsigned*)(ws + off), RLX_AGENT); }
DI void flag_st(char* ws, int off, unsigned v) {
  if (__builtin_amdgcn_mbcnt_hi(~0u, __builtin_amdgcn_mbcnt_lo(~0u, 0u)) == 0) __hip_atomic_store((GAS unsigned*)(ws + off), v, RLX_AGENT); }

constexpr int RA = 2, NWF = RA + 1;
constexpr int PART = 14336;
constexpr int L_W = 65536;
constexpr int L_CS = 9 * 16384;
constexpr int L_PF = L_CS + 2048;
constexpr int L_Q = 65536;
constexpr int wpos(int m) { return m < 9 ? L_W + (m % 5) * 16384 : m == 9 ? 49152 : m == 10 ? L_W : m < 14 ? (m - 11) * 16384 : L_W + 16384 + (m - 14) * 8192; }
constexpr int FQP = 52, FQC = 59;
constexpr int L_WD = 65536;
constexpr int LDS_BYTES = 163840;
constexpr unsigned ONES2 = 0x3f803f80u, PAD0 = 0x00003f80u;
static_assert(L_PF + 2048 <= LDS_BYTES, "LDS map");

constexpr int DN0 = 7;
constexpr int kv_at(int s, int p) { return p == 0 && s > 0 && s + 3 < 16 ? (s + 3) * 2 + 1 : p == 7 && s + 4 < 16 ? (s + 4) * 2 : -1; }
constexpr int wd_at(int s, int p) {
  if (s >= 1 && s <= 3) return p == 1 ? s * 4 - 4 : p == 3 ? s * 4 - 3 : p == 4 ? s * 4 - 2 : p == 5 ? s * 4 - 1 : -1;
  for (int g = 0; g < 2; ++g) { if (s == 10 + 4 * g && p == 7) return 12 + 4 * g; if (s == 11 + 4 * g) return p == 0 ? 13 + 4 * g : p == 2 ? 14 + 4 * g : p == 5 ? 15 + 4 * g : -1; }
  return -1;
}
constexpr int h_at(int s, int p) { const int z = (s - 5) * 3 + (p == 1 ? 1 : p == 3 ? 2 : 3); if (s == 15) return p == 1 ? 24 : p == 3 ? 25 : p == 4 ? 26 : p == 7 ? 27 : -1;
  return s == 4 && p == 7 ? 0 : s >= 5 && (p == 1 || p == 3 || p == 4) && z < 24 ? z : -1; }
constexpr bool pf_at(int s, int p) { return s == 4 && p == 1; }
constexpr int n_at(int s, int p) { return (kv_at(s, p) >= 0) + (wd_at(s, p) >= 0) + (h_at(s, p) >= 0) + pf_at(s, p); }
constexpr int cnt_after(int s0, int p0, int s) { int n = 0; for (int z = s0 * 8 + p0 + 1; z < s * 8 + 6; ++z) n += n_at(z / 8, z % 8); return n; }
constexpr int dslot(int sub) { return L_WD + (sub % 3) * 32768; }

struct QkvTab { unsigned v[44][2][4]; };
constexpr QkvTab make_tab() {
  QkvTab t = {};
  for (int tile = 0; tile < 44; ++tile) for (int h = 0; h < 2; ++h) {
    const int col0 = tile * 32, sec = col0 / 448; unsigned* e = t.v[tile][h];
    e[0] = e[1] = e[2] = OOB; e[3] = 0;
    if (tile < 42) for (int pp = 0; pp < 2; ++pp) {
      const int cu = (col0 - sec * 448) / 8 + 2 * pp + h, j = cu / 7, w = cu % 7, hd = 2 * j + (w > 3), ch = w > 3 ? w - 3 : w;
      e[pp] = WS_QKV + sec * 2048 + hd * QH + ch * 512 + (sec == 2 ? ch * 64 : 0);
      if (w == 3) { e[2] = WS_QKV + sec * 2048 + (hd + 1) * QH; e[3] |= 1u << pp; }
    }
  }
  return t;
}
__device__ const QkvTab qkv_tab = make_tab();

#define LANE_IDS() int lane; asm volatile("v_mbcnt_lo_u32_b32 %0, -1, 0\n\tv_mbcnt_hi_u32_b32 %0, -1, %0" : "=v"(lane));        \
  const int r = lane & 31, h = lane >> 5, q = (lane & 15) >> 2, p = lane & 3, grp = (lane >> 4) & 1, rr = lane >> 3, cc = lane & 7
DI void phase23(const char* x, const char* wo, const char* wd, char* ws, char* out, lptr lds, unsigned nonce, u32x4 QA0, u32x4 QA1, u32x4 QB0, u32x4 QB1,
                u32x4 k0, u32x4 k1, u32x4 v0, f32x16 sa, lptr kb, lptr vb0, lptr vb1, unsigned kvv, int wid) {
  const unsigned lds0 = (unsigned)(uintptr_t)lds;
  const int b = __builtin_amdgcn_workgroup_id_x(), xcd = b & 7, idx = b >> 3;
  const int ng = (xcd & 3) * 4 + (idx & 3), mg = (xcd >> 2) * 8 + (idx >> 2);
  const srd_t wsr = SRD(ws, WS_END);
  const f32x16 Z = {};
  const int head = 2 * xcd + (idx & 1), qg = idx >> 1, odd = head & 1;
  const int idx3 = idx < 28 ? idx : idx - 28, mq3 = (idx3 * 37) >> 8, mg3 = xcd * 4 + mq3, ng3 = idx3 - mq3 * 7;
  LANE_IDS();
  const unsigned lw2k = lds0 + wid * 2048;
  auto dma_kv = [&](auto zc) AI { constexpr int z = decltype(zc)::v; dma<(z >> 1) * QT + (z & 1) * 1024, ((z >> 1) & 3) * 16384 + (z & 1) * 1024, true>(wsr, kvv, lw2k); };
  const int w3m = wid & 1, w3k = wid >> 1;
  u32x4 hf[28];
  const int ft = w3k & 1, fs = w3k >> 1;
  unsigned vo_d = 0; const unsigned ld_d = lds0 + wid * 4096;
  auto set_vo_d = [&]() AI { vo_d = (unsigned)(((idx < 28 ? w3k * 448 + w3m * 32 : OOB / 896) + rr) * 896 + ((cc ^ (grp << 2)) << 4) + ng3 * 128); PIN(vo_d); };
  const srd_t sD = SRD(wd, 1792 * 896);
  auto dma_d = [&](auto zc) AI { constexpr int z = decltype(zc)::v; dma<((z >> 2) * 64 + (z & 3) * 8) * 896, dslot(z >> 2) + (z & 3) * 1024>(sD, vo_d, ld_d); };
  int db0 = 0, db1 = 0, dbo = L_WD;
  auto set_db = [&]() AI { db0 = L_WD + w3k * 8192 + (4 * h + q) * 128 + p * 8 + ((grp ^ ((q >> 1) << 1)) << 5); db1 = db0 ^ 64; PIN(db0); PIN(db1); };
  auto rebase = [&](int o) AI { db0 += o - dbo; db1 += o - dbo; dbo = o; PIN(db0); PIN(db1); };
  f32x16 F0 = Z, F1 = Z;
  u32x4 dw0, dw1;
  auto rd_d0 = [&](int d) AI { const int o = dslot(d >> 2) + (d & 3) * 2048 - dbo; dw0 = cat(trd(lds + db0 + o), trd(lds + db0 + o + 1024)); };
  auto rd_d1 = [&](int d) AI { const int o = dslot(d >> 2) + (d & 3) * 2048 - dbo; dw1 = cat(trd(lds + db1 + o), trd(lds + db1 + o + 1024)); };
  {
    const int wq = wid & 1, kq = wid >> 1;
    u32x4 v1, pa, pb;
    float e0, e1, e2, e3;
    f32x16 OA = Z, OB = Z, sb;
    unsigned junk, fh = ~nonce; const int fho = WS_FH + ((mg3 >> 1) * 16 + (lane & 15)) * 128;
    const int hld = WS_H + ((mg3 * 2 + w3m) * 112 + w3k * 28) * 1024, hlv = idx < 28 ? lane * 16 : OOB;
    auto send = [&](const f32x16& a, int hh, int j) AI {
      const lptr d = kb + (((wid ^ (2 * j)) * 6144 + (j - 1) * 2048) - kq * 4096);
      *(LAS f32x4*)d = (f32x4){a[8 * hh], a[8 * hh + 1], a[8 * hh + 2], a[8 * hh + 3]}; *(LAS f32x4*)(d + 1024) = (f32x4){a[8 * hh + 4], a[8 * hh + 5], a[8 * hh + 6], a[8 * hh + 7]}; };
#define EX2(S, i, a, c) do { a = EX(S[i]); c = EX(S[(i) + 1]); PIN(a); PIN(c); } while (0)
#define STEP(FIRST, SC, SN, QN0, QN1, OP, OC, L0, L1, L2, L3, X0, X1) do {                                                       \
      SBAR(); SN = MFMA(k0, QN0, Z); SBAR(); L0;                                                                          \
      if (!(FIRST)) { pb[2] = pk(e0, e1); pb[3] = pk(e2, e3); PIN(pb); } SBAR(); EX2(SC, 0, e0, e1); SBAR(); X0; SBAR(); EX2(SC, 2, e2, e3); \
      SBAR(); if (!(FIRST)) OP = MFMA(v1, pb, OP); SBAR(); L1;                                                                          \
      pa[0] = pk(e0, e1); pa[1] = pk(e2, e3); PIN(pa); SBAR(); EX2(SC, 4, e0, e1); EX2(SC, 6, e2, e3);                    \
      SBAR(); SN = MFMA(k1, QN1, SN); SBAR(); L2;                                                                         \
      pa[2] = pk(e0, e1); pa[3] = pk(e2, e3); PIN(pa); SBAR(); EX2(SC, 8, e0, e1); SBAR(); X1; SBAR(); EX2(SC, 10, e2, e3); \
      SBAR(); OC = MFMA(v0, pa, OC); SBAR(); L3;                                                                          \
      pb[0] = pk(e0, e1); pb[1] = pk(e2, e3); PIN(pb); SBAR(); EX2(SC, 12, e0, e1); EX2(SC, 14, e2, e3); SBAR(); } while (0)
    sfor<0, 16>([&](auto sc_) AI {
      constexpr int s = decltype(sc_)::v, so = (s & 3) * 16384, sn = ((s + 1) & 3) * 16384;
      auto side = [&](auto pc) AI {
        constexpr int P = decltype(pc)::v, kv = kv_at(s, P), wz = wd_at(s, P), hz = h_at(s, P);
        if constexpr (s == 0 && P == 3) set_vo_d();
        if constexpr (s == 0 && P == 5) set_db();
        if constexpr (kv >= 0) dma_kv(IC<kv>{});
        if constexpr (wz >= 0) dma_d(IC<wz>{});
        if constexpr (pf_at(s, P)) {
          const int row = wid < 2 ? mq3 * 448 + 320 + wid * 64 + lane : mq3 * 112 + (wid - 2) * 56 + (lane < 56 ? lane : 55);
          asm volatile("buffer_load_dword %0, %1, %2, 0 offen" : "=&v"(junk) : "v"(wid < 4 && idx < 28 ? row * 896 + ng3 * 128 : OOB), "s"(SRD(wid < 2 ? wd : wo, wid < 2 ? 1792 * 896 : 448 * 896)) : "memory");
        }
        if constexpr (hz >= 0) hf[hz] = __builtin_amdgcn_raw_buffer_load_b128(wsr, hlv + (hz & 3) * 1024, hld + (hz >> 2) * 4096, SC1);
      };
      auto mid = [&]() AI {
        if (s == 4 && wid == 0) {
          asm volatile("s_waitcnt vmcnt(%1)" : "+v"(fh) : "n"(cnt_after(3, 6, 4)) : "memory");
          _Pragma("nounroll") for (unsigned sp = 0; !ALL(fh == nonce) && sp < SPIN_CAP; ++sp) { fh = flag_ld(ws, fho); PIN(fh); }
        }
        if constexpr (s == 0) vwait_bar<5 + cnt_after(0, -1, 0)>();
        else if constexpr (s == 1) vwait_bar<cnt_after(0, -1, 1)>();
        else if constexpr (s == 14) vwait_bar<0>(); else if constexpr (s < 14) vwait_bar<cnt_after(s - 1, 0, s)>();
        if (s == 1 && wid == 0) flag_st(ws, WS_FH + (mg * 16 + ng) * 128, nonce);
        if (s == 3 && wid == 0) asm volatile("buffer_load_dword %0, %1, %2, 0 offen sc1" : "+v"(fh) : "v"(fho), "s"(wsr) : "memory");
        if constexpr (s == 6) asm volatile("" :: "v"(junk));
        if constexpr (s == 13) rebase(L_WD + 65536);
        if constexpr (s == 14) { UNROLL for (int k = 23; k >= 8; --k) PIN(hf[k]); }
        __builtin_amdgcn_s_setprio(3);
      };
      auto dn0 = [&](int d) AI { if (d >= 0) { F0 = MFMA(dw0, hf[d], F0); PIN(F0); } if (d >= -1) rd_d0(d + 1); };
      auto dn1 = [&](int d) AI { if (d >= 0) { F1 = MFMA(dw1, hf[d], F1); PIN(F1); } if (d >= -1) rd_d1(d + 1); };
      STEP(s == 0, sa, sb, QB0, QB1, OB, OA,
           k0 = ld128(kb + sn); side(IC<0>{}); __builtin_amdgcn_s_setprio(2),
           v1 = cat(trd(vb0 + so + 256), trd(vb1 + so + 256)); side(IC<1>{}),
           k1 = ld128(kb + sn + 1024); side(IC<2>{}); __builtin_amdgcn_s_setprio(1),
           side(IC<3>{}), dn0(s - DN0), if (s == 15) dn1(8));
      STEP(false, sb, sa, QA0, QA1, OA, OB,
           __builtin_amdgcn_s_setprio(0); side(IC<4>{}),
           side(IC<5>{}),
           mid(); if (s == 15) send(OA, 0, 1),
           v0 = cat(trd(vb0 + sn), trd(vb1 + sn)); side(IC<7>{}); if (s == 15) send(OA, 1, 3), if (s < 15) dn1(s - DN0); else dn0(9), if (s == 15) dn1(9));
    });
    pb[2] = pk(e0, e1); pb[3] = pk(e2, e3);
    OB = MFMA(v1, pb, OB);
    __builtin_amdgcn_s_setprio(0);
    send(OB, 1, 2);
    *(LAS float*)(kb + (49152 + wq * 1024)) = odd ? OB[0] : OB[4];
    {
    LANE_IDS();
    const bool padl = h != odd;
    const int qt = qg * 4 + wq * 2 + ft, lw = (wid & 3) | (odd ? 0 : 4);
    const lptr pp = lds + wid * 6144 + lane * 16, lp = lds + lw * 6144 + (odd ? 0 : 1536) + r * 16;
    const lptr lq = lds + 49152 + (lw >> 1) * 4096 + wq * 1024 + (odd ? 0 : 512) + r * 16;
    f32x4 o0 = {OB[0], OB[1], OB[2], OB[3]}, o1 = {OB[4], OB[5], OB[6], OB[7]};
    lbar();
    const float l0 = *(LAS float*)lq, l1 = *(LAS float*)lp, l2 = *(LAS float*)(lp + 2048), l3 = *(LAS float*)(lp + 4096);
    f32x4 t[6];
    UNROLL for (int k = 0; k < 6; ++k) t[k] = *(LAS f32x4*)(pp + k * 1024);
    SBAR();
    float l = (l0 + l1) + (l2 + l3);
    UNROLL for (int k = 0; k < 3; ++k) { o0 += t[2 * k]; o1 += t[2 * k + 1]; }
    if (__builtin_expect(ANY(!(l <= 1.8446744e19f && l >= 5.4210109e-20f)), 0)) {
      const int qo = WS_QKV + head * QH + qt * QT + lane * 16;
      u32x4 q0 = __builtin_amdgcn_raw_buffer_load_b128(wsr, qo, 0, SC1), q1 = __builtin_amdgcn_raw_buffer_load_b128(wsr, qo + 1024, 0, SC1);
      f32x16 O = Z; float m = -__builtin_inff();
      _Pragma("nounroll") for (int t = 0; t < 64; ++t) {
        const int ko = WS_QKV + head * QH + t * QT + 2048;
        f32x16 s = MFMA(__builtin_amdgcn_raw_buffer_load_b128(wsr, ko + lane * 16, 0, SC1), q0, Z);
        s = MFMA(__builtin_amdgcn_raw_buffer_load_b128(wsr, ko + 1024 + lane * 16, 0, SC1), q1, s);
        float mn = m;
        UNROLL for (int i = 0; i < 16; ++i) mn = __builtin_fmaxf(mn, s[i]);
        mn = max32(mn);
        const float f = EX(m - mn); m = mn;
        UNROLL for (int i = 0; i < 16; ++i) O[i] *= f;
        UNROLL for (int st = 0; st < 2; ++st) {
          unsigned vv[8];
          UNROLL for (int j = 0; j < 8; ++j) vv[j] = __builtin_amdgcn_raw_buffer_load_b16(wsr, ko + 2048 + (r >> 3) * 512 + (((16 * st + 8 * (j >> 2) + 4 * h + (j & 3)) ^ (4 * (r >> 3))) << 4) + (r & 7) * 2, 0, SC1) & 0xffffu;
          O = MFMA(((u32x4){vv[0] | vv[1] << 16, vv[2] | vv[3] << 16, vv[4] | vv[5] << 16, vv[6] | vv[7] << 16}),
                   ((u32x4){pk(EX(s[8 * st] - m), EX(s[8 * st + 1] - m)), pk(EX(s[8 * st + 2] - m), EX(s[8 * st + 3] - m)),
                            pk(EX(s[8 * st + 4] - m), EX(s[8 * st + 5] - m)), pk(EX(s[8 * st + 6] - m), EX(s[8 * st + 7] - m))}), O);
        }
      }
      const float lc = odd ? O[0] : O[12], lx = swap32(lc, h); l = padl ? lc : lx;
      o0 = fs ? (f32x4){O[8], O[9], O[10], O[11]} : (f32x4){O[0], O[1], O[2], O[3]};
      o1 = fs ? (f32x4){O[12], O[13], O[14], O[15]} : (f32x4){O[4], O[5], O[6], O[7]};
    }
    const float ri = __builtin_amdgcn_rcpf(l);
    const float r0 = padl && odd && fs == 0 ? 0.f : ri, r1 = padl && !odd && fs == 1 ? 0.f : ri;
    st128((u32x4){pk(o0[0] * r0, o0[1] * r0), pk(o0[2] * r0, o0[3] * r0), pk(o1[0] * r1, o1[1] * r1), pk(o1[2] * r1, o1[3] * r1)},
                                           wsr, lane * 16, WS_O + (qt * 16 + head) * 2048 + fs * 1024, SC1);
    }
  }
  if (idx >= 28) { wait_bar<0>();
    if (wid == 0) {
      flag_st(ws, WS_FO + (qg * 16 + head) * 128, nonce);
      if (b == 255) {
        LANE_IDS();
        _Pragma("nounroll") for (unsigned sp = 0; sp < SPIN_CAP; ++sp) {
          unsigned d = 0;
          UNROLL for (int i = 0; i < 4; ++i) d |= flag_ld(ws, WS_FQ + lane * 512 + i * 128) ^ nonce;
          if (ALL(d == 0)) break;
        }
        flag_st(ws, WS_E, nonce);
      }
    }
    return;
  }
  {
    lbar();
    const srd_t sO = SRD(wo, 448 * 896); const unsigned wov = vo_d + (wid * 56 - w3k * 448 - w3m * 32) * 896, wol = lds0 + wid * 7168;
    auto dma_o = [&](auto ic) AI { constexpr int i = decltype(ic)::v; dma<i * 8 * 896, i * 1024>(sO, wov, wol); };
    sfor<0, 4>(dma_o);
    u32x4 ew0, ew1;
    auto down2 = [&](int d, bool more) AI {
      const int o = dslot((d + 1) >> 2) + ((d + 1) & 3) * 2048 - dbo;
      ew0 = cat(trd(lds + db0 + o), trd(lds + db0 + o + 1024)); ew1 = cat(trd(lds + db1 + o), trd(lds + db1 + o + 1024));
      if (d == 10 || d == 22) rebase(L_WD);
      SBAR(); F0 = MFMA(dw0, hf[d], F0); F1 = MFMA(dw1, hf[d], F1); PIN(F0); PIN(F1); SBAR();
      if (more) { rd_d0(d + 2); rd_d1(d + 2); }
      SBAR(); F0 = MFMA(ew0, hf[d + 1], F0); F1 = MFMA(ew1, hf[d + 1], F1); PIN(F0); PIN(F1); SBAR();
    };
    static_assert(DN0 == 7, "units 0..9 ran beside the attention");
    down2(10, true); sfor<4, 7>(dma_o);
    vwait_bar<7>();
    if (wid == 0) flag_st(ws, WS_FO + (qg * 16 + head) * 128, nonce);
    sfor<20, 22>(dma_d); down2(12, true); sfor<22, 24>(dma_d); down2(14, true);
    vwait_bar<4>();
    LANE_IDS();
    const int mt = mg3 * 2 + w3m, orot = ng3 & 1;
    unsigned fo = ~nonce; const int foo = WS_FO + ((mg3 >> 1) * 16 + (lane & 15)) * 128;
    sfor<24, 26>(dma_d); down2(16, true); sfor<26, 28>(dma_d);
    if (wid == 0) asm volatile("buffer_load_dword %0, %1, %2, 0 offen sc1" : "+v"(fo) : "v"(foo), "s"(wsr) : "memory");
    down2(18, false);
    u32x4 wo0[8], wo1[8];
    UNROLL for (int k = 0; k < 8; ++k) {
      const int wbo = w3k * (PART - 8192) - L_WD + ((k >> 2) ^ orot) * 7168, o = (((k >> 1) & 1) * 24 + (k & 1) * 16) * 128;
      const lptr wb0 = lds + db0 + wbo, wb1 = lds + db1 + wbo;
      wo0[k] = cat(trd(wb0 + o), trd(wb0 + o + 1024)); wo1[k] = cat(trd(wb1 + o), trd(wb1 + o + 1024));
    }
    rebase(L_WD + 65536);
    if (wid == 0) {
      asm volatile("s_waitcnt vmcnt(0)" : "+v"(fo) :: "memory");
      _Pragma("nounroll") for (unsigned sp = 0; !ALL(fo == nonce) && sp < SPIN_CAP; ++sp) { fo = flag_ld(ws, foo); PIN(fo); }
    }
    wait_bar<0>();
    UNROLL for (int k = 27; k >= 24; --k) PIN(hf[k]);
    u32x4 of[8];
    const int oso = WS_O + (mt * 16 + w3k * 4) * 2048 + orot * 4096;
    UNROLL for (int k = 0; k < 4; ++k) of[k] = __builtin_amdgcn_raw_buffer_load_b128(wsr, lane * 16 + k * 1024, oso, SC1);
    SBAR();
    rd_d0(20); rd_d1(20); down2(20, true); down2(22, true);
    UNROLL for (int k = 4; k < 8; ++k) of[k] = __builtin_amdgcn_raw_buffer_load_b128(wsr, lane * 16 + (k & 3) * 1024, oso ^ 4096, SC1);
    const unsigned xo = (unsigned)((mt * 32 + w3k * 8 + rr) * 896 + ng3 * 128 + cc * 16);
    const u32x4 xv = *(const u32x4*)(x + xo);
    SBAR();
    down2(24, true); down2(26, false);
    auto xch_send = [&](const f32x16& a0, const f32x16& a1, int base) AI {
      UNROLL for (int c = 0; c < 8; ++c) { const f32x16& a = c < 4 ? a0 : a1; const int o = (c & 3) * 4;
        *(LAS f32x4*)(lds + (base + wid * 8192 + c * 1024) + ((lane ^ (2 * c)) << 4)) = (f32x4){a[o], a[o + 1], a[o + 2], a[o + 3]}; } };
    int xro = w3m * 8192 + cc * 1024 + (((w3k * 8 + rr) ^ (2 * cc)) << 4); PIN(xro);
    xch_send(F0, F1, 0);
    f32x16 S0 = Z, S1 = Z;
    UNROLL for (int k = 0; k < 4; ++k) { S0 = MFMA(wo0[k], of[k], S0); S1 = MFMA(wo1[k], of[k], S1); }
    PIN(S0); PIN(S1);
    lbar();
    f32x4 g0 = *(LAS f32x4*)(lds + xro), g1 = *(LAS f32x4*)(lds + xro + 512);
    UNROLL for (int k = 1; k < 4; ++k) { g0 += *(LAS f32x4*)(lds + xro + k * 16384); g1 += *(LAS f32x4*)(lds + xro + k * 16384 + 512); }
    UNROLL for (int k = 4; k < 8; ++k) { S0 = MFMA(wo0[k], of[k], S0); S1 = MFMA(wo1[k], of[k], S1); }
    UNROLL for (int i = 0; i < 4; ++i) {
      g0[i] = g0[i] * __builtin_amdgcn_rcpf(1.f + EX(g0[i] * -1.4426950408889634f)); g1[i] = g1[i] * __builtin_amdgcn_rcpf(1.f + EX(g1[i] * -1.4426950408889634f)); }
    xch_send(S0, S1, L_WD);
    f32x4 y0 = {U2F(xv[0] << 16), U2F(xv[0] & 0xffff0000u), U2F(xv[1] << 16), U2F(xv[1] & 0xffff0000u)}, y1 = {U2F(xv[2] << 16), U2F(xv[2] & 0xffff0000u), U2F(xv[3] << 16), U2F(xv[3] & 0xffff0000u)};
    xro += L_WD;
    PIN(g0); PIN(g1); PIN(y0); PIN(y1); PIN(xro);
    lbar();
    f32x4 t[8];
    UNROLL for (int k = 0; k < 8; ++k) t[k] = *(LAS f32x4*)(lds + xro + (k >> 1) * 16384 + (k & 1) * 512);
    SBAR();
    UNROLL for (int k = 0; k < 4; ++k) UNROLL for (int i = 0; i < 4; ++i) { y0[i] = __builtin_fmaf(t[2 * k][i], g0[i], y0[i]); y1[i] = __builtin_fmaf(t[2 * k + 1][i], g1[i], y1[i]); }
    __builtin_amdgcn_raw_buffer_store_b128((u32x4){pk(y0[0], y0[1]), pk(y0[2], y0[3]), pk(y1[0], y1[1]), pk(y1[2], y1[3])}, SRD(out, 2048 * 896), xo, 0, SC1);
  }
}

#if defined(__HIP_DEVICE_COMPILE__)
#define NOPK __attribute__((target("no-packed-fp32-ops")))
#else
#define NOPK
#endif
__global__ __launch_bounds__(512, 2) NOPK void fused_kernel(const char* x, const char* wqkv, const char* wo, const char* wg, const char* wu, const char* wd,
                                                       char* ws, char* out) {
  extern __shared__ __attribute__((aligned(16))) char smem[];
  const lptr lds = (lptr)smem;
  const unsigned lds0 = (unsigned)(uintptr_t)lds;
  const int tid = __builtin_amdgcn_workitem_id_x(), lane = tid & 63, wid = __builtin_amdgcn_readfirstlane(tid >> 6);
  const int rr = lane >> 3, cc = lane & 7;
  const int b = __builtin_amdgcn_workgroup_id_x(), xcd = b & 7, idx = b >> 3;
  const int ng = (xcd & 3) * 4 + (idx & 3), mg = (xcd >> 2) * 8 + (idx >> 2);
  {
    unsigned long long pc; asm volatile("s_getpc_b64 %0" : "=s"(pc));
    touch(SRD(pc & ~127ull, 28672), (idx * 8 + (tid >> 6)) * 128, lds0 + L_PF);
  }
  const int rot = (mg & 7) == 7 ? 3 : mg & 7;
  unsigned kc[7];
  UNROLL for (int c = 0; c < 7; ++c) kc[c] = (unsigned)((c + rot >= 7 ? c + rot - 7 : c + rot) * 64);
  const srd_t xr = SRD(x, 2048 * 896);
  const unsigned xv0 = (unsigned)((mg * 128 + wid * 16 + rr) * 896 + ((cc ^ rr) << 4));
  const unsigned xv1 = (unsigned)((mg * 128 + wid * 16 + 8 + (rr ^ 1)) * 896 + ((cc ^ rr ^ 1) << 4));
  const unsigned lw2k = lds0 + wid * 2048;
  auto dma_x = [&](auto cc_) AI { constexpr int c = decltype(cc_)::v; dma2<(c & 3) * 16384>(xr, xv0, xv1, lw2k, kc[c], 2); };
  const srd_t wsr = SRD(ws, WS_END);
  u32x4 srow; unsigned nonce;
  const int srw = mg * 128 + ng * 8 + wid;
  asm volatile("buffer_load_dwordx4 %0, %1, %2, %3 offen" : "=&v"(srow) : "v"(lane * 16), "s"(SRD(x, srw * 896 + 896)), "s"(srw * 896) : "memory");
  dma_x(IC<0>{});
  SBAR();
  const int r = lane & 31, h = lane >> 5, q = (lane & 15) >> 2, p = lane & 3, grp = (lane >> 4) & 1;
  const f32x16 Z = {};
  const int head = 2 * xcd + (idx & 1), qg = idx >> 1;
  auto dma_kv = [&](auto zc, unsigned kvv) AI { constexpr int z = decltype(zc)::v; dma<(z >> 1) * QT + (z & 1) * 1024, ((z >> 1) & 3) * 16384 + (z & 1) * 1024, true>(wsr, kvv, lw2k); };
  u32x4 QA0, QA1, QB0, QB1, kf0, kf1, ka0, ka1, va0; f32x16 s0; lptr kb, vb0, vb1; unsigned kvv = 0;
  {
    const int wm = wid & 3, wn = wid >> 2;
    const bool bq = ng <= 10;
    const unsigned st1 = bq ? 2688 : 3584;
    unsigned va = (16 * wm + rr) * st1 + (((cc << 4) ^ (grp << (bq ? 6 : 5))) ^ (bq ? 0 : wn << 6)) + (bq ? 2 * ng + wn : ng + 12) * 128, vb = va + 8 * st1;
    const char* wgu = wn ? wu : wg;
    const srd_t s2 = SRD(wgu, 448 * 3584), s1 = SRD(bq ? wqkv : wgu, 448 * st1);
    auto dma_w = [&](auto mc) AI {
      constexpr int m = decltype(mc)::v;
      if constexpr (m == 7) { va = (unsigned)((16 * wm + rr) * 3584 + ((cc ^ (grp << 1) ^ (wn << 2)) << 4)) + ng * 128; vb = va + 8 * 3584; PIN(va); PIN(vb); }
      if constexpr (m == 14) {
        va = (unsigned)((16 * wm + (lane >> 2)) * 3584 + (((lane & 3) ^ (wn << 1)) << 4)) + 2048 + ng * 64;
        if (ng >= 14) va = OOB;
        PIN(va);
      }
      if constexpr (m < 7) dma2<wpos(m)>(s1, va, vb, lw2k, kc[m], st1);
      else if constexpr (m < 14) dma2<wpos(m)>(s2, va, vb, lw2k, kc[m - 7], 3584);
      else dma1<wpos(m)>(s2, va, lds0 + wid * 1024, kc[m - 14], 3584);
    };
    dma_w(IC<0>{});
    unsigned epoch;
    asm volatile("buffer_load_dword %0, off, %1, 0 sc1" : "=&v"(epoch) : "s"(wsr) : "memory");
    dma_x(IC<1>{});
    SBAR();
    const int rowb = (8 * h + q) * 128 + p * 8;
    int b20 = grp * 8192 + rowb + (((2 * wn) ^ (q >> 1) ^ (grp << 1)) << 5);
    int b10 = bq ? wn * 8192 + rowb + ((grp ^ ((q >> 1) << 1)) << 5) : b20, b11 = b10 ^ (bq ? 64 : 32);
    int xb[4];
    UNROLL for (int i = 0; i < 4; ++i) { xb[i] = (4 * wm + (r >> 3)) * 1024 + ((r & 7) ^ ((r >> 3) & 1)) * 128 + (((2 * i + h) ^ (r & 7)) << 4); PIN(xb[i]); }
    PIN(b10); PIN(b11); PIN(b20);
    UNROLL for (int c = 2; c < 7; ++c) asm volatile("" : "+s"(kc[c]));
    SBAR();
    u32x4 xf[28];
    auto row_stats = [&]() AI {
      float a = dot2(srow[0], ONES2, 0.f), c = dot2(srow[0], srow[0], 0.f);
      UNROLL for (int i = 1; i < 4; ++i) { a = dot2(srow[i], ONES2, a); c = dot2(srow[i], srow[i], c); }
      a = wave_sum(a); c = wave_sum(c);
      const float mean = a * (1.f / 448.f), var = __builtin_fmaxf(c * (1.f / 448.f) - mean * mean, 0.f);
      unsigned g = pk(-mean, __builtin_amdgcn_sqrtf(rbf(rbf(var) + 1e-5f))); PIN(g); SBAR();
      asm volatile("s_waitcnt vmcnt(4)" : "+v"(epoch) :: "memory");
      nonce = __builtin_amdgcn_readfirstlane(epoch) * 0x9E3779B1u + 0x7F4A7C15u;
      if (lane == 63) __hip_atomic_store((GAS unsigned long long*)(ws + WS_ST + srw * 8), (unsigned long long)nonce << 32 | g, RLX_AGENT);
    };
    auto rd_x = [&](int k0, int k1) AI { UNROLL for (int ks = k0; ks < k1; ++ks) xf[ks] = ld128(lds + xb[ks & 3] + ((ks >> 2) & 3) * 16384); };
    u32x2 wf[NWF][4];
    int ra0 = 0, ra1 = 0;
    auto rd_w = [&](int n) AI {
      if (n < 56) {
        const int m = n >> 2, o = (n & 3) * 2048;
        if ((n & 3) == 0) { ra0 = (m < 7 ? b10 : b20) + wpos(m); ra1 = (m < 7 ? b11 : b20 ^ 32) + wpos(m); PIN(ra0); PIN(ra1); }
        wf[n % NWF][0] = trd(lds + ra0 + o); wf[n % NWF][1] = trd(lds + ra0 + o + 512); wf[n % NWF][2] = trd(lds + ra1 + o); wf[n % NWF][3] = trd(lds + ra1 + o + 512);
      } else {
        const int o = ((n - 56) >> 1) * 8192 + (n & 1) * 2048;
        if (n == 56) { ra0 = wpos(14) + grp * 4096 + (8 * h + q) * 64 + ((wn ^ grp) << 5) + p * 8; PIN(ra0); }
        wf[n % NWF][0] = trd(lds + ra0 + o); wf[n % NWF][1] = trd(lds + ra0 + o + 256); wf[n % NWF][2] = trd(lds + ra0 + o + 1024); wf[n % NWF][3] = trd(lds + ra0 + o + 1280);
      }
    };
    SBAR();
    dma_w(IC<1>{});
    asm volatile("s_waitcnt vmcnt(9)" : "+v"(srow) :: "memory");
    row_stats();
    SBAR();
    lbar();
    UNROLL for (int n = 0; n < RA; ++n) rd_w(n);
    rd_x(0, 4);
    dma_x(IC<2>{});
    f32x16 A0 = Z, A1 = Z, B0 = Z, B1 = Z, C0 = Z;
    float cs0 = 0.f, cs1 = 0.f, rstd = 0.f, sig = 0.f, kg = 0.f; unsigned mm = 0; u32x2 sg = {0u, ~nonce};
    const int mt = mg * 4 + wm;
    const unsigned csel = ((wm & 1) ? 0x05040c0cu : 0x01000c0cu) + ((wm >> 1) ? 0x02020000u : 0u);
    auto colsum = [&](int n, int hh) AI {
      cs0 += U2F(__builtin_amdgcn_perm(wf[n % NWF][2 + hh][0], wf[n % NWF][hh][0], csel)); cs1 += U2F(__builtin_amdgcn_perm(wf[n % NWF][2 + hh][1], wf[n % NWF][hh][1], csel));
      PIN(cs0); PIN(cs1);
    };
    auto cs_out = [&]() AI {
      const float cs = cs0 + cs1, hi = rbf(cs); *(LAS unsigned*)(lds + L_CS + wn * 1024 + lane * 16 + ((wm & 1) * 2 + (wm >> 1)) * 4) = pk(hi, cs - hi); cs0 = 0.f; cs1 = 0.f; };
    auto fix2 = [&](f32x16& a0, f32x16& a1) AI {
      const u32x4 c = ld128(lds + L_CS + wn * 1024 + lane * 16);
      a0 = MFMA(c, ((u32x4){mm, mm, 0u, 0u}), a0); a1 = MFMA(c, ((u32x4){0u, 0u, mm, mm}), a1); PIN(a0); PIN(a1); };
    auto glu = [&](const f32x16& acc, int i) AI { return acc[i + 8] * __builtin_amdgcn_rcpf(__builtin_fmaf(EX(acc[i] * kg), sig, sig)); };
    auto glu8 = [&](const f32x16& acc) AI { return (u32x4){pk(glu(acc, 0), glu(acc, 1)), pk(glu(acc, 2), glu(acc, 3)), pk(glu(acc, 4), glu(acc, 5)), pk(glu(acc, 6), glu(acc, 7))}; };
    const int hs2 = WS_H + (mt * 112 + ng * 4 + wn * 2) * 1024;
    u32x4 tb0 = {0u, 0u, 0u, 0u}, tb1 = tb0;
    auto epi1 = [&](const f32x16& acc, int t) AI {
      if (bq) {
        const u32x4 e = t ? tb1 : tb0; const unsigned o[3] = {e[0] ^ (r << 4), e[1] ^ (r << 4), e[2] ^ (r << 4)};
        const unsigned m[2] = {(unsigned)__builtin_amdgcn_sbfe(e[3], 0, 1), (unsigned)__builtin_amdgcn_sbfe(e[3], 1, 1)};
        const float sc = 2 * ng + wn < 7 ? rstd * (0.18898223650461362f * 1.4426950408889634f) : rstd;
        u32x4 v[2];
        UNROLL for (int pp = 0; pp < 2; ++pp)
          v[pp] = widen((u32x2){pk(acc[8 * pp] * sc, acc[8 * pp + 1] * sc), pk(acc[8 * pp + 2] * sc, acc[8 * pp + 3] * sc)},
                        (u32x2){pk(acc[8 * pp + 4] * sc, acc[8 * pp + 5] * sc), pk(acc[8 * pp + 6] * sc, acc[8 * pp + 7] * sc)});
        u32x4 d0 = {v[0][0], v[0][1], bfi(m[0], PAD0, v[0][2]), ~m[0] & v[0][3]}, d1 = {v[1][0], v[1][1], bfi(m[1], PAD0, v[1][2]), ~m[1] & v[1][3]};
        u32x4 d2 = {PAD0, 0u, bfi(m[0], v[0][2], v[1][2]), bfi(m[0], v[0][3], v[1][3])};
        PIN(d0); PIN(d1); PIN(d2); SBAR();
        __builtin_amdgcn_raw_buffer_store_b128(d0, wsr, o[0], mt * QT, SC1); __builtin_amdgcn_raw_buffer_store_b128(d1, wsr, o[1], mt * QT, SC1);
        st128(d2, wsr, o[2], mt * QT, SC1);
      } else st128(glu8(acc), wsr, lane * 16 + t * 1024, hs2 + 12 * 4096, SC1);
    };
    u32x4 hp, hq; unsigned fl0 = ~nonce; f32x16 sa, sb; u32x4 cfix;
    int fqo = 0;
    auto fq_off = [&]() AI {
      const int c0 = (lane < 8 ? 448 + (wid & 1) * 448 : 0) + head * 28 - (head & 1) * 4 + (lane & 1) * 31, g = c0 >> 7, m2 = lane < 8 ? (wid >> 1) * 4 + (lane >> 1) : qg;
      fqo = WS_FQ + (m2 * 16 + g) * 128; };
    const int sgo = WS_ST + (mg * 128 + wm * 32 + r) * 8;

    sfor<0, 70>([&](auto nc) AI {
      constexpr int n = decltype(nc)::v, u = n & 7;
      constexpr int k0 = n < 28 ? n : n < 56 ? n - 28 : 2 * (n - 56), k1 = n < 56 ? k0 : k0 + 1;
      f32x16& D0 = n < 28 ? A0 : n < 56 ? B0 : C0; f32x16& D1 = n < 28 ? A1 : n < 56 ? B1 : C0;
      constexpr bool early = n == 54 || n == 55 || n >= 68;
      PIN(wf[n % NWF][3]);
      if (u == 7) __builtin_amdgcn_s_setprio(3); if (u == 1) __builtin_amdgcn_s_setprio(2); if (u == 3) __builtin_amdgcn_s_setprio(1); if (u == 5) __builtin_amdgcn_s_setprio(0);
      SBAR();
      D0 = MFMA(cat(wf[n % NWF][0], wf[n % NWF][1]), xf[k0], D0); PIN(D0);
      SBAR();
      if constexpr (u == 6 || n == 68 || n == 2) {
        if constexpr (n == 54 || n == 68) { UNROLL for (int e = 0; e < RA; ++e) { colsum(n + e, 0); colsum(n + e, 1); } cs_out(); }
        if (n == 2) wait_bar<7>(); else if (n <= 14) wait_bar<2>(); else if (n == 22) wait_bar<4>(); else if (n == 30) wait_bar<6>();
        else if (n == 38) { if (bq) wait_bar<10>(); else wait_bar<6>(); } else if (n == 46) wait_bar<4>(); else if (n == 54) wait_bar<5>();
        else if (n == 62) lbar(); else wait_bar<4>();
        if (n == 46 && wid == 0) flag_st(ws, WS_FQ + (mg * 16 + ng) * 128, nonce);
        SBAR();
      }
      constexpr bool d1f = u == 6 || n == 68 || n == 2;
      if constexpr (d1f) { D1 = MFMA(cat(wf[n % NWF][2], wf[n % NWF][3]), xf[k1], D1); PIN(D1); SBAR(); }
      if constexpr (n + RA < 70) rd_w(n + RA);
      if constexpr (n == 68) {
        const lptr qb = lds + L_Q + (wid & 1) * 4096 + ((wid >> 1) & 1) * 2048 + lane * 16, qa = lds + L_Q + (wid & 1) * 4096 + (((wid >> 1) & 1) ^ 1) * 2048 + lane * 16;
        kf0 = ld128(lds + lane * 16); QA0 = ld128(qa); QB0 = ld128(qb); kf1 = ld128(lds + 1024 + lane * 16); QA1 = ld128(qa + 1024); QB1 = ld128(qb + 1024);
        cfix = ld128(lds + L_CS + wn * 1024 + lane * 16);
      }
      if constexpr (n == 69) { sa = MFMA(kf1, QA1, sa); sb = MFMA(kf1, QB1, sb); PIN(sa); PIN(sb); }
      if constexpr (n == 2) rd_x(4, 8);
      if constexpr (u == 6 && n < 24) rd_x(n + 2, n + 6);
      if constexpr (u == 0 && n > 0 && n < 24) rd_x(n + 4, n + 8);
      if constexpr (!early) colsum(n, 0);
      SBAR();
      if constexpr (!d1f) { D1 = MFMA(cat(wf[n % NWF][2], wf[n % NWF][3]), xf[k1], D1); PIN(D1); }
      SBAR();
      if constexpr (!early) colsum(n, 1);
      if constexpr (n == 0) dma_w(IC<2>{}); if constexpr (n == 1) dma_x(IC<3>{}); if constexpr (n == 2) dma_w(IC<3>{}); if constexpr (n == 4) dma_w(IC<4>{});
      if constexpr (n == 6) dma_x(IC<4>{}); if constexpr (n == 7) dma_x(IC<5>{}); if constexpr (n == 8) dma_w(IC<5>{}); if constexpr (n == 10) dma_w(IC<6>{});
      if constexpr (n == 14) dma_x(IC<6>{}); if constexpr (n == 15) dma_w(IC<7>{}); if constexpr (n == 18) dma_w(IC<8>{}); if constexpr (n == 20) dma_w(IC<9>{});
      if constexpr (n == 23) dma_w(IC<10>{}); if constexpr (n == 26) dma_w(IC<11>{}); if constexpr (n == 29) dma_w(IC<12>{}); if constexpr (n == 32) dma_w(IC<13>{});
      if constexpr (n == 48) touch(SRD(b < 196 ? wd : wo, b < 196 ? 1792 * 896 : 448 * 896), lane < 8 ? (((b < 196 ? b : b - 196) * 8 + wid) * 8 + lane) * 128 : OOB, lds0 + L_PF + wid * 256);
      if constexpr (n == 17) asm volatile("buffer_load_dwordx2 %0, %1, %2, 0 offen sc1" : "+v"(sg) : "v"(sgo), "s"(wsr) : "memory");
      if constexpr (n == 16) asm volatile("global_load_dwordx4 %0, %2, %3\n\tglobal_load_dwordx4 %1, %2, %3 offset:32" : "+v"(tb0), "+v"(tb1) : "v"(h * 16), "s"(&qkv_tab.v[bq ? 4 * ng + 2 * wn : 0]) : "memory");
      if constexpr (n >= 39 && n < 53 && (n & 1)) dma_w(IC<14 + (n - 39) / 2>{});
      if constexpr (n == 27) cs_out();
      if constexpr (n == 23) {
        PIN(sg); PIN(tb0); PIN(tb1);
        _Pragma("nounroll") for (unsigned sp = 0; !ALL(sg[1] == nonce) && sp < SPIN_CAP; ++sp)
          asm volatile("buffer_load_dwordx2 %0, %1, %2, 0 offen sc1\n\ts_waitcnt vmcnt(0)" : "+v"(sg) : "v"(sgo), "s"(wsr) : "memory");
        sig = U2F(sg[0] & 0xffff0000u); rstd = __builtin_amdgcn_rcpf(sig); mm = __builtin_amdgcn_perm(sg[0], sg[0], 0x01000100u); kg = -1.4426950408889634f * rstd;
        PIN(sig); PIN(rstd); PIN(kg); PIN(mm);
      }
      if constexpr (n == 30) fix2(A0, A1);
      if constexpr (n == 31) epi1(A0, 0);
      if constexpr (n == 34) epi1(A1, 1);
      if constexpr (n == FQP) { fq_off(); asm volatile("buffer_load_dword %0, %1, %2, 0 offen sc1" : "+v"(fl0) : "v"(fqo), "s"(wsr) : "memory"); }
      if constexpr (n == 55) fix2(B0, B1);
      if constexpr (n == 57) { hp = glu8(B0); PIN(hp); }
      if constexpr (n == 64) { hq = glu8(B1); PIN(hq); }
      if constexpr (n == 69) { st128(hp, wsr, lane * 16, hs2, SC1); st128(hq, wsr, lane * 16 + 1024, hs2, SC1); }
      if constexpr (n == FQC) {
        asm volatile("s_waitcnt vmcnt(0)" : "+v"(fl0) :: "memory");
        _Pragma("nounroll") for (unsigned sp = 0; !ALL(fl0 == nonce) && sp < SPIN_CAP; ++sp)
          asm volatile("buffer_load_dword %0, %1, %2, 0 offen sc1\n\ts_waitcnt vmcnt(0)" : "+v"(fl0) : "v"(fqo), "s"(wsr) : "memory");
        kvv = (unsigned)(WS_QKV + head * QH + (wid >> 1) * 16 * QT + 2048 + (wid & 1) * 2048 + lane * 16);
        dma<0, L_Q, true>(wsr, WS_QKV + head * QH + (qg * 4 + (wid >> 1)) * QT + (wid & 1) * 1024 + lane * 16, lds0 + wid * 1024); dma_kv(IC<0>{}, kvv); }
      if constexpr (n > FQC && n < FQC + 8) dma_kv(IC<n - FQC>{}, kvv);
      if constexpr (n == 67) {
        const int vc = 2 * (grp ^ wn) + (p >> 1);
        kb = lds + (wid >> 1) * 4096 + lane * 16;
        vb0 = lds + (wid >> 1) * 4096 + 2048 + vc * 512 + ((((4 * h) ^ (4 * vc)) + q) << 4) + (p & 1) * 8;
        vb1 = lds + (wid >> 1) * 4096 + 2048 + vc * 512 + ((((4 * h + 8) ^ (4 * vc)) + q) << 4) + (p & 1) * 8;
        PIN(kb); PIN(vb0); PIN(vb1);
      }
      if constexpr (n == 68) { sa = MFMA(kf0, QA0, Z); sb = MFMA(kf0, QB0, Z); PIN(sa); PIN(sb); ka0 = ld128(kb); ka1 = ld128(kb + 1024); va0 = cat(trd(vb0), trd(vb1)); }
    });
    SBAR();
    C0 = MFMA(cfix, ((u32x4){mm, mm, mm, mm}), C0);
    float ma = sa[0], mb = sb[0];
    UNROLL for (int i = 1; i < 16; ++i) { ma = __builtin_fmaxf(ma, sa[i]); mb = __builtin_fmaxf(mb, sb[i]); }
    const unsigned pa = pk(1.f - max32(ma), 0.f), pb = pk(1.f - max32(mb), 0.f);
    const bool p0 = (head & 1) && h == 0, p1 = !(head & 1) && h == 1;
    QA0[0] = p0 ? pa : QA0[0]; QA1[2] = p1 ? pa : QA1[2]; QB0[0] = p0 ? pb : QB0[0]; QB1[2] = p1 ? pb : QB1[2];
    s0 = MFMA(ka0, QA0, Z); s0 = MFMA(ka1, QA1, s0);
    PIN(s0); PIN(QB0); PIN(QB1);
    SBAR();
    st128(glu8(C0), wsr, ng < 14 ? lane * 16 : OOB, WS_H + (mt * 112 + 64 + ng * 2 + wn) * 1024, SC1);
    SBAR();
  }
  return phase23(x, wo, wd, ws, out, lds, nonce, QA0, QA1, QB0, QB1, ka0, ka1, va0, s0, kb, vb0, vb1, kvv, wid);
}

extern "C" void kernel_launch(void* const* d_in, const int* in_sizes, int n_in, void* d_out, int out_size, void* d_ws, size_t ws_size,
                              hipStream_t stream) {
  (void)hipFuncSetAttribute((const void*)fused_kernel, hipFuncAttributeMaxDynamicSharedMemorySize, LDS_BYTES);
  fused_kernel<<<256, 512, LDS_BYTES, stream>>>((const char*)d_in[0], (const char*)d_in[1], (const char*)d_in[2], (const char*)d_in[4], (const char*)d_in[5],
                                                (const char*)d_in[6], (char*)d_ws, (char*)d_out);
}
```

```cpp
#include <hip/hip_runtime.h>
#include <hip/hip_bf16.h>
#include <stdint.h>

typedef short bf16x8 __attribute__((ext_vector_type(8)));
typedef short s16x4 __attribute__((ext_vector_type(4)));
typedef float f32x16 __attribute__((ext_vector_type(16)));
typedef float f32x4 __attribute__((ext_vector_type(4)));
typedef float f32x2 __attribute__((ext_vector_type(2)));
typedef unsigned u32x4 __attribute__((ext_vector_type(4)));
typedef unsigned u32x2 __attribute__((ext_vector_type(2)));
typedef __bf16 bf2 __attribute__((ext_vector_type(2)));
typedef __amdgpu_buffer_rsrc_t srd_t;
#define LAS __attribute__((address_space(3)))
#define GAS __attribute__((address_space(1)))
typedef LAS char* lptr;
#define DI __device__ __forceinline__
#define AI __attribute__((always_inline))
#define MFMA(a, b, c) __builtin_amdgcn_mfma_f32_32x32x16_bf16(__builtin_bit_cast(bf16x8, (a)), __builtin_bit_cast(bf16x8, (b)), (c), 0, 0, 0)
#define PIN(x) asm volatile("" : "+v"(x))
#define SBAR() __builtin_amdgcn_sched_barrier(0)
#define RLX_AGENT __ATOMIC_RELAXED, __HIP_MEMORY_SCOPE_AGENT
#define EX(v) __builtin_amdgcn_exp2f(v)
#define F2U(x) __builtin_bit_cast(unsigned, (float)(x))
#define U2F(x) __builtin_bit_cast(float, (unsigned)(x))
#define ANY(p) (__builtin_amdgcn_ballot_w64(p) != 0)
#define ALL(p) (__builtin_amdgcn_ballot_w64(!(p)) == 0)
#define UNROLL _Pragma("unroll")
#define SRD(p, n) __builtin_amdgcn_make_buffer_rsrc((void*)(p), 0, (n), 0x00020000)

constexpr int WS_E = 0;
constexpr int WS_FQ = 4096;
constexpr int WS_FH = WS_FQ + 256 * 128;
constexpr int WS_FO = WS_FH + 256 * 128;
constexpr int WS_ST = WS_FO + 256 * 128;
constexpr int WS_QKV = 1 << 20;
constexpr int QT = 6144, QH = 64 * QT;
constexpr int WS_O = WS_QKV + 16 * QH;
constexpr int WS_H = WS_O + (2 << 20);
constexpr int WS_END = WS_H + 64 * 112 * 1024;
constexpr int OOB = 1 << 30;
constexpr int SC1 = 16;
constexpr unsigned SPIN_CAP = 1u << 20;

template <int I> struct IC { static constexpr int v = I; };
template <int B, int E, class F> DI void sfor(F&& f) { if constexpr (B < E) { f(IC<B>{}); sfor<B + 1, E>(f); } }
template <int N> DI void wait_bar() { asm volatile("s_waitcnt vmcnt(%0) lgkmcnt(0)\n\ts_barrier" :: "n"(N) : "memory"); }
template <int N> DI void vwait_bar() { asm volatile("s_waitcnt vmcnt(%0)\n\ts_barrier" :: "n"(N) : "memory"); }
DI void lbar() { asm volatile("s_waitcnt lgkmcnt(0)\n\ts_barrier" ::: "memory"); }
DI unsigned pk(float a, float b) { f32x2 v = {a, b}; return __builtin_bit_cast(unsigned, __builtin_convertvector(v, bf2)); }
DI float rbf(float a) { return U2F(pk(a, a) & 0xffff0000u); }
DI float dot2(unsigned a, unsigned b, float c) { return __builtin_amdgcn_fdot2_f32_bf16(__builtin_bit_cast(bf2, a), __builtin_bit_cast(bf2, b), c, false); }
DI u32x2 trd(lptr p) { return __builtin_bit_cast(u32x2, __builtin_amdgcn_ds_read_tr16_b64_v4i16((LAS s16x4*)p)); }
DI u32x4 cat(u32x2 a, u32x2 b) { return (u32x4){a[0], a[1], b[0], b[1]}; }
DI u32x4 ld128(lptr p) { return *(LAS u32x4*)p; }
DI unsigned bfi(unsigned m, unsigned a, unsigned b) { return (m & a) | (~m & b); }
template <int G, int L, bool DEV = false> DI void dma(srd_t srd, unsigned voff, unsigned lbase) {
  unsigned t;
  if constexpr (DEV)
    asm volatile("s_add_u32 m0, %3, %5\n\ts_mov_b32 %0, %4\n\tbuffer_load_dwordx4 %1, %2, %0 offen sc1 lds" : "=&s"(t) : "v"(voff), "s"(srd), "s"(lbase), "n"(G), "n"(L) : "memory", "scc");
  else
    asm volatile("s_add_u32 m0, %3, %5\n\ts_mov_b32 %0, %4\n\tbuffer_load_dwordx4 %1, %2, %0 offen lds" : "=&s"(t) : "v"(voff), "s"(srd), "s"(lbase), "n"(G), "n"(L) : "memory", "scc");
}
template <int L> DI void dma2(srd_t srd, unsigned voff0, unsigned voff1, unsigned lbase, unsigned ga, unsigned gb) {
  unsigned t;
  asm volatile("s_add_u32 m0, %4, %5\n\ts_mul_i32 %0, %6, %7\n\tbuffer_load_dwordx4 %1, %3, %0 offen lds\n\ts_add_u32 m0, m0, 1024\n\ts_nop 0\n\tbuffer_load_dwordx4 %2, %3, %0 offen lds"
               : "=&s"(t) : "v"(voff0), "v"(voff1), "s"(srd), "s"(lbase), "n"(L), "s"(ga), "s"(gb) : "memory", "scc");
}
template <int L> DI void dma1(srd_t srd, unsigned voff, unsigned lbase, unsigned ga, unsigned gb) {
  unsigned t;
  asm volatile("s_add_u32 m0, %3, %4\n\ts_mul_i32 %0, %5, %6\n\tbuffer_load_dwordx4 %1, %2, %0 offen lds" : "=&s"(t) : "v"(voff), "s"(srd), "s"(lbase), "n"(L), "s"(ga), "s"(gb) : "memory", "scc");
}
DI void touch(srd_t srd, unsigned voff, unsigned ldst) { asm volatile("s_mov_b32 m0, %2\n\ts_nop 0\n\tbuffer_load_dword %0, %1, 0 offen lds" :: "v"(voff), "s"(srd), "s"(ldst) : "memory"); }
DI void st128(u32x4 v, srd_t srd, int voff, int soff, int) { __builtin_amdgcn_raw_buffer_store_b128(v, srd, voff, soff, SC1); SBAR(); asm volatile("s_nop 1"); SBAR(); }
DI float swap32(float v, int h) {
  auto r = __builtin_amdgcn_permlane32_swap(F2U(v), F2U(v), false, false);
  return U2F(h ? r[0] : r[1]);
}
DI float max32(float v) {
  auto r = __builtin_amdgcn_permlane32_swap(F2U(v), F2U(v), false, false);
  return __builtin_fmaxf(U2F(r[0]), U2F(r[1]));
}
DI u32x4 widen(u32x2 a, u32x2 b) {
  auto r0 = __builtin_amdgcn_permlane32_swap(a[0], b[0], false, false); auto r1 = __builtin_amdgcn_permlane32_swap(a[1], b[1], false, false);
  return (u32x4){r0[0], r1[0], r0[1], r1[1]};
}
DI float wave_sum(float v) {
  v += U2F(__builtin_amdgcn_update_dpp(0u, F2U(v), 0x111, 0xf, 0xf, true));
  v += U2F(__builtin_amdgcn_update_dpp(0u, F2U(v), 0x112, 0xf, 0xf, true));
  v += U2F(__builtin_amdgcn_update_dpp(0u, F2U(v), 0x114, 0xf, 0xf, true));
  v += U2F(__builtin_amdgcn_update_dpp(0u, F2U(v), 0x118, 0xf, 0xf, true));
  v += U2F(__builtin_amdgcn_update_dpp(0u, F2U(v), 0x142, 0xa, 0xf, true));
  v += U2F(__builtin_amdgcn_update_dpp(0u, F2U(v), 0x143, 0xc, 0xf, true));
  return v;
}
DI unsigned flag_ld(const char* ws, int off) { return __hip_atomic_load((GAS unsigned*)(ws + off), RLX_AGENT); }
DI void flag_st(char* ws, int off, unsigned v) {
  if (__builtin_amdgcn_mbcnt_hi(~0u, __builtin_amdgcn_mbcnt_lo(~0u, 0u)) == 0) __hip_atomic_store((GAS unsigned*)(ws + off), v, RLX_AGENT); }

constexpr int RA = 2, NWF = RA + 1;
constexpr int PART = 14336;
constexpr int L_W = 65536;
constexpr int L_CS = 9 * 16384;
constexpr int L_PF = L_CS + 2048;
constexpr int L_Q = 65536;
constexpr int wpos(int m) { return m < 9 ? L_W + (m % 5) * 16384 : m == 9 ? 49152 : m == 10 ? L_W : m < 14 ? (m - 11) * 16384 : L_W + 16384 + (m - 14) * 8192; }
constexpr int FQP = 52, FQC = 59;
constexpr int L_WD = 65536;
constexpr int LDS_BYTES = 163840;
constexpr unsigned ONES2 = 0x3f803f80u, PAD0 = 0x00003f80u;
static_assert(L_PF + 2048 <= LDS_BYTES, "LDS map");

constexpr int DN0 = 7;
constexpr int kv_at(int s, int p) { return p == 0 && s > 0 && s + 3 < 16 ? (s + 3) * 2 + 1 : p == 7 && s + 4 < 16 ? (s + 4) * 2 : -1; }
constexpr int wd_at(int s, int p) {
  if (s >= 1 && s <= 3) return p == 1 ? s * 4 - 4 : p == 3 ? s * 4 - 3 : p == 4 ? s * 4 - 2 : p == 5 ? s * 4 - 1 : -1;
  for (int g = 0; g < 2; ++g) { if (s == 10 + 4 * g && p == 7) return 12 + 4 * g; if (s == 11 + 4 * g) return p == 0 ? 13 + 4 * g : p == 2 ? 14 + 4 * g : p == 5 ? 15 + 4 * g : -1; }
  return -1;
}
constexpr int h_at(int s, int p) { const int z = (s - 5) * 3 + (p == 1 ? 1 : p == 3 ? 2 : 3); if (s == 15) return p == 1 ? 24 : p == 3 ? 25 : p == 4 ? 26 : p == 7 ? 27 : -1;
  return s == 4 && p == 7 ? 0 : s >= 5 && (p == 1 || p == 3 || p == 4) && z < 24 ? z : -1; }
constexpr bool pf_at(int s, int p) { return s == 4 && p == 1; }
constexpr int n_at(int s, int p) { return (kv_at(s, p) >= 0) + (wd_at(s, p) >= 0) + (h_at(s, p) >= 0) + pf_at(s, p); }
constexpr int cnt_after(int s0, int p0, int s) { int n = 0; for (int z = s0 * 8 + p0 + 1; z < s * 8 + 6; ++z) n += n_at(z / 8, z % 8); return n; }
constexpr int dslot(int sub) { return L_WD + (sub % 3) * 32768; }

struct QkvTab { unsigned v[44][2][4]; };
constexpr QkvTab make_tab() {
  QkvTab t = {};
  for (int tile = 0; tile < 44; ++tile) for (int h = 0; h < 2; ++h) {
    const int col0 = tile * 32, sec = col0 / 448; unsigned* e = t.v[tile][h];
    e[0] = e[1] = e[2] = OOB; e[3] = 0;
    if (tile < 42) for (int pp = 0; pp < 2; ++pp) {
      const int cu = (col0 - sec * 448) / 8 + 2 * pp + h, j = cu / 7, w = cu % 7, hd = 2 * j + (w > 3), ch = w > 3 ? w - 3 : w;
      e[pp] = WS_QKV + sec * 2048 + hd * QH + ch * 512 + (sec == 2 ? ch * 64 : 0);
      if (w == 3) { e[2] = WS_QKV + sec * 2048 + (hd + 1) * QH; e[3] |= 1u << pp; }
    }
  }
  return t;
}
__device__ const QkvTab qkv_tab = make_tab();

#define LANE_IDS() int lane; asm volatile("v_mbcnt_lo_u32_b32 %0, -1, 0\n\tv_mbcnt_hi_u32_b32 %0, -1, %0" : "=v"(lane));        \
  const int r = lane & 31, h = lane >> 5, q = (lane & 15) >> 2, p = lane & 3, grp = (lane >> 4) & 1, rr = lane >> 3, cc = lane & 7
DI void phase23(const char* x, const char* wo, const char* wd, char* ws, char* out, lptr lds, unsigned nonce, u32x4 QA0, u32x4 QA1, u32x4 QB0, u32x4 QB1,
                u32x4 k0, u32x4 k1, u32x4 v0, f32x16 sa, lptr kb, lptr vb0, lptr vb1, unsigned kvv, int wid) {
  const unsigned lds0 = (unsigned)(uintptr_t)lds;
  const int b = __builtin_amdgcn_workgroup_id_x(), xcd = b & 7, idx = b >> 3;
  const int ng = (xcd & 3) * 4 + (idx & 3), mg = (xcd >> 2) * 8 + (idx >> 2);
  const srd_t wsr = SRD(ws, WS_END);
  const f32x16 Z = {};
  const int head = 2 * xcd + (idx & 1), qg = idx >> 1, odd = head & 1;
  const int idx3 = idx < 28 ? idx : idx - 28, mq3 = (idx3 * 37) >> 8, mg3 = xcd * 4 + mq3, ng3 = idx3 - mq3 * 7;
  LANE_IDS();
  const unsigned lw2k = lds0 + wid * 2048;
  auto dma_kv = [&](auto zc) AI { constexpr int z = decltype(zc)::v; dma<(z >> 1) * QT + (z & 1) * 1024, ((z >> 1) & 3) * 16384 + (z & 1) * 1024, true>(wsr, kvv, lw2k); };
  const int w3m = wid & 1, w3k = wid >> 1;
  u32x4 hf[28];
  const int ft = w3k & 1, fs = w3k >> 1;
  unsigned vo_d = 0; const unsigned ld_d = lds0 + wid * 4096;
  auto set_vo_d = [&]() AI { vo_d = (unsigned)(((idx < 28 ? w3k * 448 + w3m * 32 : OOB / 896) + rr) * 896 + ((cc ^ (grp << 2)) << 4) + ng3 * 128); PIN(vo_d); };
  const srd_t sD = SRD(wd, 1792 * 896);
  auto dma_d = [&](auto zc) AI { constexpr int z = decltype(zc)::v; dma<((z >> 2) * 64 + (z & 3) * 8) * 896, dslot(z >> 2) + (z & 3) * 1024>(sD, vo_d, ld_d); };
  int db0 = 0, db1 = 0, dbo = L_WD;
  auto set_db = [&]() AI { db0 = L_WD + w3k * 8192 + (4 * h + q) * 128 + p * 8 + ((grp ^ ((q >> 1) << 1)) << 5); db1 = db0 ^ 64; PIN(db0); PIN(db1); };
  auto rebase = [&](int o) AI { db0 += o - dbo; db1 += o - dbo; dbo = o; PIN(db0); PIN(db1); };
  f32x16 F0 = Z, F1 = Z;
  u32x4 dw0, dw1;
  auto rd_d0 = [&](int d) AI { const int o = dslot(d >> 2) + (d & 3) * 2048 - dbo; dw0 = cat(trd(lds + db0 + o), trd(lds + db0 + o + 1024)); };
  auto rd_d1 = [&](int d) AI { const int o = dslot(d >> 2) + (d & 3) * 2048 - dbo; dw1 = cat(trd(lds + db1 + o), trd(lds + db1 + o + 1024)); };
  {
    const int wq = wid & 1, kq = wid >> 1;
    u32x4 v1, pa, pb;
    float e0, e1, e2, e3;
    f32x16 OA = Z, OB = Z, sb;
    unsigned junk, fh = ~nonce; const int fho = WS_FH + ((mg3 >> 1) * 16 + (lane & 15)) * 128;
    const int hld = WS_H + ((mg3 * 2 + w3m) * 112 + w3k * 28) * 1024, hlv = idx < 28 ? lane * 16 : OOB;
    auto send = [&](const f32x16& a, int hh, int j) AI {
      const lptr d = kb + (((wid ^ (2 * j)) * 6144 + (j - 1) * 2048) - kq * 4096);
      *(LAS f32x4*)d = (f32x4){a[8 * hh], a[8 * hh + 1], a[8 * hh + 2], a[8 * hh + 3]}; *(LAS f32x4*)(d + 1024) = (f32x4){a[8 * hh + 4], a[8 * hh + 5], a[8 * hh + 6], a[8 * hh + 7]}; };
#define EX2(S, i, a, c) do { a = EX(S[i]); c = EX(S[(i) + 1]); PIN(a); PIN(c); } while (0)
#define STEP(FIRST, SC, SN, QN0, QN1, OP, OC, L0, L1, L2, L3, X0, X1) do {                                                       \
      SBAR(); SN = MFMA(k0, QN0, Z); SBAR(); L0;                                                                          \
      if (!(FIRST)) { pb[2] = pk(e0, e1); pb[3] = pk(e2, e3); PIN(pb); } SBAR(); EX2(SC, 0, e0, e1); SBAR(); X0; SBAR(); EX2(SC, 2, e2, e3); \
      SBAR(); if (!(FIRST)) OP = MFMA(v1, pb, OP); SBAR(); L1;                                                                          \
      pa[0] = pk(e0, e1); pa[1] = pk(e2, e3); PIN(pa); SBAR(); EX2(SC, 4, e0, e1); EX2(SC, 6, e2, e3);                    \
      SBAR(); SN = MFMA(k1, QN1, SN); SBAR(); L2;                                                                         \
      pa[2] = pk(e0, e1); pa[3] = pk(e2, e3); PIN(pa); SBAR(); EX2(SC, 8, e0, e1); SBAR(); X1; SBAR(); EX2(SC, 10, e2, e3); \
      SBAR(); OC = MFMA(v0, pa, OC); SBAR(); L3;                                                                          \
      pb[0] = pk(e0, e1); pb[1] = pk(e2, e3); PIN(pb); SBAR(); EX2(SC, 12, e0, e1); EX2(SC, 14, e2, e3); SBAR(); } while (0)
    sfor<0, 16>([&](auto sc_) AI {
      constexpr int s = decltype(sc_)::v, so = (s & 3) * 16384, sn = ((s + 1) & 3) * 16384;
      auto side = [&](auto pc) AI {
        constexpr int P = decltype(pc)::v, kv = kv_at(s, P), wz = wd_at(s, P), hz = h_at(s, P);
        if constexpr (s == 0 && P == 3) set_vo_d();
        if constexpr (s == 0 && P == 5) set_db();
        if constexpr (kv >= 0) dma_kv(IC<kv>{});
        if constexpr (wz >= 0) dma_d(IC<wz>{});
        if constexpr (pf_at(s, P)) {
          const int row = wid < 2 ? mq3 * 448 + 320 + wid * 64 + lane : mq3 * 112 + (wid - 2) * 56 + (lane < 56 ? lane : 55);
          asm volatile("buffer_load_dword %0, %1, %2, 0 offen" : "=&v"(junk) : "v"(wid < 4 && idx < 28 ? row * 896 + ng3 * 128 : OOB), "s"(SRD(wid < 2 ? wd : wo, wid < 2 ? 1792 * 896 : 448 * 896)) : "memory");
        }
        if constexpr (hz >= 0) hf[hz] = __builtin_amdgcn_raw_buffer_load_b128(wsr, hlv + (hz & 3) * 1024, hld + (hz >> 2) * 4096, SC1);
      };
      auto mid = [&]() AI {
        if (s == 4 && wid == 0) {
          asm volatile("s_waitcnt vmcnt(%1)" : "+v"(fh) : "n"(cnt_after(3, 6, 4)) : "memory");
          _Pragma("nounroll") for (unsigned sp = 0; !ALL(fh == nonce) && sp < SPIN_CAP; ++sp) { fh = flag_ld(ws, fho); PIN(fh); }
        }
        if constexpr (s == 0) vwait_bar<5 + cnt_after(0, -1, 0)>();
        else if constexpr (s == 1) vwait_bar<cnt_after(0, -1, 1)>();
        else if constexpr (s == 14) vwait_bar<0>(); else if constexpr (s < 14) vwait_bar<cnt_after(s - 1, 0, s)>();
        if (s == 1 && wid == 0) flag_st(ws, WS_FH + (mg * 16 + ng) * 128, nonce);
        if (s == 3 && wid == 0) asm volatile("buffer_load_dword %0, %1, %2, 0 offen sc1" : "+v"(fh) : "v"(fho), "s"(wsr) : "memory");
        if constexpr (s == 6) asm volatile("" :: "v"(junk));
        if constexpr (s == 13) rebase(L_WD + 65536);
        if constexpr (s == 14) { UNROLL for (int k = 23; k >= 8; --k) PIN(hf[k]); }
        __builtin_amdgcn_s_setprio(3);
      };
      auto dn0 = [&](int d) AI { if (d >= 0) { F0 = MFMA(dw0, hf[d], F0); PIN(F0); } if (d >= -1) rd_d0(d + 1); };
      auto dn1 = [&](int d) AI { if (d >= 0) { F1 = MFMA(dw1, hf[d], F1); PIN(F1); } if (d >= -1) rd_d1(d + 1); };
      STEP(s == 0, sa, sb, QB0, QB1, OB, OA,
           k0 = ld128(kb + sn); side(IC<0>{}); __builtin_amdgcn_s_setprio(2),
           v1 = cat(trd(vb0 + so + 256), trd(vb1 + so + 256)); side(IC<1>{}),
           k1 = ld128(kb + sn + 1024); side(IC<2>{}); __builtin_amdgcn_s_setprio(1),
           side(IC<3>{}), dn0(s - DN0), if (s == 15) dn1(8));
      STEP(false, sb, sa, QA0, QA1, OA, OB,
           __builtin_amdgcn_s_setprio(0); side(IC<4>{}),
           side(IC<5>{}),
           mid(); if (s == 15) send(OA, 0, 1),
           v0 = cat(trd(vb0 + sn), trd(vb1 + sn)); side(IC<7>{}); if (s == 15) send(OA, 1, 3), if (s < 15) dn1(s - DN0); else dn0(9), if (s == 15) dn1(9));
    });
    pb[2] = pk(e0, e1); pb[3] = pk(e2, e3);
    OB = MFMA(v1, pb, OB);
    __builtin_amdgcn_s_setprio(0);
    send(OB, 1, 2);
    *(LAS float*)(kb + (49152 + wq * 1024)) = odd ? OB[0] : OB[4];
    {
    LANE_IDS();
    const bool padl = h != odd;
    const int qt = qg * 4 + wq * 2 + ft, lw = (wid & 3) | (odd ? 0 : 4);
    const lptr pp = lds + wid * 6144 + lane * 16, lp = lds + lw * 6144 + (odd ? 0 : 1536) + r * 16;
    const lptr lq = lds + 49152 + (lw >> 1) * 4096 + wq * 1024 + (odd ? 0 : 512) + r * 16;
    f32x4 o0 = {OB[0], OB[1], OB[2], OB[3]}, o1 = {OB[4], OB[5], OB[6], OB[7]};
    lbar();
    const float l0 = *(LAS float*)lq, l1 = *(LAS float*)lp, l2 = *(LAS float*)(lp + 2048), l3 = *(LAS float*)(lp + 4096);
    f32x4 t[6];
    UNROLL for (int k = 0; k < 6; ++k) t[k] = *(LAS f32x4*)(pp + k * 1024);
    SBAR();
    float l = (l0 + l1) + (l2 + l3);
    UNROLL for (int k = 0; k < 3; ++k) { o0 += t[2 * k]; o1 += t[2 * k + 1]; }
    if (__builtin_expect(ANY(!(l <= 1.8446744e19f && l >= 5.4210109e-20f)), 0)) {
      const int qo = WS_QKV + head * QH + qt * QT + lane * 16;
      u32x4 q0 = __builtin_amdgcn_raw_buffer_load_b128(wsr, qo, 0, SC1), q1 = __builtin_amdgcn_raw_buffer_load_b128(wsr, qo + 1024, 0, SC1);
      f32x16 O = Z; float m = -__builtin_inff();
      _Pragma("nounroll") for (int t = 0; t < 64; ++t) {
        const int ko = WS_QKV + head * QH + t * QT + 2048;
        f32x16 s = MFMA(__builtin_amdgcn_raw_buffer_load_b128(wsr, ko + lane * 16, 0, SC1), q0, Z);
        s = MFMA(__builtin_amdgcn_raw_buffer_load_b128(wsr, ko + 1024 + lane * 16, 0, SC1), q1, s);
        float mn = m;
        UNROLL for (int i = 0; i < 16; ++i) mn = __builtin_fmaxf(mn, s[i]);
        mn = max32(mn);
        const float f = EX(m - mn); m = mn;
        UNROLL for (int i = 0; i < 16; ++i) O[i] *= f;
        UNROLL for (int st = 0; st < 2; ++st) {
          unsigned vv[8];
          UNROLL for (int j = 0; j < 8; ++j) vv[j] = __builtin_amdgcn_raw_buffer_load_b16(wsr, ko + 2048 + (r >> 3) * 512 + (((16 * st + 8 * (j >> 2) + 4 * h + (j & 3)) ^ (4 * (r >> 3))) << 4) + (r & 7) * 2, 0, SC1) & 0xffffu;
          O = MFMA(((u32x4){vv[0] | vv[1] << 16, vv[2] | vv[3] << 16, vv[4] | vv[5] << 16, vv[6] | vv[7] << 16}),
                   ((u32x4){pk(EX(s[8 * st] - m), EX(s[8 * st + 1] - m)), pk(EX(s[8 * st + 2] - m), EX(s[8 * st + 3] - m)),
                            pk(EX(s[8 * st + 4] - m), EX(s[8 * st + 5] - m)), pk(EX(s[8 * st + 6] - m), EX(s[8 * st + 7] - m))}), O);
        }
      }
      const float lc = odd ? O[0] : O[12], lx = swap32(lc, h); l = padl ? lc : lx;
      o0 = fs ? (f32x4){O[8], O[9], O[10], O[11]} : (f32x4){O[0], O[1], O[2], O[3]};
      o1 = fs ? (f32x4){O[12], O[13], O[14], O[15]} : (f32x4){O[4], O[5], O[6], O[7]};
    }
    const float ri = __builtin_amdgcn_rcpf(l);
    const float r0 = padl && odd && fs == 0 ? 0.f : ri, r1 = padl && !odd && fs == 1 ? 0.f : ri;
    st128((u32x4){pk(o0[0] * r0, o0[1] * r0), pk(o0[2] * r0, o0[3] * r0), pk(o1[0] * r1, o1[1] * r1), pk(o1[2] * r1, o1[3] * r1)},
                                           wsr, lane * 16, WS_O + (qt * 16 + head) * 2048 + fs * 1024, SC1);
    }
  }
  if (idx >= 28) { wait_bar<0>();
    if (wid == 0) {
      flag_st(ws, WS_FO + (qg * 16 + head) * 128, nonce);
      if (b == 255) {
        LANE_IDS();
        _Pragma("nounroll") for (unsigned sp = 0; sp < SPIN_CAP; ++sp) {
          unsigned d = 0;
          UNROLL for (int i = 0; i < 4; ++i) d |= flag_ld(ws, WS_FQ + lane * 512 + i * 128) ^ nonce;
          if (ALL(d == 0)) break;
        }
        flag_st(ws, WS_E, nonce);
      }
    }
    return;
  }
  {
    lbar();
    const srd_t sO = SRD(wo, 448 * 896); const unsigned wov = vo_d + (wid * 56 - w3k * 448 - w3m * 32) * 896, wol = lds0 + wid * 7168;
    auto dma_o = [&](auto ic) AI { constexpr int i = decltype(ic)::v; dma<i * 8 * 896, i * 1024>(sO, wov, wol); };
    sfor<0, 4>(dma_o);
    u32x4 ew0, ew1;
    auto down2 = [&](int d, bool more) AI {
      const int o = dslot((d + 1) >> 2) + ((d + 1) & 3) * 2048 - dbo;
      ew0 = cat(trd(lds + db0 + o), trd(lds + db0 + o + 1024)); ew1 = cat(trd(lds + db1 + o), trd(lds + db1 + o + 1024));
      if (d == 10 || d == 22) rebase(L_WD);
      SBAR(); F0 = MFMA(dw0, hf[d], F0); F1 = MFMA(dw1, hf[d], F1); PIN(F0); PIN(F1); SBAR();
      if (more) { rd_d0(d + 2); rd_d1(d + 2); }
      SBAR(); F0 = MFMA(ew0, hf[d + 1], F0); F1 = MFMA(ew1, hf[d + 1], F1); PIN(F0); PIN(F1); SBAR();
    };
    static_assert(DN0 == 7, "units 0..9 ran beside the attention");
    down2(10, true); sfor<4, 7>(dma_o);
    vwait_bar<7>();
    if (wid == 0) flag_st(ws, WS_FO + (qg * 16 + head) * 128, nonce);
    sfor<20, 22>(dma_d); down2(12, true); sfor<22, 24>(dma_d); down2(14, true);
    vwait_bar<4>();
    LANE_IDS();
    const int mt = mg3 * 2 + w3m, orot = ng3 & 1;
    unsigned fo = ~nonce; const int foo = WS_FO + ((mg3 >> 1) * 16 + (lane & 15)) * 128;
    sfor<24, 26>(dma_d); down2(16, true); sfor<26, 28>(dma_d);
    if (wid == 0) asm volatile("buffer_load_dword %0, %1, %2, 0 offen sc1" : "+v"(fo) : "v"(foo), "s"(wsr) : "memory");
    down2(18, false);
    u32x4 wo0[8], wo1[8];
    UNROLL for (int k = 0; k < 8; ++k) {
      const int wbo = w3k * (PART - 8192) - L_WD + ((k >> 2) ^ orot) * 7168, o = (((k >> 1) & 1) * 24 + (k & 1) * 16) * 128;
      const lptr wb0 = lds + db0 + wbo, wb1 = lds + db1 + wbo;
      wo0[k] = cat(trd(wb0 + o), trd(wb0 + o + 1024)); wo1[k] = cat(trd(wb1 + o), trd(wb1 + o + 1024));
    }
    rebase(L_WD + 65536);
    if (wid == 0) {
      asm volatile("s_waitcnt vmcnt(0)" : "+v"(fo) :: "memory");
      _Pragma("nounroll") for (unsigned sp = 0; !ALL(fo == nonce) && sp < SPIN_CAP; ++sp) { fo = flag_ld(ws, foo); PIN(fo); }
    }
    wait_bar<0>();
    UNROLL for (int k = 27; k >= 24; --k) PIN(hf[k]);
    u32x4 of[8];
    const int oso = WS_O + (mt * 16 + w3k * 4) * 2048 + orot * 4096;
    UNROLL for (int k = 0; k < 4; ++k) of[k] = __builtin_amdgcn_raw_buffer_load_b128(wsr, lane * 16 + k * 1024, oso, SC1);
    SBAR();
    rd_d0(20); rd_d1(20); down2(20, true); down2(22, true);
    UNROLL for (int k = 4; k < 8; ++k) of[k] = __builtin_amdgcn_raw_buffer_load_b128(wsr, lane * 16 + (k & 3) * 1024, oso ^ 4096, SC1);
    const unsigned xo = (unsigned)((mt * 32 + w3k * 8 + rr) * 896 + ng3 * 128 + cc * 16);
    const u32x4 xv = *(const u32x4*)(x + xo);
    SBAR();
    down2(24, true); down2(26, false);
    auto xch_send = [&](const f32x16& a0, const f32x16& a1, int base) AI {
      UNROLL for (int c = 0; c < 8; ++c) { const f32x16& a = c < 4 ? a0 : a1; const int o = (c & 3) * 4;
        *(LAS f32x4*)(lds + (base + wid * 8192 + c * 1024) + ((lane ^ (2 * c)) << 4)) = (f32x4){a[o], a[o + 1], a[o + 2], a[o + 3]}; } };
    int xro = w3m * 8192 + cc * 1024 + (((w3k * 8 + rr) ^ (2 * cc)) << 4); PIN(xro);
    xch_send(F0, F1, 0);
    f32x16 S0 = Z, S1 = Z;
    UNROLL for (int k = 0; k < 4; ++k) { S0 = MFMA(wo0[k], of[k], S0); S1 = MFMA(wo1[k], of[k], S1); }
    PIN(S0); PIN(S1);
    lbar();
    f32x4 g0 = *(LAS f32x4*)(lds + xro), g1 = *(LAS f32x4*)(lds + xro + 512);
    UNROLL for (int k = 1; k < 4; ++k) { g0 += *(LAS f32x4*)(lds + xro + k * 16384); g1 += *(LAS f32x4*)(lds + xro + k * 16384 + 512); }
    UNROLL for (int k = 4; k < 8; ++k) { S0 = MFMA(wo0[k], of[k], S0); S1 = MFMA(wo1[k], of[k], S1); }
    UNROLL for (int i = 0; i < 4; ++i) {
      g0[i] = g0[i] * __builtin_amdgcn_rcpf(1.f + EX(g0[i] * -1.4426950408889634f)); g1[i] = g1[i] * __builtin_amdgcn_rcpf(1.f + EX(g1[i] * -1.4426950408889634f)); }
    xch_send(S0, S1, L_WD);
    f32x4 y0 = {U2F(xv[0] << 16), U2F(xv[0] & 0xffff0000u), U2F(xv[1] << 16), U2F(xv[1] & 0xffff0000u)}, y1 = {U2F(xv[2] << 16), U2F(xv[2] & 0xffff0000u), U2F(xv[3] << 16), U2F(xv[3] & 0xffff0000u)};
    xro += L_WD;
    PIN(g0); PIN(g1); PIN(y0); PIN(y1); PIN(xro);
    lbar();
    f32x4 t[8];
    UNROLL for (int k = 0; k < 8; ++k) t[k] = *(LAS f32x4*)(lds + xro + (k >> 1) * 16384 + (k & 1) * 512);
    SBAR();
    UNROLL for (int k = 0; k < 4; ++k) UNROLL for (int i = 0; i < 4; ++i) { y0[i] = __builtin_fmaf(t[2 * k][i], g0[i], y0[i]); y1[i] = __builtin_fmaf(t[2 * k + 1][i], g1[i], y1[i]); }
    __builtin_amdgcn_raw_buffer_store_b128((u32x4){pk(y0[0], y0[1]), pk(y0[2], y0[3]), pk(y1[0], y1[1]), pk(y1[2], y1[3])}, SRD(out, 2048 * 896), xo, 0, SC1);
  }
}

#if defined(__HIP_DEVICE_COMPILE__)
#define NOPK __attribute__((target("no-packed-fp32-ops")))
#else
#define NOPK
#endif
__global__ __launch_bounds__(512, 2) NOPK void fused_kernel(const char* x, const char* wqkv, const char* wo, const char* wg, const char* wu, const char* wd,
                                                       char* ws, char* out) {
  extern __shared__ __attribute__((aligned(16))) char smem[];
  const lptr lds = (lptr)smem;
  const unsigned lds0 = (unsigned)(uintptr_t)lds;
  const int tid = __builtin_amdgcn_workitem_id_x(), lane = tid & 63, wid = __builtin_amdgcn_readfirstlane(tid >> 6);
  const int rr = lane >> 3, cc = lane & 7;
  const int b = __builtin_amdgcn_workgroup_id_x(), xcd = b & 7, idx = b >> 3;
  const int ng = (xcd & 3) * 4 + (idx & 3), mg = (xcd >> 2) * 8 + (idx >> 2);
  {
    unsigned long long pc; asm volatile("s_getpc_b64 %0" : "=s"(pc));
    touch(SRD(pc & ~127ull, 28672), (idx * 8 + (tid >> 6)) * 128, lds0 + L_PF);
  }
  const int rot = (mg & 7) == 7 ? 3 : mg & 7;
  unsigned kc[7];
  UNROLL for (int c = 0; c < 7; ++c) kc[c] = (unsigned)((c + rot >= 7 ? c + rot - 7 : c + rot) * 64);
  const srd_t xr = SRD(x, 2048 * 896);
  const unsigned xv0 = (unsigned)((mg * 128 + wid * 16 + rr) * 896 + ((cc ^ rr) << 4));
  const unsigned xv1 = (unsigned)((mg * 128 + wid * 16 + 8 + (rr ^ 1)) * 896 + ((cc ^ rr ^ 1) << 4));
  const unsigned lw2k = lds0 + wid * 2048;
  auto dma_x = [&](auto cc_) AI { constexpr int c = decltype(cc_)::v; dma2<(c & 3) * 16384>(xr, xv0, xv1, lw2k, kc[c], 2); };
  const srd_t wsr = SRD(ws, WS_END);
  u32x4 srow; unsigned nonce;
  const int srw = mg * 128 + ng * 8 + wid;
  asm volatile("buffer_load_dwordx4 %0, %1, %2, %3 offen" : "=&v"(srow) : "v"(lane * 16), "s"(SRD(x, srw * 896 + 896)), "s"(srw * 896) : "memory");
  dma_x(IC<0>{});
  SBAR();
  const int r = lane & 31, h = lane >> 5, q = (lane & 15) >> 2, p = lane & 3, grp = (lane >> 4) & 1;
  const f32x16 Z = {};
  const int head = 2 * xcd + (idx & 1), qg = idx >> 1;
  auto dma_kv = [&](auto zc, unsigned kvv) AI { constexpr int z = decltype(zc)::v; dma<(z >> 1) * QT + (z & 1) * 1024, ((z >> 1) & 3) * 16384 + (z & 1) * 1024, true>(wsr, kvv, lw2k); };
  u32x4 QA0, QA1, QB0, QB1, kf0, kf1, ka0, ka1, va0; f32x16 s0; lptr kb, vb0, vb1; unsigned kvv = 0;
  {
    const int wm = wid & 3, wn = wid >> 2;
    const bool bq = ng <= 10;
    const unsigned st1 = bq ? 2688 : 3584;
    unsigned va = (16 * wm + rr) * st1 + (((cc << 4) ^ (grp << (bq ? 6 : 5))) ^ (bq ? 0 : wn << 6)) + (bq ? 2 * ng + wn : ng + 12) * 128, vb = va + 8 * st1;
    const char* wgu = wn ? wu : wg;
    const srd_t s2 = SRD(wgu, 448 * 3584), s1 = SRD(bq ? wqkv : wgu, 448 * st1);
    auto dma_w = [&](auto mc) AI {
      constexpr int m = decltype(mc)::v;
      if constexpr (m == 7) { va = (unsigned)((16 * wm + rr) * 3584 + ((cc ^ (grp << 1) ^ (wn << 2)) << 4)) + ng * 128; vb = va + 8 * 3584; PIN(va); PIN(vb); }
      if constexpr (m == 14) {
        va = (unsigned)((16 * wm + (lane >> 2)) * 3584 + (((lane & 3) ^ (wn << 1)) << 4)) + 2048 + ng * 64;
        if (ng >= 14) va = OOB;
        PIN(va);
      }
      if constexpr (m < 7) dma2<wpos(m)>(s1, va, vb, lw2k, kc[m], st1);
      else if constexpr (m < 14) dma2<wpos(m)>(s2, va, vb, lw2k, kc[m - 7], 3584);
      else dma1<wpos(m)>(s2, va, lds0 + wid * 1024, kc[m - 14], 3584);
    };
    dma_w(IC<0>{});
    unsigned epoch;
    asm volatile("buffer_load_dword %0, off, %1, 0 sc1" : "=&v"(epoch) : "s"(wsr) : "memory");
    dma_x(IC<1>{});
    SBAR();
    const int rowb = (8 * h + q) * 128 + p * 8;
    int b20 = grp * 8192 + rowb + (((2 * wn) ^ (q >> 1) ^ (grp << 1)) << 5);
    int b10 = bq ? wn * 8192 + rowb + ((grp ^ ((q >> 1) << 1)) << 5) : b20, b11 = b10 ^ (bq ? 64 : 32);
    int xb[4];
    UNROLL for (int i = 0; i < 4; ++i) { xb[i] = (4 * wm + (r >> 3)) * 1024 + ((r & 7) ^ ((r >> 3) & 1)) * 128 + (((2 * i + h) ^ (r & 7)) << 4); PIN(xb[i]); }
    PIN(b10); PIN(b11); PIN(b20);
    UNROLL for (int c = 2; c < 7; ++c) asm volatile("" : "+s"(kc[c]));
    SBAR();
    u32x4 xf[28];
    auto row_stats = [&]() AI {
      float a = dot2(srow[0], ONES2, 0.f), c = dot2(srow[0], srow[0], 0.f);
      UNROLL for (int i = 1; i < 4; ++i) { a = dot2(srow[i], ONES2, a); c = dot2(srow[i], srow[i], c); }
      a = wave_sum(a); c = wave_sum(c);
      const float mean = a * (1.f / 448.f), var = __builtin_fmaxf(c * (1.f / 448.f) - mean * mean, 0.f);
      unsigned g = pk(-mean, __builtin_amdgcn_sqrtf(rbf(rbf(var) + 1e-5f))); PIN(g); SBAR();
      asm volatile("s_waitcnt vmcnt(5)" : "+v"(epoch) :: "memory");
      nonce = __builtin_amdgcn_readfirstlane(epoch) * 0x9E3779B1u + 0x7F4A7C15u;
      if (lane == 63) __hip_atomic_store((GAS unsigned long long*)(ws + WS_ST + srw * 8), (unsigned long long)nonce << 32 | g, RLX_AGENT);
    };
    auto rd_x = [&](int k0, int k1) AI { UNROLL for (int ks = k0; ks < k1; ++ks) xf[ks] = ld128(lds + xb[ks & 3] + ((ks >> 2) & 3) * 16384); };
    u32x2 wf[NWF][4];
    int ra0 = 0, ra1 = 0;
    auto rd_w = [&](int n) AI {
      if (n < 56) {
        const int m = n >> 2, o = (n & 3) * 2048;
        if ((n & 3) == 0) { ra0 = (m < 7 ? b10 : b20) + wpos(m); ra1 = (m < 7 ? b11 : b20 ^ 32) + wpos(m); PIN(ra0); PIN(ra1); }
        wf[n % NWF][0] = trd(lds + ra0 + o); wf[n % NWF][1] = trd(lds + ra0 + o + 512); wf[n % NWF][2] = trd(lds + ra1 + o); wf[n % NWF][3] = trd(lds + ra1 + o + 512);
      } else {
        const int o = ((n - 56) >> 1) * 8192 + (n & 1) * 2048;
        if (n == 56) { ra0 = wpos(14) + grp * 4096 + (8 * h + q) * 64 + ((wn ^ grp) << 5) + p * 8; PIN(ra0); }
        wf[n % NWF][0] = trd(lds + ra0 + o); wf[n % NWF][1] = trd(lds + ra0 + o + 256); wf[n % NWF][2] = trd(lds + ra0 + o + 1024); wf[n % NWF][3] = trd(lds + ra0 + o + 1280);
      }
    };
    SBAR();
    dma_w(IC<1>{});
    {
      const int g = idx * 8 + wid;
      const char* tp = wid == 0 ? wqkv : wid == 1 ? wg : wid == 2 ? wu : wid == 3 ? wd : wid == 4 ? wo : wid == 5 ? out : ws;
      const int tn = wid == 0 ? 448 * 2688 : wid == 4 ? 448 * 896 : wid == 5 ? 2048 * 896 : wid >= 6 ? WS_END : 448 * 3584;
      const unsigned to = wid < 6 ? (lane < 1 ? (idx * 1 + lane) * 65536 : OOB) : (lane < 5 ? ((idx * 2 + wid - 6) * 5 + lane) * 65536 : OOB);
      asm volatile("s_mov_b32 m0, %2\n\ts_nop 0\n\tbuffer_load_dword %0, %1, 0 offen sc1 lds" :: "v"(to), "s"(SRD(tp, tn)), "s"(lds0 + L_PF + wid * 256) : "memory");
    }
    asm volatile("s_waitcnt vmcnt(10)" : "+v"(srow) :: "memory");
    row_stats();
    SBAR();
    lbar();
    UNROLL for (int n = 0; n < RA; ++n) rd_w(n);
    rd_x(0, 4);
    dma_x(IC<2>{});
    f32x16 A0 = Z, A1 = Z, B0 = Z, B1 = Z, C0 = Z;
    float cs0 = 0.f, cs1 = 0.f, rstd = 0.f, sig = 0.f, kg = 0.f; unsigned mm = 0; u32x2 sg = {0u, ~nonce};
    const int mt = mg * 4 + wm;
    const unsigned csel = ((wm & 1) ? 0x05040c0cu : 0x01000c0cu) + ((wm >> 1) ? 0x02020000u : 0u);
    auto colsum = [&](int n, int hh) AI {
      cs0 += U2F(__builtin_amdgcn_perm(wf[n % NWF][2 + hh][0], wf[n % NWF][hh][0], csel)); cs1 += U2F(__builtin_amdgcn_perm(wf[n % NWF][2 + hh][1], wf[n % NWF][hh][1], csel));
      PIN(cs0); PIN(cs1);
    };
    auto cs_out = [&]() AI {
      const float cs = cs0 + cs1, hi = rbf(cs); *(LAS unsigned*)(lds + L_CS + wn * 1024 + lane * 16 + ((wm & 1) * 2 + (wm >> 1)) * 4) = pk(hi, cs - hi); cs0 = 0.f; cs1 = 0.f; };
    auto fix2 = [&](f32x16& a0, f32x16& a1) AI {
      const u32x4 c = ld128(lds + L_CS + wn * 1024 + lane * 16);
      a0 = MFMA(c, ((u32x4){mm, mm, 0u, 0u}), a0); a1 = MFMA(c, ((u32x4){0u, 0u, mm, mm}), a1); PIN(a0); PIN(a1); };
    auto glu = [&](const f32x16& acc, int i) AI { return acc[i + 8] * __builtin_amdgcn_rcpf(__builtin_fmaf(EX(acc[i] * kg), sig, sig)); };
    auto glu8 = [&](const f32x16& acc) AI { return (u32x4){pk(glu(acc, 0), glu(acc, 1)), pk(glu(acc, 2), glu(acc, 3)), pk(glu(acc, 4), glu(acc, 5)), pk(glu(acc, 6), glu(acc, 7))}; };
    const int hs2 = WS_H + (mt * 112 + ng * 4 + wn * 2) * 1024;
    u32x4 tb0 = {0u, 0u, 0u, 0u}, tb1 = tb0;
    auto epi1 = [&](const f32x16& acc, int t) AI {
      if (bq) {
        const u32x4 e = t ? tb1 : tb0; const unsigned o[3] = {e[0] ^ (r << 4), e[1] ^ (r << 4), e[2] ^ (r << 4)};
        const unsigned m[2] = {(unsigned)__builtin_amdgcn_sbfe(e[3], 0, 1), (unsigned)__builtin_amdgcn_sbfe(e[3], 1, 1)};
        const float sc = 2 * ng + wn < 7 ? rstd * (0.18898223650461362f * 1.4426950408889634f) : rstd;
        u32x4 v[2];
        UNROLL for (int pp = 0; pp < 2; ++pp)
          v[pp] = widen((u32x2){pk(acc[8 * pp] * sc, acc[8 * pp + 1] * sc), pk(acc[8 * pp + 2] * sc, acc[8 * pp + 3] * sc)},
                        (u32x2){pk(acc[8 * pp + 4] * sc, acc[8 * pp + 5] * sc), pk(acc[8 * pp + 6] * sc, acc[8 * pp + 7] * sc)});
        u32x4 d0 = {v[0][0], v[0][1], bfi(m[0], PAD0, v[0][2]), ~m[0] & v[0][3]}, d1 = {v[1][0], v[1][1], bfi(m[1], PAD0, v[1][2]), ~m[1] & v[1][3]};
        u32x4 d2 = {PAD0, 0u, bfi(m[0], v[0][2], v[1][2]), bfi(m[0], v[0][3], v[1][3])};
        PIN(d0); PIN(d1); PIN(d2); SBAR();
        __builtin_amdgcn_raw_buffer_store_b128(d0, wsr, o[0], mt * QT, SC1); __builtin_amdgcn_raw_buffer_store_b128(d1, wsr, o[1], mt * QT, SC1);
        st128(d2, wsr, o[2], mt * QT, SC1);
      } else st128(glu8(acc), wsr, lane * 16 + t * 1024, hs2 + 12 * 4096, SC1);
    };
    u32x4 hp, hq; unsigned fl0 = ~nonce; f32x16 sa, sb; u32x4 cfix;
    int fqo = 0;
    auto fq_off = [&]() AI {
      const int c0 = (lane < 8 ? 448 + (wid & 1) * 448 : 0) + head * 28 - (head & 1) * 4 + (lane & 1) * 31, g = c0 >> 7, m2 = lane < 8 ? (wid >> 1) * 4 + (lane >> 1) : qg;
      fqo = WS_FQ + (m2 * 16 + g) * 128; };
    const int sgo = WS_ST + (mg * 128 + wm * 32 + r) * 8;

    sfor<0, 70>([&](auto nc) AI {
      constexpr int n = decltype(nc)::v, u = n & 7;
      constexpr int k0 = n < 28 ? n : n < 56 ? n - 28 : 2 * (n - 56), k1 = n < 56 ? k0 : k0 + 1;
      f32x16& D0 = n < 28 ? A0 : n < 56 ? B0 : C0; f32x16& D1 = n < 28 ? A1 : n < 56 ? B1 : C0;
      constexpr bool early = n == 54 || n == 55 || n >= 68;
      PIN(wf[n % NWF][3]);
      if (u == 7) __builtin_amdgcn_s_setprio(3); if (u == 1) __builtin_amdgcn_s_setprio(2); if (u == 3) __builtin_amdgcn_s_setprio(1); if (u == 5) __builtin_amdgcn_s_setprio(0);
      SBAR();
      D0 = MFMA(cat(wf[n % NWF][0], wf[n % NWF][1]), xf[k0], D0); PIN(D0);
      SBAR();
      if constexpr (u == 6 || n == 68 || n == 2) {
        if constexpr (n == 54 || n == 68) { UNROLL for (int e = 0; e < RA; ++e) { colsum(n + e, 0); colsum(n + e, 1); } cs_out(); }
        if (n == 2) wait_bar<8>(); else if (n <= 14) wait_bar<2>(); else if (n == 22) wait_bar<4>(); else if (n == 30) wait_bar<6>();
        else if (n == 38) { if (bq) wait_bar<10>(); else wait_bar<6>(); } else if (n == 46) wait_bar<4>(); else if (n == 54) wait_bar<5>();
        else if (n == 62) lbar(); else wait_bar<4>();
        if (n == 46 && wid == 0) flag_st(ws, WS_FQ + (mg * 16 + ng) * 128, nonce);
        SBAR();
      }
      constexpr bool d1f = u == 6 || n == 68 || n == 2;
      if constexpr (d1f) { D1 = MFMA(cat(wf[n % NWF][2], wf[n % NWF][3]), xf[k1], D1); PIN(D1); SBAR(); }
      if constexpr (n + RA < 70) rd_w(n + RA);
      if constexpr (n == 68) {
        const lptr qb = lds + L_Q + (wid & 1) * 4096 + ((wid >> 1) & 1) * 2048 + lane * 16, qa = lds + L_Q + (wid & 1) * 4096 + (((wid >> 1) & 1) ^ 1) * 2048 + lane * 16;
        kf0 = ld128(lds + lane * 16); QA0 = ld128(qa); QB0 = ld128(qb); kf1 = ld128(lds + 1024 + lane * 16); QA1 = ld128(qa + 1024); QB1 = ld128(qb + 1024);
        cfix = ld128(lds + L_CS + wn * 1024 + lane * 16);
      }
      if constexpr (n == 69) { sa = MFMA(kf1, QA1, sa); sb = MFMA(kf1, QB1, sb); PIN(sa); PIN(sb); }
      if constexpr (n == 2) rd_x(4, 8);
      if constexpr (u == 6 && n < 24) rd_x(n + 2, n + 6);
      if constexpr (u == 0 && n > 0 && n < 24) rd_x(n + 4, n + 8);
      if constexpr (!early) colsum(n, 0);
      SBAR();
      if constexpr (!d1f) { D1 = MFMA(cat(wf[n % NWF][2], wf[n % NWF][3]), xf[k1], D1); PIN(D1); }
      SBAR();
      if constexpr (!early) colsum(n, 1);
      if constexpr (n == 0) dma_w(IC<2>{}); if constexpr (n == 1) dma_x(IC<3>{}); if constexpr (n == 2) dma_w(IC<3>{}); if constexpr (n == 4) dma_w(IC<4>{});
      if constexpr (n == 6) dma_x(IC<4>{}); if constexpr (n == 7) dma_x(IC<5>{}); if constexpr (n == 8) dma_w(IC<5>{}); if constexpr (n == 10) dma_w(IC<6>{});
      if constexpr (n == 14) dma_x(IC<6>{}); if constexpr (n == 15) dma_w(IC<7>{}); if constexpr (n == 18) dma_w(IC<8>{}); if constexpr (n == 20) dma_w(IC<9>{});
      if constexpr (n == 23) dma_w(IC<10>{}); if constexpr (n == 26) dma_w(IC<11>{}); if constexpr (n == 29) dma_w(IC<12>{}); if constexpr (n == 32) dma_w(IC<13>{});
      if constexpr (n == 48) touch(SRD(b < 196 ? wd : wo, b < 196 ? 1792 * 896 : 448 * 896), lane < 8 ? (((b < 196 ? b : b - 196) * 8 + wid) * 8 + lane) * 128 : OOB, lds0 + L_PF + wid * 256);
      if constexpr (n == 17) asm volatile("buffer_load_dwordx2 %0, %1, %2, 0 offen sc1" : "+v"(sg) : "v"(sgo), "s"(wsr) : "memory");
      if constexpr (n == 16) asm volatile("global_load_dwordx4 %0, %2, %3\n\tglobal_load_dwordx4 %1, %2, %3 offset:32" : "+v"(tb0), "+v"(tb1) : "v"(h * 16), "s"(&qkv_tab.v[bq ? 4 * ng + 2 * wn : 0]) : "memory");
      if constexpr (n >= 39 && n < 53 && (n & 1)) dma_w(IC<14 + (n - 39) / 2>{});
      if constexpr (n == 27) cs_out();
      if constexpr (n == 23) {
        PIN(sg); PIN(tb0); PIN(tb1);
        _Pragma("nounroll") for (unsigned sp = 0; !ALL(sg[1] == nonce) && sp < SPIN_CAP; ++sp)
          asm volatile("buffer_load_dwordx2 %0, %1, %2, 0 offen sc1\n\ts_waitcnt vmcnt(0)" : "+v"(sg) : "v"(sgo), "s"(wsr) : "memory");
        sig = U2F(sg[0] & 0xffff0000u); rstd = __builtin_amdgcn_rcpf(sig); mm = __builtin_amdgcn_perm(sg[0], sg[0], 0x01000100u); kg = -1.4426950408889634f * rstd;
        PIN(sig); PIN(rstd); PIN(kg); PIN(mm);
      }
      if constexpr (n == 30) fix2(A0, A1);
      if constexpr (n == 31) epi1(A0, 0);
      if constexpr (n == 34) epi1(A1, 1);
      if constexpr (n == FQP) { fq_off(); asm volatile("buffer_load_dword %0, %1, %2, 0 offen sc1" : "+v"(fl0) : "v"(fqo), "s"(wsr) : "memory"); }
      if constexpr (n == 55) fix2(B0, B1);
      if constexpr (n == 57) { hp = glu8(B0); PIN(hp); }
      if constexpr (n == 64) { hq = glu8(B1); PIN(hq); }
      if constexpr (n == 69) { st128(hp, wsr, lane * 16, hs2, SC1); st128(hq, wsr, lane * 16 + 1024, hs2, SC1); }
      if constexpr (n == FQC) {
        asm volatile("s_waitcnt vmcnt(0)" : "+v"(fl0) :: "memory");
        _Pragma("nounroll") for (unsigned sp = 0; !ALL(fl0 == nonce) && sp < SPIN_CAP; ++sp)
          asm volatile("buffer_load_dword %0, %1, %2, 0 offen sc1\n\ts_waitcnt vmcnt(0)" : "+v"(fl0) : "v"(fqo), "s"(wsr) : "memory");
        kvv = (unsigned)(WS_QKV + head * QH + (wid >> 1) * 16 * QT + 2048 + (wid & 1) * 2048 + lane * 16);
        dma<0, L_Q, true>(wsr, WS_QKV + head * QH + (qg * 4 + (wid >> 1)) * QT + (wid & 1) * 1024 + lane * 16, lds0 + wid * 1024); dma_kv(IC<0>{}, kvv); }
      if constexpr (n > FQC && n < FQC + 8) dma_kv(IC<n - FQC>{}, kvv);
      if constexpr (n == 67) {
        const int vc = 2 * (grp ^ wn) + (p >> 1);
        kb = lds + (wid >> 1) * 4096 + lane * 16;
        vb0 = lds + (wid >> 1) * 4096 + 2048 + vc * 512 + ((((4 * h) ^ (4 * vc)) + q) << 4) + (p & 1) * 8;
        vb1 = lds + (wid >> 1) * 4096 + 2048 + vc * 512 + ((((4 * h + 8) ^ (4 * vc)) + q) << 4) + (p & 1) * 8;
        PIN(kb); PIN(vb0); PIN(vb1);
      }
      if constexpr (n == 68) { sa = MFMA(kf0, QA0, Z); sb = MFMA(kf0, QB0, Z); PIN(sa); PIN(sb); ka0 = ld128(kb); ka1 = ld128(kb + 1024); va0 = cat(trd(vb0), trd(vb1)); }
    });
    SBAR();
    C0 = MFMA(cfix, ((u32x4){mm, mm, mm, mm}), C0);
    float ma = sa[0], mb = sb[0];
    UNROLL for (int i = 1; i < 16; ++i) { ma = __builtin_fmaxf(ma, sa[i]); mb = __builtin_fmaxf(mb, sb[i]); }
    const unsigned pa = pk(1.f - max32(ma), 0.f), pb = pk(1.f - max32(mb), 0.f);
    const bool p0 = (head & 1) && h == 0, p1 = !(head & 1) && h == 1;
    QA0[0] = p0 ? pa : QA0[0]; QA1[2] = p1 ? pa : QA1[2]; QB0[0] = p0 ? pb : QB0[0]; QB1[2] = p1 ? pb : QB1[2];
    s0 = MFMA(ka0, QA0, Z); s0 = MFMA(ka1, QA1, s0);
    PIN(s0); PIN(QB0); PIN(QB1);
    SBAR();
    st128(glu8(C0), wsr, ng < 14 ? lane * 16 : OOB, WS_H + (mt * 112 + 64 + ng * 2 + wn) * 1024, SC1);
    SBAR();
  }
  return phase23(x, wo, wd, ws, out, lds, nonce, QA0, QA1, QB0, QB1, ka0, ka1, va0, s0, kb, vb0, vb1, kvv, wid);
}

extern "C" void kernel_launch(void* const* d_in, const int* in_sizes, int n_in, void* d_out, int out_size, void* d_ws, size_t ws_size,
                              hipStream_t stream) {
  (void)hipFuncSetAttribute((const void*)fused_kernel, hipFuncAttributeMaxDynamicSharedMemorySize, LDS_BYTES);
  fused_kernel<<<256, 512, LDS_BYTES, stream>>>((const char*)d_in[0], (const char*)d_in[1], (const char*)d_in[2], (const char*)d_in[4], (const char*)d_in[5],
                                                (const char*)d_in[6], (char*)d_ws, (char*)d_out);
}
```

```cpp
#include <hip/hip_runtime.h>
#include <hip/hip_bf16.h>
#include <stdint.h>

typedef short bf16x8 __attribute__((ext_vector_type(8)));
typedef short s16x4 __attribute__((ext_vector_type(4)));
typedef float f32x16 __attribute__((ext_vector_type(16)));
typedef float f32x4 __attribute__((ext_vector_type(4)));
typedef float f32x2 __attribute__((ext_vector_type(2)));
typedef unsigned u32x4 __attribute__((ext_vector_type(4)));
typedef unsigned u32x2 __attribute__((ext_vector_type(2)));
typedef __bf16 bf2 __attribute__((ext_vector_type(2)));
typedef __amdgpu_buffer_rsrc_t srd_t;
#define LAS __attribute__((address_space(3)))
#define GAS __attribute__((address_space(1)))
typedef LAS char* lptr;
#define DI __device__ __forceinline__
#define AI __attribute__((always_inline))
#define MFMA(a, b, c) __builtin_amdgcn_mfma_f32_32x32x16_bf16(__builtin_bit_cast(bf16x8, (a)), __builtin_bit_cast(bf16x8, (b)), (c), 0, 0, 0)
#define PIN(x) asm volatile("" : "+v"(x))
#define SBAR() __builtin_amdgcn_sched_barrier(0)
#define RLX_AGENT __ATOMIC_RELAXED, __HIP_MEMORY_SCOPE_AGENT
#define EX(v) __builtin_amdgcn_exp2f(v)
#define F2U(x) __builtin_bit_cast(unsigned, (float)(x))
#define U2F(x) __builtin_bit_cast(float, (unsigned)(x))
#define ANY(p) (__builtin_amdgcn_ballot_w64(p) != 0)
#define ALL(p) (__builtin_amdgcn_ballot_w64(!(p)) == 0)
#define UNROLL _Pragma("unroll")
#define SRD(p, n) __builtin_amdgcn_make_buffer_rsrc((void*)(p), 0, (n), 0x00020000)

constexpr int WS_E = 0;
constexpr int WS_FQ = 4096;
constexpr int WS_FH = WS_FQ + 256 * 128;
constexpr int WS_FO = WS_FH + 256 * 128;
constexpr int WS_ST = WS_FO + 256 * 128;
constexpr int WS_QKV = 1 << 20;
constexpr int QT = 6144, QH = 64 * QT;
constexpr int WS_O = WS_QKV + 16 * QH;
constexpr int WS_H = WS_O + (2 << 20);
constexpr int WS_END = WS_H + 64 * 112 * 1024;
constexpr int OOB = 1 << 30;
constexpr int SC1 = 16;
constexpr unsigned SPIN_CAP = 1u << 20;

#define PADPT 4

#define DBG_TS
#define RTIME() ({ unsigned long long t_; asm volatile("s_memrealtime %0\n\ts_waitcnt lgkmcnt(0)" : "=s"(t_) :: "memory"); (unsigned)t_; })
#define TS(i) do { if ((i) == PADPT) tsv = RTIME(); } while (0)
#define TSR(i) do { } while (0)
#define TS_OUT() do { const unsigned tend = tsv + 6000; while ((int)(RTIME() - tend) < 0) __builtin_amdgcn_s_sleep(1); } while (0)

template <int I> struct IC { static constexpr int v = I; };
template <int B, int E, class F> DI void sfor(F&& f) { if constexpr (B < E) { f(IC<B>{}); sfor<B + 1, E>(f); } }
template <int N> DI void wait_bar() { asm volatile("s_waitcnt vmcnt(%0) lgkmcnt(0)\n\ts_barrier" :: "n"(N) : "memory"); }
template <int N> DI void vwait_bar() { asm volatile("s_waitcnt vmcnt(%0)\n\ts_barrier" :: "n"(N) : "memory"); }
DI void lbar() { asm volatile("s_waitcnt lgkmcnt(0)\n\ts_barrier" ::: "memory"); }
DI unsigned pk(float a, float b) { f32x2 v = {a, b}; return __builtin_bit_cast(unsigned, __builtin_convertvector(v, bf2)); }
DI float rbf(float a) { return U2F(pk(a, a) & 0xffff0000u); }
DI float dot2(unsigned a, unsigned b, float c) { return __builtin_amdgcn_fdot2_f32_bf16(__builtin_bit_cast(bf2, a), __builtin_bit_cast(bf2, b), c, false); }
DI u32x2 trd(lptr p) { return __builtin_bit_cast(u32x2, __builtin_amdgcn_ds_read_tr16_b64_v4i16((LAS s16x4*)p)); }
DI u32x4 cat(u32x2 a, u32x2 b) { return (u32x4){a[0], a[1], b[0], b[1]}; }
DI u32x4 ld128(lptr p) { return *(LAS u32x4*)p; }
DI unsigned bfi(unsigned m, unsigned a, unsigned b) { return (m & a) | (~m & b); }
template <int G, int L, bool DEV = false> DI void dma(srd_t srd, unsigned voff, unsigned lbase) {
  unsigned t;
  if constexpr (DEV)
    asm volatile("s_add_u32 m0, %3, %5\n\ts_mov_b32 %0, %4\n\tbuffer_load_dwordx4 %1, %2, %0 offen sc1 lds" : "=&s"(t) : "v"(voff), "s"(srd), "s"(lbase), "n"(G), "n"(L) : "memory", "scc");
  else
    asm volatile("s_add_u32 m0, %3, %5\n\ts_mov_b32 %0, %4\n\tbuffer_load_dwordx4 %1, %2, %0 offen lds" : "=&s"(t) : "v"(voff), "s"(srd), "s"(lbase), "n"(G), "n"(L) : "memory", "scc");
}
template <int L> DI void dma2(srd_t srd, unsigned voff0, unsigned voff1, unsigned lbase, unsigned ga, unsigned gb) {
  unsigned t;
  asm volatile("s_add_u32 m0, %4, %5\n\ts_mul_i32 %0, %6, %7\n\tbuffer_load_dwordx4 %1, %3, %0 offen lds\n\ts_add_u32 m0, m0, 1024\n\ts_nop 0\n\tbuffer_load_dwordx4 %2, %3, %0 offen lds"
               : "=&s"(t) : "v"(voff0), "v"(voff1), "s"(srd), "s"(lbase), "n"(L), "s"(ga), "s"(gb) : "memory", "scc");
}
template <int L> DI void dma1(srd_t srd, unsigned voff, unsigned lbase, unsigned ga, unsigned gb) {
  unsigned t;
  asm volatile("s_add_u32 m0, %3, %4\n\ts_mul_i32 %0, %5, %6\n\tbuffer_load_dwordx4 %1, %2, %0 offen lds" : "=&s"(t) : "v"(voff), "s"(srd), "s"(lbase), "n"(L), "s"(ga), "s"(gb) : "memory", "scc");
}
DI void touch(srd_t srd, unsigned voff, unsigned ldst) { asm volatile("s_mov_b32 m0, %2\n\ts_nop 0\n\tbuffer_load_dword %0, %1, 0 offen lds" :: "v"(voff), "s"(srd), "s"(ldst) : "memory"); }
DI void st128(u32x4 v, srd_t srd, int voff, int soff, int) { __builtin_amdgcn_raw_buffer_store_b128(v, srd, voff, soff, SC1); SBAR(); asm volatile("s_nop 1"); SBAR(); }
DI float swap32(float v, int h) {
  auto r = __builtin_amdgcn_permlane32_swap(F2U(v), F2U(v), false, false);
  return U2F(h ? r[0] : r[1]);
}
DI float max32(float v) {
  auto r = __builtin_amdgcn_permlane32_swap(F2U(v), F2U(v), false, false);
  return __builtin_fmaxf(U2F(r[0]), U2F(r[1]));
}
DI u32x4 widen(u32x2 a, u32x2 b) {
  auto r0 = __builtin_amdgcn_permlane32_swap(a[0], b[0], false, false); auto r1 = __builtin_amdgcn_permlane32_swap(a[1], b[1], false, false);
  return (u32x4){r0[0], r1[0], r0[1], r1[1]};
}
DI float wave_sum(float v) {
  v += U2F(__builtin_amdgcn_update_dpp(0u, F2U(v), 0x111, 0xf, 0xf, true));
  v += U2F(__builtin_amdgcn_update_dpp(0u, F2U(v), 0x112, 0xf, 0xf, true));
  v += U2F(__builtin_amdgcn_update_dpp(0u, F2U(v), 0x114, 0xf, 0xf, true));
  v += U2F(__builtin_amdgcn_update_dpp(0u, F2U(v), 0x118, 0xf, 0xf, true));
  v += U2F(__builtin_amdgcn_update_dpp(0u, F2U(v), 0x142, 0xa, 0xf, true));
  v += U2F(__builtin_amdgcn_update_dpp(0u, F2U(v), 0x143, 0xc, 0xf, true));
  return v;
}
DI unsigned flag_ld(const char* ws, int off) { return __hip_atomic_load((GAS unsigned*)(ws + off), RLX_AGENT); }
DI void flag_st(char* ws, int off, unsigned v) {
  if (__builtin_amdgcn_mbcnt_hi(~0u, __builtin_amdgcn_mbcnt_lo(~0u, 0u)) == 0) __hip_atomic_store((GAS unsigned*)(ws + off), v, RLX_AGENT); }

constexpr int RA = 2, NWF = RA + 1;
constexpr int PART = 14336;
constexpr int L_W = 65536;
constexpr int L_CS = 9 * 16384;
constexpr int L_PF = L_CS + 2048;
constexpr int L_Q = 65536;
constexpr int wpos(int m) { return m < 9 ? L_W + (m % 5) * 16384 : m == 9 ? 49152 : m == 10 ? L_W : m < 14 ? (m - 11) * 16384 : L_W + 16384 + (m - 14) * 8192; }
#define FQP 52
#define FQC 59
#define MAP 0
#define NG_OF(xcd, idx) ((xcd & 3) * 4 + (idx & 3))
#define MG_OF(xcd, idx) ((xcd >> 2) * 8 + (idx >> 2))
#define ROTX 0
constexpr int L_WD = 65536;
constexpr int LDS_BYTES = 163840;
constexpr unsigned ONES2 = 0x3f803f80u, PAD0 = 0x00003f80u;
static_assert(L_PF + 2048 <= LDS_BYTES, "LDS map");

constexpr int DN0 = 7;
constexpr int kv_at(int s, int p) { return p == 0 && s > 0 && s + 3 < 16 ? (s + 3) * 2 + 1 : p == 7 && s + 4 < 16 ? (s + 4) * 2 : -1; }
constexpr int wd_at(int s, int p) {
  if (s >= 1 && s <= 3) return p == 1 ? s * 4 - 4 : p == 3 ? s * 4 - 3 : p == 4 ? s * 4 - 2 : p == 5 ? s * 4 - 1 : -1;
  for (int g = 0; g < 2; ++g) { if (s == 10 + 4 * g && p == 7) return 12 + 4 * g; if (s == 11 + 4 * g) return p == 0 ? 13 + 4 * g : p == 2 ? 14 + 4 * g : p == 5 ? 15 + 4 * g : -1; }
  return -1;
}
constexpr int h_at(int s, int p) { const int z = (s - 5) * 3 + (p == 1 ? 1 : p == 3 ? 2 : 3); if (s == 15) return p == 1 ? 24 : p == 3 ? 25 : p == 4 ? 26 : p == 7 ? 27 : -1;
  return s == 4 && p == 7 ? 0 : s >= 5 && (p == 1 || p == 3 || p == 4) && z < 24 ? z : -1; }
constexpr bool pf_at(int s, int p) { return s == 4 && p == 1; }
constexpr int n_at(int s, int p) { return (kv_at(s, p) >= 0) + (wd_at(s, p) >= 0) + (h_at(s, p) >= 0) + pf_at(s, p); }
constexpr int cnt_after(int s0, int p0, int s) { int n = 0; for (int z = s0 * 8 + p0 + 1; z < s * 8 + 6; ++z) n += n_at(z / 8, z % 8); return n; }
constexpr int dslot(int sub) { return L_WD + (sub % 3) * 32768; }

struct QkvTab { unsigned v[44][2][4]; };
constexpr QkvTab make_tab() {
  QkvTab t = {};
  for (int tile = 0; tile < 44; ++tile) for (int h = 0; h < 2; ++h) {
    const int col0 = tile * 32, sec = col0 / 448; unsigned* e = t.v[tile][h];
    e[0] = e[1] = e[2] = OOB; e[3] = 0;
    if (tile < 42) for (int pp = 0; pp < 2; ++pp) {
      const int cu = (col0 - sec * 448) / 8 + 2 * pp + h, j = cu / 7, w = cu % 7, hd = 2 * j + (w > 3), ch = w > 3 ? w - 3 : w;
      e[pp] = WS_QKV + sec * 2048 + hd * QH + ch * 512 + (sec == 2 ? ch * 64 : 0);
      if (w == 3) { e[2] = WS_QKV + sec * 2048 + (hd + 1) * QH; e[3] |= 1u << pp; }
    }
  }
  return t;
}
__device__ const QkvTab qkv_tab = make_tab();

#define TSARG , tsv
#define TSPAR , unsigned tsv
#define LANE_IDS() int lane; asm volatile("v_mbcnt_lo_u32_b32 %0, -1, 0\n\tv_mbcnt_hi_u32_b32 %0, -1, %0" : "=v"(lane));        \
  const int r = lane & 31, h = lane >> 5, q = (lane & 15) >> 2, p = lane & 3, grp = (lane >> 4) & 1, rr = lane >> 3, cc = lane & 7
DI void phase23(const char* x, const char* wo, const char* wd, char* ws, char* out, lptr lds, unsigned nonce, u32x4 QA0, u32x4 QA1, u32x4 QB0, u32x4 QB1,
                u32x4 k0, u32x4 k1, u32x4 v0, f32x16 sa, lptr kb, lptr vb0, lptr vb1, unsigned kvv, int wid TSPAR) {
  const unsigned lds0 = (unsigned)(uintptr_t)lds;
  const int b = __builtin_amdgcn_workgroup_id_x(), xcd = b & 7, idx = b >> 3;
  const int ng = NG_OF(xcd, idx), mg = MG_OF(xcd, idx);
  const srd_t wsr = SRD(ws, WS_END);
  const f32x16 Z = {};
  const int head = 2 * xcd + (idx & 1), qg = idx >> 1, odd = head & 1;
  const int idx3 = idx < 28 ? idx : idx - 28, mq3 = (idx3 * 37) >> 8, mg3 = xcd * 4 + mq3, ng3 = idx3 - mq3 * 7;
  LANE_IDS();
  const unsigned lw2k = lds0 + wid * 2048;
  auto dma_kv = [&](auto zc) AI { constexpr int z = decltype(zc)::v; dma<(z >> 1) * QT + (z & 1) * 1024, ((z >> 1) & 3) * 16384 + (z & 1) * 1024, true>(wsr, kvv, lw2k); };
  const int w3m = wid & 1, w3k = wid >> 1;
  u32x4 hf[28];
  const int ft = w3k & 1, fs = w3k >> 1;
  unsigned vo_d = 0; const unsigned ld_d = lds0 + wid * 4096;
  auto set_vo_d = [&]() AI { vo_d = (unsigned)(((idx < 28 ? w3k * 448 + w3m * 32 : OOB / 896) + rr) * 896 + ((cc ^ (grp << 2)) << 4) + ng3 * 128); PIN(vo_d); };
  const srd_t sD = SRD(wd, 1792 * 896);
  auto dma_d = [&](auto zc) AI { constexpr int z = decltype(zc)::v; dma<((z >> 2) * 64 + (z & 3) * 8) * 896, dslot(z >> 2) + (z & 3) * 1024>(sD, vo_d, ld_d); };
  int db0 = 0, db1 = 0, dbo = L_WD;
  auto set_db = [&]() AI { db0 = L_WD + w3k * 8192 + (4 * h + q) * 128 + p * 8 + ((grp ^ ((q >> 1) << 1)) << 5); db1 = db0 ^ 64; PIN(db0); PIN(db1); };
  auto rebase = [&](int o) AI { db0 += o - dbo; db1 += o - dbo; dbo = o; PIN(db0); PIN(db1); };
  f32x16 F0 = Z, F1 = Z;
  u32x4 dw0, dw1;
  auto rd_d0 = [&](int d) AI { const int o = dslot(d >> 2) + (d & 3) * 2048 - dbo; dw0 = cat(trd(lds + db0 + o), trd(lds + db0 + o + 1024)); };
  auto rd_d1 = [&](int d) AI { const int o = dslot(d >> 2) + (d & 3) * 2048 - dbo; dw1 = cat(trd(lds + db1 + o), trd(lds + db1 + o + 1024)); };
  {
    const int wq = wid & 1, kq = wid >> 1;
    u32x4 v1, pa, pb;
    float e0, e1, e2, e3;
    f32x16 OA = Z, OB = Z, sb;
    unsigned junk, fh = ~nonce; const int fho = WS_FH + ((mg3 >> 1) * 16 + (lane & 15)) * 128;
    const int hld = WS_H + ((mg3 * 2 + w3m) * 112 + w3k * 28) * 1024, hlv = idx < 28 ? lane * 16 : OOB;
    auto send = [&](const f32x16& a, int hh, int j) AI {
      const lptr d = kb + (((wid ^ (2 * j)) * 6144 + (j - 1) * 2048) - kq * 4096);
      *(LAS f32x4*)d = (f32x4){a[8 * hh], a[8 * hh + 1], a[8 * hh + 2], a[8 * hh + 3]}; *(LAS f32x4*)(d + 1024) = (f32x4){a[8 * hh + 4], a[8 * hh + 5], a[8 * hh + 6], a[8 * hh + 7]}; };
    TS(10); TS(19);
#define EX2(S, i, a, c) do { a = EX(S[i]); c = EX(S[(i) + 1]); PIN(a); PIN(c); } while (0)
#define STEP(FIRST, SC, SN, QN0, QN1, OP, OC, L0, L1, L2, L3, X0, X1) do {                                                       \
      SBAR(); SN = MFMA(k0, QN0, Z); SBAR(); L0;                                                                          \
      if (!(FIRST)) { pb[2] = pk(e0, e1); pb[3] = pk(e2, e3); PIN(pb); } SBAR(); EX2(SC, 0, e0, e1); SBAR(); X0; SBAR(); EX2(SC, 2, e2, e3); \
      SBAR(); if (!(FIRST)) OP = MFMA(v1, pb, OP); SBAR(); L1;                                                                          \
      pa[0] = pk(e0, e1); pa[1] = pk(e2, e3); PIN(pa); SBAR(); EX2(SC, 4, e0, e1); EX2(SC, 6, e2, e3);                    \
      SBAR(); SN = MFMA(k1, QN1, SN); SBAR(); L2;                                                                         \
      pa[2] = pk(e0, e1); pa[3] = pk(e2, e3); PIN(pa); SBAR(); EX2(SC, 8, e0, e1); SBAR(); X1; SBAR(); EX2(SC, 10, e2, e3); \
      SBAR(); OC = MFMA(v0, pa, OC); SBAR(); L3;                                                                          \
      pb[0] = pk(e0, e1); pb[1] = pk(e2, e3); PIN(pb); SBAR(); EX2(SC, 12, e0, e1); EX2(SC, 14, e2, e3); SBAR(); } while (0)
    sfor<0, 16>([&](auto sc_) AI {
      constexpr int s = decltype(sc_)::v, so = (s & 3) * 16384, sn = ((s + 1) & 3) * 16384;
      auto side = [&](auto pc) AI {
        constexpr int P = decltype(pc)::v, kv = kv_at(s, P), wz = wd_at(s, P), hz = h_at(s, P);
        if constexpr (s == 0 && P == 3) set_vo_d();
        if constexpr (s == 0 && P == 5) set_db();
        if constexpr (kv >= 0) dma_kv(IC<kv>{});
        if constexpr (wz >= 0) dma_d(IC<wz>{});
        if constexpr (pf_at(s, P)) {
          const int row = wid < 2 ? mq3 * 448 + 320 + wid * 64 + lane : mq3 * 112 + (wid - 2) * 56 + (lane < 56 ? lane : 55);
          asm volatile("buffer_load_dword %0, %1, %2, 0 offen" : "=&v"(junk) : "v"(wid < 4 && idx < 28 ? row * 896 + ng3 * 128 : OOB), "s"(SRD(wid < 2 ? wd : wo, wid < 2 ? 1792 * 896 : 448 * 896)) : "memory");
        }
        if constexpr (hz >= 0) hf[hz] = __builtin_amdgcn_raw_buffer_load_b128(wsr, hlv + (hz & 3) * 1024, hld + (hz >> 2) * 4096, SC1);
      };
      auto mid = [&]() AI {
        if (s == 4 && wid == 0) {
          asm volatile("s_waitcnt vmcnt(%1)" : "+v"(fh) : "n"(cnt_after(3, 6, 4)) : "memory");
          _Pragma("nounroll") for (unsigned sp = 0; !ALL(fh == nonce) && sp < SPIN_CAP; ++sp) { fh = flag_ld(ws, fho); PIN(fh); }
        }
        if constexpr (s == 0) vwait_bar<5 + cnt_after(0, -1, 0)>();
        else if constexpr (s == 1) vwait_bar<cnt_after(0, -1, 1)>();
        else if constexpr (s == 14) vwait_bar<0>(); else if constexpr (s < 14) vwait_bar<cnt_after(s - 1, 0, s)>();
        if (s == 1 && wid == 0) flag_st(ws, WS_FH + (mg * 16 + ng) * 128, nonce);
        if (s == 3 && wid == 0) asm volatile("buffer_load_dword %0, %1, %2, 0 offen sc1" : "+v"(fh) : "v"(fho), "s"(wsr) : "memory");
        if constexpr (s == 6) asm volatile("" :: "v"(junk));
        if constexpr (s == 13) rebase(L_WD + 65536);
        if constexpr (s == 14) { UNROLL for (int k = 23; k >= 8; --k) PIN(hf[k]); }
        if (s == 1) TS(20); if (s == 3) TS(21); if (s == 5) TS(22); if (s == 7) TS(23); if (s == 9) TS(24); if (s == 11) TS(25); if (s == 13) TS(26);
        __builtin_amdgcn_s_setprio(3);
      };
      auto dn0 = [&](int d) AI { if (d >= 0) { F0 = MFMA(dw0, hf[d], F0); PIN(F0); } if (d >= -1) rd_d0(d + 1); };
      auto dn1 = [&](int d) AI { if (d >= 0) { F1 = MFMA(dw1, hf[d], F1); PIN(F1); } if (d >= -1) rd_d1(d + 1); };
      STEP(s == 0, sa, sb, QB0, QB1, OB, OA,
           k0 = ld128(kb + sn); side(IC<0>{}); __builtin_amdgcn_s_setprio(2),
           v1 = cat(trd(vb0 + so + 256), trd(vb1 + so + 256)); side(IC<1>{}),
           k1 = ld128(kb + sn + 1024); side(IC<2>{}); __builtin_amdgcn_s_setprio(1),
           side(IC<3>{}), dn0(s - DN0), if (s == 15) dn1(8));
      STEP(false, sb, sa, QA0, QA1, OA, OB,
           __builtin_amdgcn_s_setprio(0); side(IC<4>{}),
           side(IC<5>{}),
           mid(); if (s == 15) send(OA, 0, 1),
           v0 = cat(trd(vb0 + sn), trd(vb1 + sn)); side(IC<7>{}); if (s == 15) send(OA, 1, 3), if (s < 15) dn1(s - DN0); else dn0(9), if (s == 15) dn1(9));
    });
    pb[2] = pk(e0, e1); pb[3] = pk(e2, e3);
    OB = MFMA(v1, pb, OB);
    __builtin_amdgcn_s_setprio(0);
    TS(11);
    send(OB, 1, 2);
    *(LAS float*)(kb + (49152 + wq * 1024)) = odd ? OB[0] : OB[4];
    {
    LANE_IDS();
    const bool padl = h != odd;
    const int qt = qg * 4 + wq * 2 + ft, lw = (wid & 3) | (odd ? 0 : 4);
    const lptr pp = lds + wid * 6144 + lane * 16, lp = lds + lw * 6144 + (odd ? 0 : 1536) + r * 16;
    const lptr lq = lds + 49152 + (lw >> 1) * 4096 + wq * 1024 + (odd ? 0 : 512) + r * 16;
    f32x4 o0 = {OB[0], OB[1], OB[2], OB[3]}, o1 = {OB[4], OB[5], OB[6], OB[7]};
    lbar();
    const float l0 = *(LAS float*)lq, l1 = *(LAS float*)lp, l2 = *(LAS float*)(lp + 2048), l3 = *(LAS float*)(lp + 4096);
    f32x4 t[6];
    UNROLL for (int k = 0; k < 6; ++k) t[k] = *(LAS f32x4*)(pp + k * 1024);
    SBAR();
    float l = (l0 + l1) + (l2 + l3);
    UNROLL for (int k = 0; k < 3; ++k) { o0 += t[2 * k]; o1 += t[2 * k + 1]; }
    if (__builtin_expect(ANY(!(l <= 1.8446744e19f && l >= 5.4210109e-20f)), 0)) {
      const int qo = WS_QKV + head * QH + qt * QT + lane * 16;
      u32x4 q0 = __builtin_amdgcn_raw_buffer_load_b128(wsr, qo, 0, SC1), q1 = __builtin_amdgcn_raw_buffer_load_b128(wsr, qo + 1024, 0, SC1);
      f32x16 O = Z; float m = -__builtin_inff();
      _Pragma("nounroll") for (int t = 0; t < 64; ++t) {
        const int ko = WS_QKV + head * QH + t * QT + 2048;
        f32x16 s = MFMA(__builtin_amdgcn_raw_buffer_load_b128(wsr, ko + lane * 16, 0, SC1), q0, Z);
        s = MFMA(__builtin_amdgcn_raw_buffer_load_b128(wsr, ko + 1024 + lane * 16, 0, SC1), q1, s);
        float mn = m;
        UNROLL for (int i = 0; i < 16; ++i) mn = __builtin_fmaxf(mn, s[i]);
        mn = max32(mn);
        const float f = EX(m - mn); m = mn;
        UNROLL for (int i = 0; i < 16; ++i) O[i] *= f;
        UNROLL for (int st = 0; st < 2; ++st) {
          unsigned vv[8];
          UNROLL for (int j = 0; j < 8; ++j) vv[j] = __builtin_amdgcn_raw_buffer_load_b16(wsr, ko + 2048 + (r >> 3) * 512 + (((16 * st + 8 * (j >> 2) + 4 * h + (j & 3)) ^ (4 * (r >> 3))) << 4) + (r & 7) * 2, 0, SC1) & 0xffffu;
          O = MFMA(((u32x4){vv[0] | vv[1] << 16, vv[2] | vv[3] << 16, vv[4] | vv[5] << 16, vv[6] | vv[7] << 16}),
                   ((u32x4){pk(EX(s[8 * st] - m), EX(s[8 * st + 1] - m)), pk(EX(s[8 * st + 2] - m), EX(s[8 * st + 3] - m)),
                            pk(EX(s[8 * st + 4] - m), EX(s[8 * st + 5] - m)), pk(EX(s[8 * st + 6] - m), EX(s[8 * st + 7] - m))}), O);
        }
      }
      const float lc = odd ? O[0] : O[12], lx = swap32(lc, h); l = padl ? lc : lx;
      o0 = fs ? (f32x4){O[8], O[9], O[10], O[11]} : (f32x4){O[0], O[1], O[2], O[3]};
      o1 = fs ? (f32x4){O[12], O[13], O[14], O[15]} : (f32x4){O[4], O[5], O[6], O[7]};
    }
    const float ri = __builtin_amdgcn_rcpf(l);
    const float r0 = padl && odd && fs == 0 ? 0.f : ri, r1 = padl && !odd && fs == 1 ? 0.f : ri;
    st128((u32x4){pk(o0[0] * r0, o0[1] * r0), pk(o0[2] * r0, o0[3] * r0), pk(o1[0] * r1, o1[1] * r1), pk(o1[2] * r1, o1[3] * r1)},
                                           wsr, lane * 16, WS_O + (qt * 16 + head) * 2048 + fs * 1024, SC1);
    }
  }
  TS(12);
  if (idx >= 28) { wait_bar<0>();
    if (wid == 0) {
      flag_st(ws, WS_FO + (qg * 16 + head) * 128, nonce);
      if (b == 255) {
        LANE_IDS();
        _Pragma("nounroll") for (unsigned sp = 0; sp < SPIN_CAP; ++sp) {
          unsigned d = 0;
          UNROLL for (int i = 0; i < 4; ++i) d |= flag_ld(ws, WS_FQ + lane * 512 + i * 128) ^ nonce;
          if (ALL(d == 0)) break;
        }
        flag_st(ws, WS_E, nonce);
      }
    }
    return;
  }
  {
    lbar();
    const srd_t sO = SRD(wo, 448 * 896); const unsigned wov = vo_d + (wid * 56 - w3k * 448 - w3m * 32) * 896, wol = lds0 + wid * 7168;
    auto dma_o = [&](auto ic) AI { constexpr int i = decltype(ic)::v; dma<i * 8 * 896, i * 1024>(sO, wov, wol); };
    sfor<0, 4>(dma_o);
    u32x4 ew0, ew1;
    auto down2 = [&](int d, bool more) AI {
      const int o = dslot((d + 1) >> 2) + ((d + 1) & 3) * 2048 - dbo;
      ew0 = cat(trd(lds + db0 + o), trd(lds + db0 + o + 1024)); ew1 = cat(trd(lds + db1 + o), trd(lds + db1 + o + 1024));
      if (d == 10 || d == 22) rebase(L_WD);
      SBAR(); F0 = MFMA(dw0, hf[d], F0); F1 = MFMA(dw1, hf[d], F1); PIN(F0); PIN(F1); SBAR();
      if (more) { rd_d0(d + 2); rd_d1(d + 2); }
      SBAR(); F0 = MFMA(ew0, hf[d + 1], F0); F1 = MFMA(ew1, hf[d + 1], F1); PIN(F0); PIN(F1); SBAR();
    };
    static_assert(DN0 == 7, "units 0..9 ran beside the attention");
    down2(10, true); sfor<4, 7>(dma_o);
    vwait_bar<7>();
    if (wid == 0) flag_st(ws, WS_FO + (qg * 16 + head) * 128, nonce);
    TS(13);
    sfor<20, 22>(dma_d); down2(12, true); sfor<22, 24>(dma_d); down2(14, true);
    vwait_bar<4>();
    LANE_IDS();
    const int mt = mg3 * 2 + w3m, orot = ng3 & 1;
    unsigned fo = ~nonce; const int foo = WS_FO + ((mg3 >> 1) * 16 + (lane & 15)) * 128;
    sfor<24, 26>(dma_d); down2(16, true); sfor<26, 28>(dma_d);
    if (wid == 0) asm volatile("buffer_load_dword %0, %1, %2, 0 offen sc1" : "+v"(fo) : "v"(foo), "s"(wsr) : "memory");
    down2(18, false);
    u32x4 wo0[8], wo1[8];
    UNROLL for (int k = 0; k < 8; ++k) {
      const int wbo = w3k * (PART - 8192) - L_WD + ((k >> 2) ^ orot) * 7168, o = (((k >> 1) & 1) * 24 + (k & 1) * 16) * 128;
      const lptr wb0 = lds + db0 + wbo, wb1 = lds + db1 + wbo;
      wo0[k] = cat(trd(wb0 + o), trd(wb0 + o + 1024)); wo1[k] = cat(trd(wb1 + o), trd(wb1 + o + 1024));
    }
    rebase(L_WD + 65536);
    TS(14);
    if (wid == 0) {
      asm volatile("s_waitcnt vmcnt(0)" : "+v"(fo) :: "memory");
      _Pragma("nounroll") for (unsigned sp = 0; !ALL(fo == nonce) && sp < SPIN_CAP; ++sp) { fo = flag_ld(ws, foo); PIN(fo); }
    }
    wait_bar<0>();
    UNROLL for (int k = 27; k >= 24; --k) PIN(hf[k]);
    TS(15);
    u32x4 of[8];
    const int oso = WS_O + (mt * 16 + w3k * 4) * 2048 + orot * 4096;
    UNROLL for (int k = 0; k < 4; ++k) of[k] = __builtin_amdgcn_raw_buffer_load_b128(wsr, lane * 16 + k * 1024, oso, SC1);
    SBAR();
    rd_d0(20); rd_d1(20); down2(20, true); down2(22, true);
    UNROLL for (int k = 4; k < 8; ++k) of[k] = __builtin_amdgcn_raw_buffer_load_b128(wsr, lane * 16 + (k & 3) * 1024, oso ^ 4096, SC1);
    const unsigned xo = (unsigned)((mt * 32 + w3k * 8 + rr) * 896 + ng3 * 128 + cc * 16);
    const u32x4 xv = *(const u32x4*)(x + xo);
    SBAR();
    down2(24, true); down2(26, false);
    auto xch_send = [&](const f32x16& a0, const f32x16& a1, int base) AI {
      UNROLL for (int c = 0; c < 8; ++c) { const f32x16& a = c < 4 ? a0 : a1; const int o = (c & 3) * 4;
        *(LAS f32x4*)(lds + (base + wid * 8192 + c * 1024) + ((lane ^ (2 * c)) << 4)) = (f32x4){a[o], a[o + 1], a[o + 2], a[o + 3]}; } };
    int xro = w3m * 8192 + cc * 1024 + (((w3k * 8 + rr) ^ (2 * cc)) << 4); PIN(xro);
    xch_send(F0, F1, 0);
    f32x16 S0 = Z, S1 = Z;
    UNROLL for (int k = 0; k < 4; ++k) { S0 = MFMA(wo0[k], of[k], S0); S1 = MFMA(wo1[k], of[k], S1); }
    PIN(S0); PIN(S1);
    lbar();
    f32x4 g0 = *(LAS f32x4*)(lds + xro), g1 = *(LAS f32x4*)(lds + xro + 512);
    UNROLL for (int k = 1; k < 4; ++k) { g0 += *(LAS f32x4*)(lds + xro + k * 16384); g1 += *(LAS f32x4*)(lds + xro + k * 16384 + 512); }
    UNROLL for (int k = 4; k < 8; ++k) { S0 = MFMA(wo0[k], of[k], S0); S1 = MFMA(wo1[k], of[k], S1); }
    UNROLL for (int i = 0; i < 4; ++i) {
      g0[i] = g0[i] * __builtin_amdgcn_rcpf(1.f + EX(g0[i] * -1.4426950408889634f)); g1[i] = g1[i] * __builtin_amdgcn_rcpf(1.f + EX(g1[i] * -1.4426950408889634f)); }
    TS(16);
    xch_send(S0, S1, L_WD);
    f32x4 y0 = {U2F(xv[0] << 16), U2F(xv[0] & 0xffff0000u), U2F(xv[1] << 16), U2F(xv[1] & 0xffff0000u)}, y1 = {U2F(xv[2] << 16), U2F(xv[2] & 0xffff0000u), U2F(xv[3] << 16), U2F(xv[3] & 0xffff0000u)};
    xro += L_WD;
    PIN(g0); PIN(g1); PIN(y0); PIN(y1); PIN(xro);
    lbar();
    f32x4 t[8];
    UNROLL for (int k = 0; k < 8; ++k) t[k] = *(LAS f32x4*)(lds + xro + (k >> 1) * 16384 + (k & 1) * 512);
    SBAR();
    UNROLL for (int k = 0; k < 4; ++k) UNROLL for (int i = 0; i < 4; ++i) { y0[i] = __builtin_fmaf(t[2 * k][i], g0[i], y0[i]); y1[i] = __builtin_fmaf(t[2 * k + 1][i], g1[i], y1[i]); }
    __builtin_amdgcn_raw_buffer_store_b128((u32x4){pk(y0[0], y0[1]), pk(y0[2], y0[3]), pk(y1[0], y1[1]), pk(y1[2], y1[3])}, SRD(out, 2048 * 896), xo, 0, SC1);
    TS(17); TS_OUT();
  }
}

#if defined(__HIP_DEVICE_COMPILE__)
#define NOPK __attribute__((target("no-packed-fp32-ops")))
#else
#define NOPK
#endif
__global__ __launch_bounds__(512, 2) NOPK void fused_kernel(const char* x, const char* wqkv, const char* wo, const char* wg, const char* wu, const char* wd,
                                                       char* ws, char* out) {
  extern __shared__ __attribute__((aligned(16))) char smem[];
  const lptr lds = (lptr)smem;
  const unsigned lds0 = (unsigned)(uintptr_t)lds;
  const int tid = __builtin_amdgcn_workitem_id_x(), lane = tid & 63, wid = __builtin_amdgcn_readfirstlane(tid >> 6);
  const int rr = lane >> 3, cc = lane & 7;
  const int b = __builtin_amdgcn_workgroup_id_x(), xcd = b & 7, idx = b >> 3;
  const int ng = NG_OF(xcd, idx), mg = MG_OF(xcd, idx);
  {
    unsigned long long pc; asm volatile("s_getpc_b64 %0" : "=s"(pc));
    touch(SRD(pc & ~127ull, 28672), (idx * 8 + (tid >> 6)) * 128, lds0 + L_PF);
  }
  unsigned tsv = 0; TSR(62); TS(0);
  const int rot0 = MAP == 2 ? (mg & 3) * 2 + ((mg >> 2) & 1) : (mg + ROTX * (mg >> 3)) & 7, rot = rot0 == 7 ? 3 : rot0;
  unsigned kc[7];
  UNROLL for (int c = 0; c < 7; ++c) kc[c] = (unsigned)((c + rot >= 7 ? c + rot - 7 : c + rot) * 64);
  const srd_t xr = SRD(x, 2048 * 896);
  const unsigned xv0 = (unsigned)((mg * 128 + wid * 16 + rr) * 896 + ((cc ^ rr) << 4));
  const unsigned xv1 = (unsigned)((mg * 128 + wid * 16 + 8 + (rr ^ 1)) * 896 + ((cc ^ rr ^ 1) << 4));
  const unsigned lw2k = lds0 + wid * 2048;
  auto dma_x = [&](auto cc_) AI { constexpr int c = decltype(cc_)::v; dma2<(c & 3) * 16384>(xr, xv0, xv1, lw2k, kc[c], 2); };
  const srd_t wsr = SRD(ws, WS_END);
  u32x4 srow; unsigned nonce;
  const int srw = mg * 128 + ng * 8 + wid;
  asm volatile("buffer_load_dwordx4 %0, %1, %2, %3 offen" : "=&v"(srow) : "v"(lane * 16), "s"(SRD(x, srw * 896 + 896)), "s"(srw * 896) : "memory");
  dma_x(IC<0>{});
  SBAR();
  const int r = lane & 31, h = lane >> 5, q = (lane & 15) >> 2, p = lane & 3, grp = (lane >> 4) & 1;
  const f32x16 Z = {};
  const int head = 2 * xcd + (idx & 1), qg = idx >> 1;
  auto dma_kv = [&](auto zc, unsigned kvv) AI { constexpr int z = decltype(zc)::v; dma<(z >> 1) * QT + (z & 1) * 1024, ((z >> 1) & 3) * 16384 + (z & 1) * 1024, true>(wsr, kvv, lw2k); };
  u32x4 QA0, QA1, QB0, QB1, kf0, kf1, ka0, ka1, va0; f32x16 s0; lptr kb, vb0, vb1; unsigned kvv = 0;
  {
    const int wm = wid & 3, wn = wid >> 2;
    const bool bq = ng <= 10;
    const unsigned st1 = bq ? 2688 : 3584;
    unsigned va = (16 * wm + rr) * st1 + (((cc << 4) ^ (grp << (bq ? 6 : 5))) ^ (bq ? 0 : wn << 6)) + (bq ? 2 * ng + wn : ng + 12) * 128, vb = va + 8 * st1;
    const char* wgu = wn ? wu : wg;
    const srd_t s2 = SRD(wgu, 448 * 3584), s1 = SRD(bq ? wqkv : wgu, 448 * st1);
    auto dma_w = [&](auto mc) AI {
      constexpr int m = decltype(mc)::v;
      if constexpr (m == 7) { va = (unsigned)((16 * wm + rr) * 3584 + ((cc ^ (grp << 1) ^ (wn << 2)) << 4)) + ng * 128; vb = va + 8 * 3584; PIN(va); PIN(vb); }
      if constexpr (m == 14) {
        va = (unsigned)((16 * wm + (lane >> 2)) * 3584 + (((lane & 3) ^ (wn << 1)) << 4)) + 2048 + ng * 64;
        if (ng >= 14) va = OOB;
        PIN(va);
      }
      if constexpr (m < 7) dma2<wpos(m)>(s1, va, vb, lw2k, kc[m], st1);
      else if constexpr (m < 14) dma2<wpos(m)>(s2, va, vb, lw2k, kc[m - 7], 3584);
      else dma1<wpos(m)>(s2, va, lds0 + wid * 1024, kc[m - 14], 3584);
    };
    dma_w(IC<0>{});
    unsigned epoch;
    asm volatile("buffer_load_dword %0, off, %1, 0 sc1" : "=&v"(epoch) : "s"(wsr) : "memory");
    dma_x(IC<1>{});
    SBAR();
    const int rowb = (8 * h + q) * 128 + p * 8;
    int b20 = grp * 8192 + rowb + (((2 * wn) ^ (q >> 1) ^ (grp << 1)) << 5);
    int b10 = bq ? wn * 8192 + rowb + ((grp ^ ((q >> 1) << 1)) << 5) : b20, b11 = b10 ^ (bq ? 64 : 32);
    int xb[4];
    UNROLL for (int i = 0; i < 4; ++i) { xb[i] = (4 * wm + (r >> 3)) * 1024 + ((r & 7) ^ ((r >> 3) & 1)) * 128 + (((2 * i + h) ^ (r & 7)) << 4); PIN(xb[i]); }
    PIN(b10); PIN(b11); PIN(b20);
    UNROLL for (int c = 2; c < 7; ++c) asm volatile("" : "+s"(kc[c]));
    SBAR();
    u32x4 xf[28];
    auto row_stats = [&]() AI {
      float a = dot2(srow[0], ONES2, 0.f), c = dot2(srow[0], srow[0], 0.f);
      UNROLL for (int i = 1; i < 4; ++i) { a = dot2(srow[i], ONES2, a); c = dot2(srow[i], srow[i], c); }
      a = wave_sum(a); c = wave_sum(c);
      const float mean = a * (1.f / 448.f), var = __builtin_fmaxf(c * (1.f / 448.f) - mean * mean, 0.f);
      unsigned g = pk(-mean, __builtin_amdgcn_sqrtf(rbf(rbf(var) + 1e-5f))); PIN(g); SBAR();
      asm volatile("s_waitcnt vmcnt(4)" : "+v"(epoch) :: "memory");
      nonce = __builtin_amdgcn_readfirstlane(epoch) * 0x9E3779B1u + 0x7F4A7C15u;
      if (lane == 63) __hip_atomic_store((GAS unsigned long long*)(ws + WS_ST + srw * 8), (unsigned long long)nonce << 32 | g, RLX_AGENT);
    };
    auto rd_x = [&](int k0, int k1) AI { UNROLL for (int ks = k0; ks < k1; ++ks) xf[ks] = ld128(lds + xb[ks & 3] + ((ks >> 2) & 3) * 16384); };
    u32x2 wf[NWF][4];
    int ra0 = 0, ra1 = 0;
    auto rd_w = [&](int n) AI {
      if (n < 56) {
        const int m = n >> 2, o = (n & 3) * 2048;
        if ((n & 3) == 0) { ra0 = (m < 7 ? b10 : b20) + wpos(m); ra1 = (m < 7 ? b11 : b20 ^ 32) + wpos(m); PIN(ra0); PIN(ra1); }
        wf[n % NWF][0] = trd(lds + ra0 + o); wf[n % NWF][1] = trd(lds + ra0 + o + 512); wf[n % NWF][2] = trd(lds + ra1 + o); wf[n % NWF][3] = trd(lds + ra1 + o + 512);
      } else {
        const int o = ((n - 56) >> 1) * 8192 + (n & 1) * 2048;
        if (n == 56) { ra0 = wpos(14) + grp * 4096 + (8 * h + q) * 64 + ((wn ^ grp) << 5) + p * 8; PIN(ra0); }
        wf[n % NWF][0] = trd(lds + ra0 + o); wf[n % NWF][1] = trd(lds + ra0 + o + 256); wf[n % NWF][2] = trd(lds + ra0 + o + 1024); wf[n % NWF][3] = trd(lds + ra0 + o + 1280);
      }
    };
    SBAR();
    dma_w(IC<1>{});
    asm volatile("s_waitcnt vmcnt(9)" : "+v"(srow) :: "memory");
    row_stats();
    SBAR();
    lbar();
    TS(1);
    UNROLL for (int n = 0; n < RA; ++n) rd_w(n);
    rd_x(0, 4);
    dma_x(IC<2>{});
    f32x16 A0 = Z, A1 = Z, B0 = Z, B1 = Z, C0 = Z;
    float cs0 = 0.f, cs1 = 0.f, rstd = 0.f, sig = 0.f, kg = 0.f; unsigned mm = 0; u32x2 sg = {0u, ~nonce};
    const int mt = mg * 4 + wm;
    const unsigned csel = ((wm & 1) ? 0x05040c0cu : 0x01000c0cu) + ((wm >> 1) ? 0x02020000u : 0u);
    auto colsum = [&](int n, int hh) AI {
      cs0 += U2F(__builtin_amdgcn_perm(wf[n % NWF][2 + hh][0], wf[n % NWF][hh][0], csel)); cs1 += U2F(__builtin_amdgcn_perm(wf[n % NWF][2 + hh][1], wf[n % NWF][hh][1], csel));
      PIN(cs0); PIN(cs1);
    };
    auto cs_out = [&]() AI {
      const float cs = cs0 + cs1, hi = rbf(cs); *(LAS unsigned*)(lds + L_CS + wn * 1024 + lane * 16 + ((wm & 1) * 2 + (wm >> 1)) * 4) = pk(hi, cs - hi); cs0 = 0.f; cs1 = 0.f; };
    auto fix2 = [&](f32x16& a0, f32x16& a1) AI {
      const u32x4 c = ld128(lds + L_CS + wn * 1024 + lane * 16);
      a0 = MFMA(c, ((u32x4){mm, mm, 0u, 0u}), a0); a1 = MFMA(c, ((u32x4){0u, 0u, mm, mm}), a1); PIN(a0); PIN(a1); };
    auto glu = [&](const f32x16& acc, int i) AI { return acc[i + 8] * __builtin_amdgcn_rcpf(__builtin_fmaf(EX(acc[i] * kg), sig, sig)); };
    auto glu8 = [&](const f32x16& acc) AI { return (u32x4){pk(glu(acc, 0), glu(acc, 1)), pk(glu(acc, 2), glu(acc, 3)), pk(glu(acc, 4), glu(acc, 5)), pk(glu(acc, 6), glu(acc, 7))}; };
    const int hs2 = WS_H + (mt * 112 + ng * 4 + wn * 2) * 1024;
    u32x4 tb0 = {0u, 0u, 0u, 0u}, tb1 = tb0;
    auto epi1 = [&](const f32x16& acc, int t) AI {
      if (bq) {
        const u32x4 e = t ? tb1 : tb0; const unsigned o[3] = {e[0] ^ (r << 4), e[1] ^ (r << 4), e[2] ^ (r << 4)};
        const unsigned m[2] = {(unsigned)__builtin_amdgcn_sbfe(e[3], 0, 1), (unsigned)__builtin_amdgcn_sbfe(e[3], 1, 1)};
        const float sc = 2 * ng + wn < 7 ? rstd * (0.18898223650461362f * 1.4426950408889634f) : rstd;
        u32x4 v[2];
        UNROLL for (int pp = 0; pp < 2; ++pp)
          v[pp] = widen((u32x2){pk(acc[8 * pp] * sc, acc[8 * pp + 1] * sc), pk(acc[8 * pp + 2] * sc, acc[8 * pp + 3] * sc)},
                        (u32x2){pk(acc[8 * pp + 4] * sc, acc[8 * pp + 5] * sc), pk(acc[8 * pp + 6] * sc, acc[8 * pp + 7] * sc)});
        u32x4 d0 = {v[0][0], v[0][1], bfi(m[0], PAD0, v[0][2]), ~m[0] & v[0][3]}, d1 = {v[1][0], v[1][1], bfi(m[1], PAD0, v[1][2]), ~m[1] & v[1][3]};
        u32x4 d2 = {PAD0, 0u, bfi(m[0], v[0][2], v[1][2]), bfi(m[0], v[0][3], v[1][3])};
        PIN(d0); PIN(d1); PIN(d2); SBAR();
        __builtin_amdgcn_raw_buffer_store_b128(d0, wsr, o[0], mt * QT, SC1); __builtin_amdgcn_raw_buffer_store_b128(d1, wsr, o[1], mt * QT, SC1);
        st128(d2, wsr, o[2], mt * QT, SC1);
      } else st128(glu8(acc), wsr, lane * 16 + t * 1024, hs2 + 12 * 4096, SC1);
    };
    u32x4 hp, hq; unsigned fl0 = ~nonce; f32x16 sa, sb; u32x4 cfix;
    int fqo = 0;
    auto fq_off = [&]() AI {
      const int c0 = (lane < 8 ? 448 + (wid & 1) * 448 : 0) + head * 28 - (head & 1) * 4 + (lane & 1) * 31, g = c0 >> 7, m2 = lane < 8 ? (wid >> 1) * 4 + (lane >> 1) : qg;
      fqo = WS_FQ + (m2 * 16 + g) * 128; };
    const int sgo = WS_ST + (mg * 128 + wm * 32 + r) * 8;

    sfor<0, 70>([&](auto nc) AI {
      constexpr int n = decltype(nc)::v, u = n & 7;
      constexpr int k0 = n < 28 ? n : n < 56 ? n - 28 : 2 * (n - 56), k1 = n < 56 ? k0 : k0 + 1;
      f32x16& D0 = n < 28 ? A0 : n < 56 ? B0 : C0; f32x16& D1 = n < 28 ? A1 : n < 56 ? B1 : C0;
      constexpr bool early = n == 54 || n == 55 || n >= 68;
      PIN(wf[n % NWF][3]);
      if (u == 7) __builtin_amdgcn_s_setprio(3); if (u == 1) __builtin_amdgcn_s_setprio(2); if (u == 3) __builtin_amdgcn_s_setprio(1); if (u == 5) __builtin_amdgcn_s_setprio(0);
      SBAR();
      D0 = MFMA(cat(wf[n % NWF][0], wf[n % NWF][1]), xf[k0], D0); PIN(D0);
      SBAR();
      if constexpr (u == 6 || n == 68 || n == 2) {
        if constexpr (n == 54 || n == 68) { UNROLL for (int e = 0; e < RA; ++e) { colsum(n + e, 0); colsum(n + e, 1); } cs_out(); }
        if (n == 2) wait_bar<7>(); else if (n <= 14) wait_bar<2>(); else if (n == 22) wait_bar<4>(); else if (n == 30) wait_bar<6>();
        else if (n == 38) { if (bq) wait_bar<10>(); else wait_bar<6>(); } else if (n == 46) wait_bar<4>(); else if (n == 54) wait_bar<5>();
        else if (n == 62) lbar(); else wait_bar<4>();
        if (n == 46 && wid == 0) flag_st(ws, WS_FQ + (mg * 16 + ng) * 128, nonce);
        if (n == 6) TS(2); if (n == 14) TS(3); if (n == 30) TS(4); if (n == 46) TS(5); if (n == 62) TS(6); if (n == 68) TS(7);
        SBAR();
      }
      constexpr bool d1f = u == 6 || n == 68 || n == 2;
      if constexpr (d1f) { D1 = MFMA(cat(wf[n % NWF][2], wf[n % NWF][3]), xf[k1], D1); PIN(D1); SBAR(); }
      if constexpr (n + RA < 70) rd_w(n + RA);
      if constexpr (n == 68) {
        const lptr qb = lds + L_Q + (wid & 1) * 4096 + ((wid >> 1) & 1) * 2048 + lane * 16, qa = lds + L_Q + (wid & 1) * 4096 + (((wid >> 1) & 1) ^ 1) * 2048 + lane * 16;
        kf0 = ld128(lds + lane * 16); QA0 = ld128(qa); QB0 = ld128(qb); kf1 = ld128(lds + 1024 + lane * 16); QA1 = ld128(qa + 1024); QB1 = ld128(qb + 1024);
        cfix = ld128(lds + L_CS + wn * 1024 + lane * 16);
      }
      if constexpr (n == 69) { sa = MFMA(kf1, QA1, sa); sb = MFMA(kf1, QB1, sb); PIN(sa); PIN(sb); }
      if constexpr (n == 2) rd_x(4, 8);
      if constexpr (u == 6 && n < 24) rd_x(n + 2, n + 6);
      if constexpr (u == 0 && n > 0 && n < 24) rd_x(n + 4, n + 8);
      if constexpr (!early) colsum(n, 0);
      SBAR();
      if constexpr (!d1f) { D1 = MFMA(cat(wf[n % NWF][2], wf[n % NWF][3]), xf[k1], D1); PIN(D1); }
      SBAR();
      if constexpr (!early) colsum(n, 1);
      if constexpr (n == 0) dma_w(IC<2>{}); if constexpr (n == 1) dma_x(IC<3>{}); if constexpr (n == 2) dma_w(IC<3>{}); if constexpr (n == 4) dma_w(IC<4>{});
      if constexpr (n == 6) dma_x(IC<4>{}); if constexpr (n == 7) dma_x(IC<5>{}); if constexpr (n == 8) dma_w(IC<5>{}); if constexpr (n == 10) dma_w(IC<6>{});
      if constexpr (n == 14) dma_x(IC<6>{}); if constexpr (n == 15) dma_w(IC<7>{}); if constexpr (n == 18) dma_w(IC<8>{}); if constexpr (n == 20) dma_w(IC<9>{});
      if constexpr (n == 23) dma_w(IC<10>{}); if constexpr (n == 26) dma_w(IC<11>{}); if constexpr (n == 29) dma_w(IC<12>{}); if constexpr (n == 32) dma_w(IC<13>{});
      if constexpr (n == 48) touch(SRD(b < 196 ? wd : wo, b < 196 ? 1792 * 896 : 448 * 896), lane < 8 ? (((b < 196 ? b : b - 196) * 8 + wid) * 8 + lane) * 128 : OOB, lds0 + L_PF + wid * 256);
      if constexpr (n == 17) asm volatile("buffer_load_dwordx2 %0, %1, %2, 0 offen sc1" : "+v"(sg) : "v"(sgo), "s"(wsr) : "memory");
      if constexpr (n == 16) asm volatile("global_load_dwordx4 %0, %2, %3\n\tglobal_load_dwordx4 %1, %2, %3 offset:32" : "+v"(tb0), "+v"(tb1) : "v"(h * 16), "s"(&qkv_tab.v[bq ? 4 * ng + 2 * wn : 0]) : "memory");
      if constexpr (n >= 39 && n < 53 && (n & 1)) dma_w(IC<14 + (n - 39) / 2>{});
      if constexpr (n == 27) cs_out();
      if constexpr (n == 23) {
        PIN(sg); PIN(tb0); PIN(tb1);
        _Pragma("nounroll") for (unsigned sp = 0; !ALL(sg[1] == nonce) && sp < SPIN_CAP; ++sp)
          asm volatile("buffer_load_dwordx2 %0, %1, %2, 0 offen sc1\n\ts_waitcnt vmcnt(0)" : "+v"(sg) : "v"(sgo), "s"(wsr) : "memory");
        sig = U2F(sg[0] & 0xffff0000u); rstd = __builtin_amdgcn_rcpf(sig); mm = __builtin_amdgcn_perm(sg[0], sg[0], 0x01000100u); kg = -1.4426950408889634f * rstd;
        PIN(sig); PIN(rstd); PIN(kg); PIN(mm);
      }
      if constexpr (n == 30) fix2(A0, A1);
      if constexpr (n == 31) epi1(A0, 0);
      if constexpr (n == 34) epi1(A1, 1);
      if constexpr (n == FQP) { fq_off(); asm volatile("buffer_load_dword %0, %1, %2, 0 offen sc1" : "+v"(fl0) : "v"(fqo), "s"(wsr) : "memory"); }
      if constexpr (n == 55) fix2(B0, B1);
      if constexpr (n == 57) { hp = glu8(B0); PIN(hp); }
      if constexpr (n == 64) { hq = glu8(B1); PIN(hq); }
      if constexpr (n == 69) { st128(hp, wsr, lane * 16, hs2, SC1); st128(hq, wsr, lane * 16 + 1024, hs2, SC1); }
      if constexpr (n == FQC) {
        asm volatile("s_waitcnt vmcnt(0)" : "+v"(fl0) :: "memory");
        _Pragma("nounroll") for (unsigned sp = 0; !ALL(fl0 == nonce) && sp < SPIN_CAP; ++sp)
          asm volatile("buffer_load_dword %0, %1, %2, 0 offen sc1\n\ts_waitcnt vmcnt(0)" : "+v"(fl0) : "v"(fqo), "s"(wsr) : "memory");
        kvv = (unsigned)(WS_QKV + head * QH + (wid >> 1) * 16 * QT + 2048 + (wid & 1) * 2048 + lane * 16);
        dma<0, L_Q, true>(wsr, WS_QKV + head * QH + (qg * 4 + (wid >> 1)) * QT + (wid & 1) * 1024 + lane * 16, lds0 + wid * 1024); dma_kv(IC<0>{}, kvv); }
      if constexpr (n > FQC && n < FQC + 8) dma_kv(IC<n - FQC>{}, kvv);
      if constexpr (n == 67) {
        const int vc = 2 * (grp ^ wn) + (p >> 1);
        kb = lds + (wid >> 1) * 4096 + lane * 16;
        vb0 = lds + (wid >> 1) * 4096 + 2048 + vc * 512 + ((((4 * h) ^ (4 * vc)) + q) << 4) + (p & 1) * 8;
        vb1 = lds + (wid >> 1) * 4096 + 2048 + vc * 512 + ((((4 * h + 8) ^ (4 * vc)) + q) << 4) + (p & 1) * 8;
        PIN(kb); PIN(vb0); PIN(vb1);
      }
      if constexpr (n == 68) { sa = MFMA(kf0, QA0, Z); sb = MFMA(kf0, QB0, Z); PIN(sa); PIN(sb); ka0 = ld128(kb); ka1 = ld128(kb + 1024); va0 = cat(trd(vb0), trd(vb1)); }
    });
    SBAR();
    C0 = MFMA(cfix, ((u32x4){mm, mm, mm, mm}), C0);
    float ma = sa[0], mb = sb[0];
    UNROLL for (int i = 1; i < 16; ++i) { ma = __builtin_fmaxf(ma, sa[i]); mb = __builtin_fmaxf(mb, sb[i]); }
    const unsigned pa = pk(1.f - max32(ma), 0.f), pb = pk(1.f - max32(mb), 0.f);
    const bool p0 = (head & 1) && h == 0, p1 = !(head & 1) && h == 1;
    QA0[0] = p0 ? pa : QA0[0]; QA1[2] = p1 ? pa : QA1[2]; QB0[0] = p0 ? pb : QB0[0]; QB1[2] = p1 ? pb : QB1[2];
    s0 = MFMA(ka0, QA0, Z); s0 = MFMA(ka1, QA1, s0);
    PIN(s0); PIN(QB0); PIN(QB1);
    SBAR();
    st128(glu8(C0), wsr, ng < 14 ? lane * 16 : OOB, WS_H + (mt * 112 + 64 + ng * 2 + wn) * 1024, SC1);
    SBAR();
    TS(8);
  }
  return phase23(x, wo, wd, ws, out, lds, nonce, QA0, QA1, QB0, QB1, ka0, ka1, va0, s0, kb, vb0, vb1, kvv, wid TSARG);
}

extern "C" void kernel_launch(void* const* d_in, const int* in_sizes, int n_in, void* d_out, int out_size, void* d_ws, size_t ws_size,
                              hipStream_t stream) {
  (void)hipFuncSetAttribute((const void*)fused_kernel, hipFuncAttributeMaxDynamicSharedMemorySize, LDS_BYTES);
  fused_kernel<<<256, 512, LDS_BYTES, stream>>>((const char*)d_in[0], (const char*)d_in[1], (const char*)d_in[2], (const char*)d_in[4], (const char*)d_in[5],
                                                (const char*)d_in[6], (char*)d_ws, (char*)d_out);
}

#undef DBG_TS
```

```cpp
#include <hip/hip_runtime.h>
#include <hip/hip_bf16.h>
#include <stdint.h>

typedef short bf16x8 __attribute__((ext_vector_type(8)));
typedef short s16x4 __attribute__((ext_vector_type(4)));
typedef float f32x16 __attribute__((ext_vector_type(16)));
typedef float f32x4 __attribute__((ext_vector_type(4)));
typedef float f32x2 __attribute__((ext_vector_type(2)));
typedef unsigned u32x4 __attribute__((ext_vector_type(4)));
typedef unsigned u32x2 __attribute__((ext_vector_type(2)));
typedef __bf16 bf2 __attribute__((ext_vector_type(2)));
typedef __amdgpu_buffer_rsrc_t srd_t;
#define LAS __attribute__((address_space(3)))
#define GAS __attribute__((address_space(1)))
typedef LAS char* lptr;
#define DI __device__ __forceinline__
#define AI __attribute__((always_inline))
#define MFMA(a, b, c) __builtin_amdgcn_mfma_f32_32x32x16_bf16(__builtin_bit_cast(bf16x8, (a)), __builtin_bit_cast(bf16x8, (b)), (c), 0, 0, 0)
#define PIN(x) asm volatile("" : "+v"(x))
#define SBAR() __builtin_amdgcn_sched_barrier(0)
#define RLX_AGENT __ATOMIC_RELAXED, __HIP_MEMORY_SCOPE_AGENT
#define EX(v) __builtin_amdgcn_exp2f(v)
#define F2U(x) __builtin_bit_cast(unsigned, (float)(x))
#define U2F(x) __builtin_bit_cast(float, (unsigned)(x))
#define ANY(p) (__builtin_amdgcn_ballot_w64(p) != 0)
#define ALL(p) (__builtin_amdgcn_ballot_w64(!(p)) == 0)
#define UNROLL _Pragma("unroll")
#define SRD(p, n) __builtin_amdgcn_make_buffer_rsrc((void*)(p), 0, (n), 0x00020000)

constexpr int WS_E = 0;
constexpr int WS_FQ = 4096;
constexpr int WS_FH = WS_FQ + 256 * 128;
constexpr int WS_FO = WS_FH + 256 * 128;
constexpr int WS_ST = WS_FO + 256 * 128;
constexpr int WS_QKV = 1 << 20;
constexpr int QT = 6144, QH = 64 * QT;
constexpr int WS_O = WS_QKV + 16 * QH;
constexpr int WS_H = WS_O + (2 << 20);
constexpr int WS_END = WS_H + 64 * 112 * 1024;
constexpr int OOB = 1 << 30;
constexpr int SC1 = 16;
constexpr unsigned SPIN_CAP = 1u << 20;

#define TS(i) do { } while (0)
#define TSR(i) do { } while (0)
#define TS_OUT() do { } while (0)

template <int I> struct IC { static constexpr int v = I; };
template <int B, int E, class F> DI void sfor(F&& f) { if constexpr (B < E) { f(IC<B>{}); sfor<B + 1, E>(f); } }
template <int N> DI void wait_bar() { asm volatile("s_waitcnt vmcnt(%0) lgkmcnt(0)\n\ts_barrier" :: "n"(N) : "memory"); }
template <int N> DI void vwait_bar() { asm volatile("s_waitcnt vmcnt(%0)\n\ts_barrier" :: "n"(N) : "memory"); }
DI void lbar() { asm volatile("s_waitcnt lgkmcnt(0)\n\ts_barrier" ::: "memory"); }
DI unsigned pk(float a, float b) { f32x2 v = {a, b}; return __builtin_bit_cast(unsigned, __builtin_convertvector(v, bf2)); }
DI float rbf(float a) { return U2F(pk(a, a) & 0xffff0000u); }
DI float dot2(unsigned a, unsigned b, float c) { return __builtin_amdgcn_fdot2_f32_bf16(__builtin_bit_cast(bf2, a), __builtin_bit_cast(bf2, b), c, false); }
DI u32x2 trd(lptr p) { return __builtin_bit_cast(u32x2, __builtin_amdgcn_ds_read_tr16_b64_v4i16((LAS s16x4*)p)); }
DI u32x4 cat(u32x2 a, u32x2 b) { return (u32x4){a[0], a[1], b[0], b[1]}; }
DI u32x4 ld128(lptr p) { return *(LAS u32x4*)p; }
DI unsigned bfi(unsigned m, unsigned a, unsigned b) { return (m & a) | (~m & b); }
template <int G, int L, bool DEV = false> DI void dma(srd_t srd, unsigned voff, unsigned lbase) {
  unsigned t;
  if constexpr (DEV)
    asm volatile("s_add_u32 m0, %3, %5\n\ts_mov_b32 %0, %4\n\tbuffer_load_dwordx4 %1, %2, %0 offen sc1 lds" : "=&s"(t) : "v"(voff), "s"(srd), "s"(lbase), "n"(G), "n"(L) : "memory", "scc");
  else
    asm volatile("s_add_u32 m0, %3, %5\n\ts_mov_b32 %0, %4\n\tbuffer_load_dwordx4 %1, %2, %0 offen lds" : "=&s"(t) : "v"(voff), "s"(srd), "s"(lbase), "n"(G), "n"(L) : "memory", "scc");
}
template <int L> DI void dma2(srd_t srd, unsigned voff0, unsigned voff1, unsigned lbase, unsigned ga, unsigned gb) {
  unsigned t;
  asm volatile("s_add_u32 m0, %4, %5\n\ts_mul_i32 %0, %6, %7\n\tbuffer_load_dwordx4 %1, %3, %0 offen lds\n\ts_add_u32 m0, m0, 1024\n\ts_nop 0\n\tbuffer_load_dwordx4 %2, %3, %0 offen lds"
               : "=&s"(t) : "v"(voff0), "v"(voff1), "s"(srd), "s"(lbase), "n"(L), "s"(ga), "s"(gb) : "memory", "scc");
}
template <int L> DI void dma1(srd_t srd, unsigned voff, unsigned lbase, unsigned ga, unsigned gb) {
  unsigned t;
  asm volatile("s_add_u32 m0, %3, %4\n\ts_mul_i32 %0, %5, %6\n\tbuffer_load_dwordx4 %1, %2, %0 offen lds" : "=&s"(t) : "v"(voff), "s"(srd), "s"(lbase), "n"(L), "s"(ga), "s"(gb) : "memory", "scc");
}
DI void touch(srd_t srd, unsigned voff, unsigned ldst) { asm volatile("s_mov_b32 m0, %2\n\ts_nop 0\n\tbuffer_load_dword %0, %1, 0 offen lds" :: "v"(voff), "s"(srd), "s"(ldst) : "memory"); }
DI void st128(u32x4 v, srd_t srd, int voff, int soff, int aux) { __builtin_amdgcn_raw_buffer_store_b128(v, srd, voff, soff, SC1); SBAR(); asm volatile("s_nop 1"); SBAR(); }
DI float swap32(float v, int h) {
  auto r = __builtin_amdgcn_permlane32_swap(F2U(v), F2U(v), false, false);
  return U2F(h ? r[0] : r[1]);
}
DI float max32(float v) {
  auto r = __builtin_amdgcn_permlane32_swap(F2U(v), F2U(v), false, false);
  return __builtin_fmaxf(U2F(r[0]), U2F(r[1]));
}
DI u32x4 widen(u32x2 a, u32x2 b) {
  auto r0 = __builtin_amdgcn_permlane32_swap(a[0], b[0], false, false); auto r1 = __builtin_amdgcn_permlane32_swap(a[1], b[1], false, false);
  return (u32x4){r0[0], r1[0], r0[1], r1[1]};
}
DI float wave_sum(float v) {
  v += U2F(__builtin_amdgcn_update_dpp(0u, F2U(v), 0x111, 0xf, 0xf, true));
  v += U2F(__builtin_amdgcn_update_dpp(0u, F2U(v), 0x112, 0xf, 0xf, true));
  v += U2F(__builtin_amdgcn_update_dpp(0u, F2U(v), 0x114, 0xf, 0xf, true));
  v += U2F(__builtin_amdgcn_update_dpp(0u, F2U(v), 0x118, 0xf, 0xf, true));
  v += U2F(__builtin_amdgcn_update_dpp(0u, F2U(v), 0x142, 0xa, 0xf, true));
  v += U2F(__builtin_amdgcn_update_dpp(0u, F2U(v), 0x143, 0xc, 0xf, true));
  return v;
}
DI unsigned flag_ld(const char* ws, int off) { return __hip_atomic_load((GAS unsigned*)(ws + off), RLX_AGENT); }
DI void flag_st(char* ws, int off, unsigned v) {
  if (__builtin_amdgcn_mbcnt_hi(~0u, __builtin_amdgcn_mbcnt_lo(~0u, 0u)) == 0) __hip_atomic_store((GAS unsigned*)(ws + off), v, RLX_AGENT); }

constexpr int RA = 2, NWF = RA + 1;
constexpr int PART = 14336;
constexpr int L_W = 65536;
constexpr int L_CS = 9 * 16384;
constexpr int L_PF = L_CS + 2048;
constexpr int L_Q = 65536;
constexpr int wpos(int m) { return m < 9 ? L_W + (m % 5) * 16384 : m == 9 ? 49152 : m == 10 ? L_W : m < 14 ? (m - 11) * 16384 : L_W + 16384 + (m - 14) * 8192; }
#define FQP 54
#define FQC 60
#define PUB 45
#define MAP 0
#define NG_OF(xcd, idx) ((xcd & 3) * 4 + (idx & 3))
#define MG_OF(xcd, idx) ((xcd >> 2) * 8 + (idx >> 2))
#define ROTX 0
constexpr int L_WD = 65536;
constexpr int LDS_BYTES = 163840;
constexpr unsigned ONES2 = 0x3f803f80u, PAD0 = 0x00003f80u;
static_assert(L_PF + 2048 <= LDS_BYTES, "LDS map");

constexpr int DN0 = 7;
constexpr int kv_at(int s, int p) { return p == 0 && s > 0 && s + 3 < 16 ? (s + 3) * 2 + 1 : p == 7 && s + 4 < 16 ? (s + 4) * 2 : -1; }
constexpr int wd_at(int s, int p) {
  if (s >= 1 && s <= 3) return p == 1 ? s * 4 - 4 : p == 3 ? s * 4 - 3 : p == 4 ? s * 4 - 2 : p == 5 ? s * 4 - 1 : -1;
  for (int g = 0; g < 2; ++g) { if (s == 10 + 4 * g && p == 7) return 12 + 4 * g; if (s == 11 + 4 * g) return p == 0 ? 13 + 4 * g : p == 2 ? 14 + 4 * g : p == 5 ? 15 + 4 * g : -1; }
  return -1;
}
constexpr int h_at(int s, int p) { const int z = (s - 5) * 3 + (p == 1 ? 1 : p == 3 ? 2 : 3); if (s == 15) return p == 1 ? 24 : p == 3 ? 25 : p == 4 ? 26 : p == 7 ? 27 : -1;
  return s == 4 && p == 7 ? 0 : s >= 5 && (p == 1 || p == 3 || p == 4) && z < 24 ? z : -1; }
constexpr bool pf_at(int s, int p) { return s == 4 && p == 1; }
constexpr int n_at(int s, int p) { return (kv_at(s, p) >= 0) + (wd_at(s, p) >= 0) + (h_at(s, p) >= 0) + pf_at(s, p); }
constexpr int cnt_after(int s0, int p0, int s) { int n = 0; for (int z = s0 * 8 + p0 + 1; z < s * 8 + 6; ++z) n += n_at(z / 8, z % 8); return n; }
constexpr int dslot(int sub) { return L_WD + (sub % 3) * 32768; }

struct QkvTab { unsigned v[44][2][4]; };
constexpr QkvTab make_tab() {
  QkvTab t = {};
  for (int tile = 0; tile < 44; ++tile) for (int h = 0; h < 2; ++h) {
    const int col0 = tile * 32, sec = col0 / 448; unsigned* e = t.v[tile][h];
    e[0] = e[1] = e[2] = OOB; e[3] = 0;
    if (tile < 42) for (int pp = 0; pp < 2; ++pp) {
      const int cu = (col0 - sec * 448) / 8 + 2 * pp + h, j = cu / 7, w = cu % 7, hd = 2 * j + (w > 3), ch = w > 3 ? w - 3 : w;
      e[pp] = WS_QKV + sec * 2048 + hd * QH + ch * 512 + (sec == 2 ? ch * 64 : 0);
      if (w == 3) { e[2] = WS_QKV + sec * 2048 + (hd + 1) * QH; e[3] |= 1u << pp; }
    }
  }
  return t;
}
__device__ const QkvTab qkv_tab = make_tab();

#define TSARG
#define TSPAR
#define LANE_IDS() int lane; asm volatile("v_mbcnt_lo_u32_b32 %0, -1, 0\n\tv_mbcnt_hi_u32_b32 %0, -1, %0" : "=v"(lane));        \
  const int r = lane & 31, h = lane >> 5, q = (lane & 15) >> 2, p = lane & 3, grp = (lane >> 4) & 1, rr = lane >> 3, cc = lane & 7
DI void phase23(const char* x, const char* wo, const char* wd, char* ws, char* out, lptr lds, unsigned nonce, u32x4 QA0, u32x4 QA1, u32x4 QB0, u32x4 QB1,
                u32x4 k0, u32x4 k1, u32x4 v0, f32x16 sa, lptr kb, lptr vb0, lptr vb1, unsigned kvv, int wid TSPAR) {
  const unsigned lds0 = (unsigned)(uintptr_t)lds;
  const int b = __builtin_amdgcn_workgroup_id_x(), xcd = b & 7, idx = b >> 3;
  const int ng = NG_OF(xcd, idx), mg = MG_OF(xcd, idx);
  const srd_t wsr = SRD(ws, WS_END);
  const f32x16 Z = {};
  const int head = 2 * xcd + (idx & 1), qg = idx >> 1, odd = head & 1;
  const int idx3 = idx < 28 ? idx : idx - 28, mq3 = (idx3 * 37) >> 8, mg3 = xcd * 4 + mq3, ng3 = idx3 - mq3 * 7;
  LANE_IDS();
  const unsigned lw2k = lds0 + wid * 2048;
  auto dma_kv = [&](auto zc) AI { constexpr int z = decltype(zc)::v; dma<(z >> 1) * QT + (z & 1) * 1024, ((z >> 1) & 3) * 16384 + (z & 1) * 1024, true>(wsr, kvv, lw2k); };
  const int w3m = wid & 1, w3k = wid >> 1;
  u32x4 hf[28];
  const int ft = w3k & 1, fs = w3k >> 1;
  unsigned vo_d = 0; const unsigned ld_d = lds0 + wid * 4096;
  auto set_vo_d = [&]() AI { vo_d = (unsigned)(((idx < 28 ? w3k * 448 + w3m * 32 : OOB / 896) + rr) * 896 + ((cc ^ (grp << 2)) << 4) + ng3 * 128); PIN(vo_d); };
  const srd_t sD = SRD(wd, 1792 * 896);
  auto dma_d = [&](auto zc) AI { constexpr int z = decltype(zc)::v; dma<((z >> 2) * 64 + (z & 3) * 8) * 896, dslot(z >> 2) + (z & 3) * 1024>(sD, vo_d, ld_d); };
  int db0 = 0, db1 = 0, dbo = L_WD;
  auto set_db = [&]() AI { db0 = L_WD + w3k * 8192 + (4 * h + q) * 128 + p * 8 + ((grp ^ ((q >> 1) << 1)) << 5); db1 = db0 ^ 64; PIN(db0); PIN(db1); };
  auto rebase = [&](int o) AI { db0 += o - dbo; db1 += o - dbo; dbo = o; PIN(db0); PIN(db1); };
  f32x16 F0 = Z, F1 = Z;
  u32x4 dw0, dw1;
  auto rd_d0 = [&](int d) AI { const int o = dslot(d >> 2) + (d & 3) * 2048 - dbo; dw0 = cat(trd(lds + db0 + o), trd(lds + db0 + o + 1024)); };
  auto rd_d1 = [&](int d) AI { const int o = dslot(d >> 2) + (d & 3) * 2048 - dbo; dw1 = cat(trd(lds + db1 + o), trd(lds + db1 + o + 1024)); };
  {
    const int wq = wid & 1, kq = wid >> 1;
    u32x4 v1, pa, pb;
    float e0, e1, e2, e3;
    f32x16 OA = Z, OB = Z, sb;
    unsigned junk, fh = ~nonce; const int fho = WS_FH + ((mg3 >> 1) * 16 + (lane & 15)) * 128;
    const int hld = WS_H + ((mg3 * 2 + w3m) * 112 + w3k * 28) * 1024, hlv = idx < 28 ? lane * 16 : OOB;
    auto send = [&](const f32x16& a, int hh, int j) AI {
      const lptr d = kb + (((wid ^ (2 * j)) * 6144 + (j - 1) * 2048) - kq * 4096);
      *(LAS f32x4*)d = (f32x4){a[8 * hh], a[8 * hh + 1], a[8 * hh + 2], a[8 * hh + 3]}; *(LAS f32x4*)(d + 1024) = (f32x4){a[8 * hh + 4], a[8 * hh + 5], a[8 * hh + 6], a[8 * hh + 7]}; };
    TS(10); TS(19);
#define EX2(S, i, a, c) do { a = EX(S[i]); c = EX(S[(i) + 1]); PIN(a); PIN(c); } while (0)
#define STEP(FIRST, SC, SN, QN0, QN1, OP, OC, L0, L1, L2, L3, X0, X1) do {                                                       \
      SBAR(); SN = MFMA(k0, QN0, Z); SBAR(); L0;                                                                          \
      if (!(FIRST)) { pb[2] = pk(e0, e1); pb[3] = pk(e2, e3); PIN(pb); } SBAR(); EX2(SC, 0, e0, e1); SBAR(); X0; SBAR(); EX2(SC, 2, e2, e3); \
      SBAR(); if (!(FIRST)) OP = MFMA(v1, pb, OP); SBAR(); L1;                                                                          \
      pa[0] = pk(e0, e1); pa[1] = pk(e2, e3); PIN(pa); SBAR(); EX2(SC, 4, e0, e1); EX2(SC, 6, e2, e3);                    \
      SBAR(); SN = MFMA(k1, QN1, SN); SBAR(); L2;                                                                         \
      pa[2] = pk(e0, e1); pa[3] = pk(e2, e3); PIN(pa); SBAR(); EX2(SC, 8, e0, e1); SBAR(); X1; SBAR(); EX2(SC, 10, e2, e3); \
      SBAR(); OC = MFMA(v0, pa, OC); SBAR(); L3;                                                                          \
      pb[0] = pk(e0, e1); pb[1] = pk(e2, e3); PIN(pb); SBAR(); EX2(SC, 12, e0, e1); EX2(SC, 14, e2, e3); SBAR(); } while (0)
    sfor<0, 16>([&](auto sc_) AI {
      constexpr int s = decltype(sc_)::v, so = (s & 3) * 16384, sn = ((s + 1) & 3) * 16384;
      auto side = [&](auto pc) AI {
        constexpr int P = decltype(pc)::v, kv = kv_at(s, P), wz = wd_at(s, P), hz = h_at(s, P);
        if constexpr (s == 0 && P == 3) set_vo_d();
        if constexpr (s == 0 && P == 5) set_db();
        if constexpr (kv >= 0) dma_kv(IC<kv>{});
        if constexpr (wz >= 0) dma_d(IC<wz>{});
        if constexpr (pf_at(s, P)) {
          const int row = wid < 2 ? mq3 * 448 + 320 + wid * 64 + lane : mq3 * 112 + (wid - 2) * 56 + (lane < 56 ? lane : 55);
          asm volatile("buffer_load_dword %0, %1, %2, 0 offen" : "=&v"(junk) : "v"(wid < 4 && idx < 28 ? row * 896 + ng3 * 128 : OOB), "s"(SRD(wid < 2 ? wd : wo, wid < 2 ? 1792 * 896 : 448 * 896)) : "memory");
        }
        if constexpr (hz >= 0) hf[hz] = __builtin_amdgcn_raw_buffer_load_b128(wsr, hlv + (hz & 3) * 1024, hld + (hz >> 2) * 4096, SC1);
      };
      auto mid = [&]() AI {
        if (s == 4 && wid == 0) {
          asm volatile("s_waitcnt vmcnt(%1)" : "+v"(fh) : "n"(cnt_after(3, 6, 4)) : "memory");
          _Pragma("nounroll") for (unsigned sp = 0; !ALL(fh == nonce) && sp < SPIN_CAP; ++sp) { fh = flag_ld(ws, fho); PIN(fh); }
        }
        if constexpr (s == 0) vwait_bar<5 + cnt_after(0, -1, 0)>();
        else if constexpr (s == 1) vwait_bar<cnt_after(0, -1, 1)>();
        else if constexpr (s == 14) vwait_bar<0>(); else if constexpr (s < 14) vwait_bar<cnt_after(s - 1, 0, s)>();
        if (s == 1 && wid == 0) flag_st(ws, WS_FH + (mg * 16 + ng) * 128, nonce);
        if (s == 3 && wid == 0) asm volatile("buffer_load_dword %0, %1, %2, 0 offen sc1" : "+v"(fh) : "v"(fho), "s"(wsr) : "memory");
        if constexpr (s == 6) asm volatile("" :: "v"(junk));
        if constexpr (s == 13) rebase(L_WD + 65536);
        if constexpr (s == 14) { UNROLL for (int k = 23; k >= 8; --k) PIN(hf[k]); }
        if (s == 1) TS(20); if (s == 3) TS(21); if (s == 5) TS(22); if (s == 7) TS(23); if (s == 9) TS(24); if (s == 11) TS(25); if (s == 13) TS(26);
        __builtin_amdgcn_s_setprio(3);
      };
      auto dn0 = [&](int d) AI { if (d >= 0) { F0 = MFMA(dw0, hf[d], F0); PIN(F0); } if (d >= -1) rd_d0(d + 1); };
      auto dn1 = [&](int d) AI { if (d >= 0) { F1 = MFMA(dw1, hf[d], F1); PIN(F1); } if (d >= -1) rd_d1(d + 1); };
      STEP(s == 0, sa, sb, QB0, QB1, OB, OA,
           k0 = ld128(kb + sn); side(IC<0>{}); __builtin_amdgcn_s_setprio(2),
           v1 = cat(trd(vb0 + so + 256), trd(vb1 + so + 256)); side(IC<1>{}),
           k1 = ld128(kb + sn + 1024); side(IC<2>{}); __builtin_amdgcn_s_setprio(1),
           side(IC<3>{}), dn0(s - DN0), if (s == 15) dn1(8));
      STEP(false, sb, sa, QA0, QA1, OA, OB,
           __builtin_amdgcn_s_setprio(0); side(IC<4>{}),
           side(IC<5>{}),
           mid(); if (s == 15) send(OA, 0, 1),
           v0 = cat(trd(vb0 + sn), trd(vb1 + sn)); side(IC<7>{}); if (s == 15) send(OA, 1, 3), if (s < 15) dn1(s - DN0); else dn0(9), if (s == 15) dn1(9));
    });
    pb[2] = pk(e0, e1); pb[3] = pk(e2, e3);
    OB = MFMA(v1, pb, OB);
    __builtin_amdgcn_s_setprio(0);
    TS(11);
    send(OB, 1, 2);
    *(LAS float*)(kb + (49152 + wq * 1024)) = odd ? OB[0] : OB[4];
    {
    LANE_IDS();
    const bool padl = h != odd;
    const int qt = qg * 4 + wq * 2 + ft, lw = (wid & 3) | (odd ? 0 : 4);
    const lptr pp = lds + wid * 6144 + lane * 16, lp = lds + lw * 6144 + (odd ? 0 : 1536) + r * 16;
    const lptr lq = lds + 49152 + (lw >> 1) * 4096 + wq * 1024 + (odd ? 0 : 512) + r * 16;
    f32x4 o0 = {OB[0], OB[1], OB[2], OB[3]}, o1 = {OB[4], OB[5], OB[6], OB[7]};
    lbar();
    const float l0 = *(LAS float*)lq, l1 = *(LAS float*)lp, l2 = *(LAS float*)(lp + 2048), l3 = *(LAS float*)(lp + 4096);
    f32x4 t[6];
    UNROLL for (int k = 0; k < 6; ++k) t[k] = *(LAS f32x4*)(pp + k * 1024);
    SBAR();
    float l = (l0 + l1) + (l2 + l3);
    UNROLL for (int k = 0; k < 3; ++k) { o0 += t[2 * k]; o1 += t[2 * k + 1]; }
    if (__builtin_expect(ANY(!(l <= 1.8446744e19f && l >= 5.4210109e-20f)), 0)) {
      const int qo = WS_QKV + head * QH + qt * QT + lane * 16;
      u32x4 q0 = __builtin_amdgcn_raw_buffer_load_b128(wsr, qo, 0, SC1), q1 = __builtin_amdgcn_raw_buffer_load_b128(wsr, qo + 1024, 0, SC1);
      f32x16 O = Z; float m = -__builtin_inff();
      _Pragma("nounroll") for (int t = 0; t < 64; ++t) {
        const int ko = WS_QKV + head * QH + t * QT + 2048;
        f32x16 s = MFMA(__builtin_amdgcn_raw_buffer_load_b128(wsr, ko + lane * 16, 0, SC1), q0, Z);
        s = MFMA(__builtin_amdgcn_raw_buffer_load_b128(wsr, ko + 1024 + lane * 16, 0, SC1), q1, s);
        float mn = m;
        UNROLL for (int i = 0; i < 16; ++i) mn = __builtin_fmaxf(mn, s[i]);
        mn = max32(mn);
        const float f = EX(m - mn); m = mn;
        UNROLL for (int i = 0; i < 16; ++i) O[i] *= f;
        UNROLL for (int st = 0; st < 2; ++st) {
          unsigned vv[8];
          UNROLL for (int j = 0; j < 8; ++j) vv[j] = __builtin_amdgcn_raw_buffer_load_b16(wsr, ko + 2048 + (r >> 3) * 512 + (((16 * st + 8 * (j >> 2) + 4 * h + (j & 3)) ^ (4 * (r >> 3))) << 4) + (r & 7) * 2, 0, SC1) & 0xffffu;
          O = MFMA(((u32x4){vv[0] | vv[1] << 16, vv[2] | vv[3] << 16, vv[4] | vv[5] << 16, vv[6] | vv[7] << 16}),
                   ((u32x4){pk(EX(s[8 * st] - m), EX(s[8 * st + 1] - m)), pk(EX(s[8 * st + 2] - m), EX(s[8 * st + 3] - m)),
                            pk(EX(s[8 * st + 4] - m), EX(s[8 * st + 5] - m)), pk(EX(s[8 * st + 6] - m), EX(s[8 * st + 7] - m))}), O);
        }
      }
      const float lc = odd ? O[0] : O[12], lx = swap32(lc, h); l = padl ? lc : lx;
      o0 = fs ? (f32x4){O[8], O[9], O[10], O[11]} : (f32x4){O[0], O[1], O[2], O[3]};
      o1 = fs ? (f32x4){O[12], O[13], O[14], O[15]} : (f32x4){O[4], O[5], O[6], O[7]};
    }
    const float ri = __builtin_amdgcn_rcpf(l);
    const float r0 = padl && odd && fs == 0 ? 0.f : ri, r1 = padl && !odd && fs == 1 ? 0.f : ri;
    st128((u32x4){pk(o0[0] * r0, o0[1] * r0), pk(o0[2] * r0, o0[3] * r0), pk(o1[0] * r1, o1[1] * r1), pk(o1[2] * r1, o1[3] * r1)},
                                           wsr, lane * 16, WS_O + (qt * 16 + head) * 2048 + fs * 1024, SC1);
    }
  }
  TS(12);
  if (idx >= 28) { wait_bar<0>();
    if (wid == 0) {
      flag_st(ws, WS_FO + (qg * 16 + head) * 128, nonce);
      if (b == 255) {
        LANE_IDS();
        _Pragma("nounroll") for (unsigned sp = 0; sp < SPIN_CAP; ++sp) {
          unsigned d = 0;
          UNROLL for (int i = 0; i < 4; ++i) d |= flag_ld(ws, WS_FQ + lane * 512 + i * 128) ^ nonce;
          if (ALL(d == 0)) break;
        }
        flag_st(ws, WS_E, nonce);
      }
    }
    return;
  }
  {
    lbar();
    const srd_t sO = SRD(wo, 448 * 896); const unsigned wov = vo_d + (wid * 56 - w3k * 448 - w3m * 32) * 896, wol = lds0 + wid * 7168;
    auto dma_o = [&](auto ic) AI { constexpr int i = decltype(ic)::v; dma<i * 8 * 896, i * 1024>(sO, wov, wol); };
    sfor<0, 4>(dma_o);
    u32x4 ew0, ew1;
    auto down2 = [&](int d, bool more) AI {
      const int o = dslot((d + 1) >> 2) + ((d + 1) & 3) * 2048 - dbo;
      ew0 = cat(trd(lds + db0 + o), trd(lds + db0 + o + 1024)); ew1 = cat(trd(lds + db1 + o), trd(lds + db1 + o + 1024));
      if (d == 10 || d == 22) rebase(L_WD);
      SBAR(); F0 = MFMA(dw0, hf[d], F0); F1 = MFMA(dw1, hf[d], F1); PIN(F0); PIN(F1); SBAR();
      if (more) { rd_d0(d + 2); rd_d1(d + 2); }
      SBAR(); F0 = MFMA(ew0, hf[d + 1], F0); F1 = MFMA(ew1, hf[d + 1], F1); PIN(F0); PIN(F1); SBAR();
    };
    static_assert(DN0 == 7, "units 0..9 ran beside the attention");
    down2(10, true); sfor<4, 7>(dma_o);
    vwait_bar<7>();
    if (wid == 0) flag_st(ws, WS_FO + (qg * 16 + head) * 128, nonce);
    TS(13);
    sfor<20, 22>(dma_d); down2(12, true); sfor<22, 24>(dma_d); down2(14, true);
    vwait_bar<4>();
    LANE_IDS();
    const int mt = mg3 * 2 + w3m, orot = ng3 & 1;
    unsigned fo = ~nonce; const int foo = WS_FO + ((mg3 >> 1) * 16 + (lane & 15)) * 128;
    sfor<24, 26>(dma_d); down2(16, true); sfor<26, 28>(dma_d);
    if (wid == 0) asm volatile("buffer_load_dword %0, %1, %2, 0 offen sc1" : "+v"(fo) : "v"(foo), "s"(wsr) : "memory");
    down2(18, false);
    u32x4 wo0[8], wo1[8];
    UNROLL for (int k = 0; k < 8; ++k) {
      const int wbo = w3k * (PART - 8192) - L_WD + ((k >> 2) ^ orot) * 7168, o = (((k >> 1) & 1) * 24 + (k & 1) * 16) * 128;
      const lptr wb0 = lds + db0 + wbo, wb1 = lds + db1 + wbo;
      wo0[k] = cat(trd(wb0 + o), trd(wb0 + o + 1024)); wo1[k] = cat(trd(wb1 + o), trd(wb1 + o + 1024));
    }
    rebase(L_WD + 65536);
    TS(14);
    if (wid == 0) {
      asm volatile("s_waitcnt vmcnt(0)" : "+v"(fo) :: "memory");
      _Pragma("nounroll") for (unsigned sp = 0; !ALL(fo == nonce) && sp < SPIN_CAP; ++sp) { fo = flag_ld(ws, foo); PIN(fo); }
    }
    wait_bar<0>();
    UNROLL for (int k = 27; k >= 24; --k) PIN(hf[k]);
    TS(15);
    u32x4 of[8];
    const int oso = WS_O + (mt * 16 + w3k * 4) * 2048 + orot * 4096;
    UNROLL for (int k = 0; k < 4; ++k) of[k] = __builtin_amdgcn_raw_buffer_load_b128(wsr, lane * 16 + k * 1024, oso, SC1);
    SBAR();
    rd_d0(20); rd_d1(20); down2(20, true); down2(22, true);
    UNROLL for (int k = 4; k < 8; ++k) of[k] = __builtin_amdgcn_raw_buffer_load_b128(wsr, lane * 16 + (k & 3) * 1024, oso ^ 4096, SC1);
    const unsigned xo = (unsigned)((mt * 32 + w3k * 8 + rr) * 896 + ng3 * 128 + cc * 16);
    const u32x4 xv = *(const u32x4*)(x + xo);
    SBAR();
    down2(24, true); down2(26, false);
    auto xch_send = [&](const f32x16& a0, const f32x16& a1, int base) AI {
      UNROLL for (int c = 0; c < 8; ++c) { const f32x16& a = c < 4 ? a0 : a1; const int o = (c & 3) * 4;
        *(LAS f32x4*)(lds + (base + wid * 8192 + c * 1024) + ((lane ^ (2 * c)) << 4)) = (f32x4){a[o], a[o + 1], a[o + 2], a[o + 3]}; } };
    int xro = w3m * 8192 + cc * 1024 + (((w3k * 8 + rr) ^ (2 * cc)) << 4); PIN(xro);
    xch_send(F0, F1, 0);
    f32x16 S0 = Z, S1 = Z;
    UNROLL for (int k = 0; k < 4; ++k) { S0 = MFMA(wo0[k], of[k], S0); S1 = MFMA(wo1[k], of[k], S1); }
    PIN(S0); PIN(S1);
    lbar();
    f32x4 g0 = *(LAS f32x4*)(lds + xro), g1 = *(LAS f32x4*)(lds + xro + 512);
    UNROLL for (int k = 1; k < 4; ++k) { g0 += *(LAS f32x4*)(lds + xro + k * 16384); g1 += *(LAS f32x4*)(lds + xro + k * 16384 + 512); }
    UNROLL for (int k = 4; k < 8; ++k) { S0 = MFMA(wo0[k], of[k], S0); S1 = MFMA(wo1[k], of[k], S1); }
    UNROLL for (int i = 0; i < 4; ++i) {
      g0[i] = g0[i] * __builtin_amdgcn_rcpf(1.f + EX(g0[i] * -1.4426950408889634f)); g1[i] = g1[i] * __builtin_amdgcn_rcpf(1.f + EX(g1[i] * -1.4426950408889634f)); }
    TS(16);
    xch_send(S0, S1, L_WD);
    f32x4 y0 = {U2F(xv[0] << 16), U2F(xv[0] & 0xffff0000u), U2F(xv[1] << 16), U2F(xv[1] & 0xffff0000u)}, y1 = {U2F(xv[2] << 16), U2F(xv[2] & 0xffff0000u), U2F(xv[3] << 16), U2F(xv[3] & 0xffff0000u)};
    xro += L_WD;
    PIN(g0); PIN(g1); PIN(y0); PIN(y1); PIN(xro);
    lbar();
    f32x4 t[8];
    UNROLL for (int k = 0; k < 8; ++k) t[k] = *(LAS f32x4*)(lds + xro + (k >> 1) * 16384 + (k & 1) * 512);
    SBAR();
    UNROLL for (int k = 0; k < 4; ++k) UNROLL for (int i = 0; i < 4; ++i) { y0[i] = __builtin_fmaf(t[2 * k][i], g0[i], y0[i]); y1[i] = __builtin_fmaf(t[2 * k + 1][i], g1[i], y1[i]); }
    __builtin_amdgcn_raw_buffer_store_b128((u32x4){pk(y0[0], y0[1]), pk(y0[2], y0[3]), pk(y1[0], y1[1]), pk(y1[2], y1[3])}, SRD(out, 2048 * 896), xo, 0, SC1);
    TS(17); TS_OUT();
  }
}

#if defined(__HIP_DEVICE_COMPILE__)
#define NOPK __attribute__((target("no-packed-fp32-ops")))
#else
#define NOPK
#endif
__global__ __launch_bounds__(512, 2) NOPK void fused_kernel(const char* x, const char* wqkv, const char* wo, const char* wg, const char* wu, const char* wd,
                                                       char* ws, char* out) {
  extern __shared__ __attribute__((aligned(16))) char smem[];
  const lptr lds = (lptr)smem;
  const unsigned lds0 = (unsigned)(uintptr_t)lds;
  const int tid = __builtin_amdgcn_workitem_id_x(), lane = tid & 63, wid = __builtin_amdgcn_readfirstlane(tid >> 6);
  const int rr = lane >> 3, cc = lane & 7;
  const int b = __builtin_amdgcn_workgroup_id_x(), xcd = b & 7, idx = b >> 3;
  const int ng = NG_OF(xcd, idx), mg = MG_OF(xcd, idx);
  {
    unsigned long long pc; asm volatile("s_getpc_b64 %0" : "=s"(pc));
    touch(SRD(pc & ~127ull, 28672), (idx * 8 + (tid >> 6)) * 128, lds0 + L_PF);
  }
  unsigned tsv = 0; TSR(62); TS(0);
  const int rot0 = MAP == 2 ? (mg & 3) * 2 + ((mg >> 2) & 1) : (mg + ROTX * (mg >> 3)) & 7, rot = rot0 == 7 ? 3 : rot0;
  unsigned kc[7];
  UNROLL for (int c = 0; c < 7; ++c) kc[c] = (unsigned)((c + rot >= 7 ? c + rot - 7 : c + rot) * 64);
  const srd_t xr = SRD(x, 2048 * 896);
  const unsigned xv0 = (unsigned)((mg * 128 + wid * 16 + rr) * 896 + ((cc ^ rr) << 4));
  const unsigned xv1 = (unsigned)((mg * 128 + wid * 16 + 8 + (rr ^ 1)) * 896 + ((cc ^ rr ^ 1) << 4));
  const unsigned lw2k = lds0 + wid * 2048;
  auto dma_x = [&](auto cc_) AI { constexpr int c = decltype(cc_)::v; dma2<(c & 3) * 16384>(xr, xv0, xv1, lw2k, kc[c], 2); };
  const srd_t wsr = SRD(ws, WS_END);
  u32x4 srow; unsigned nonce;
  const int srw = mg * 128 + ng * 8 + wid;
  asm volatile("buffer_load_dwordx4 %0, %1, %2, %3 offen" : "=&v"(srow) : "v"(lane * 16), "s"(SRD(x, srw * 896 + 896)), "s"(srw * 896) : "memory");
  dma_x(IC<0>{});
  SBAR();
  const int r = lane & 31, h = lane >> 5, q = (lane & 15) >> 2, p = lane & 3, grp = (lane >> 4) & 1;
  const f32x16 Z = {};
  const int head = 2 * xcd + (idx & 1), qg = idx >> 1;
  auto dma_kv = [&](auto zc, unsigned kvv) AI { constexpr int z = decltype(zc)::v; dma<(z >> 1) * QT + (z & 1) * 1024, ((z >> 1) & 3) * 16384 + (z & 1) * 1024, true>(wsr, kvv, lw2k); };
  u32x4 QA0, QA1, QB0, QB1, kf0, kf1, ka0, ka1, va0; f32x16 s0; lptr kb, vb0, vb1; unsigned kvv = 0;
  {
    const int wm = wid & 3, wn = wid >> 2;
    const bool bq = ng <= 10;
    const unsigned st1 = bq ? 2688 : 3584;
    unsigned va = (16 * wm + rr) * st1 + (((cc << 4) ^ (grp << (bq ? 6 : 5))) ^ (bq ? 0 : wn << 6)) + (bq ? 2 * ng + wn : ng + 12) * 128, vb = va + 8 * st1;
    const char* wgu = wn ? wu : wg;
    const srd_t s2 = SRD(wgu, 448 * 3584), s1 = SRD(bq ? wqkv : wgu, 448 * st1);
    auto dma_w = [&](auto mc) AI {
      constexpr int m = decltype(mc)::v;
      if constexpr (m == 7) { va = (unsigned)((16 * wm + rr) * 3584 + ((cc ^ (grp << 1) ^ (wn << 2)) << 4)) + ng * 128; vb = va + 8 * 3584; PIN(va); PIN(vb); }
      if constexpr (m == 14) {
        va = (unsigned)((16 * wm + (lane >> 2)) * 3584 + (((lane & 3) ^ (wn << 1)) << 4)) + 2048 + ng * 64;
        if (ng >= 14) va = OOB;
        PIN(va);
      }
      if constexpr (m < 7) dma2<wpos(m)>(s1, va, vb, lw2k, kc[m], st1);
      else if constexpr (m < 14) dma2<wpos(m)>(s2, va, vb, lw2k, kc[m - 7], 3584);
      else dma1<wpos(m)>(s2, va, lds0 + wid * 1024, kc[m - 14], 3584);
    };
    dma_w(IC<0>{});
    unsigned epoch;
    asm volatile("buffer_load_dword %0, off, %1, 0 sc1" : "=&v"(epoch) : "s"(wsr) : "memory");
    dma_x(IC<1>{});
    SBAR();
    const int rowb = (8 * h + q) * 128 + p * 8;
    int b20 = grp * 8192 + rowb + (((2 * wn) ^ (q >> 1) ^ (grp << 1)) << 5);
    int b10 = bq ? wn * 8192 + rowb + ((grp ^ ((q >> 1) << 1)) << 5) : b20, b11 = b10 ^ (bq ? 64 : 32);
    int xb[4];
    UNROLL for (int i = 0; i < 4; ++i) { xb[i] = (4 * wm + (r >> 3)) * 1024 + ((r & 7) ^ ((r >> 3) & 1)) * 128 + (((2 * i + h) ^ (r & 7)) << 4); PIN(xb[i]); }
    PIN(b10); PIN(b11); PIN(b20);
    UNROLL for (int c = 2; c < 7; ++c) asm volatile("" : "+s"(kc[c]));
    SBAR();
    u32x4 xf[28];
    auto row_stats = [&]() AI {
      float a = dot2(srow[0], ONES2, 0.f), c = dot2(srow[0], srow[0], 0.f);
      UNROLL for (int i = 1; i < 4; ++i) { a = dot2(srow[i], ONES2, a); c = dot2(srow[i], srow[i], c); }
      a = wave_sum(a); c = wave_sum(c);
      const float mean = a * (1.f / 448.f), var = __builtin_fmaxf(c * (1.f / 448.f) - mean * mean, 0.f);
      unsigned g = pk(-mean, __builtin_amdgcn_sqrtf(rbf(rbf(var) + 1e-5f))); PIN(g); SBAR();
      asm volatile("s_waitcnt vmcnt(4)" : "+v"(epoch) :: "memory");
      nonce = __builtin_amdgcn_readfirstlane(epoch) * 0x9E3779B1u + 0x7F4A7C15u;
      if (lane == 63) __hip_atomic_store((GAS unsigned long long*)(ws + WS_ST + srw * 8), (unsigned long long)nonce << 32 | g, RLX_AGENT);
    };
    auto rd_x = [&](int k0, int k1) AI { UNROLL for (int ks = k0; ks < k1; ++ks) xf[ks] = ld128(lds + xb[ks & 3] + ((ks >> 2) & 3) * 16384); };
    u32x2 wf[NWF][4];
    int ra0 = 0, ra1 = 0;
    auto rd_w = [&](int n) AI {
      if (n < 56) {
        const int m = n >> 2, o = (n & 3) * 2048;
        if ((n & 3) == 0) { ra0 = (m < 7 ? b10 : b20) + wpos(m); ra1 = (m < 7 ? b11 : b20 ^ 32) + wpos(m); PIN(ra0); PIN(ra1); }
        wf[n % NWF][0] = trd(lds + ra0 + o); wf[n % NWF][1] = trd(lds + ra0 + o + 512); wf[n % NWF][2] = trd(lds + ra1 + o); wf[n % NWF][3] = trd(lds + ra1 + o + 512);
      } else {
        const int o = ((n - 56) >> 1) * 8192 + (n & 1) * 2048;
        if (n == 56) { ra0 = wpos(14) + grp * 4096 + (8 * h + q) * 64 + ((wn ^ grp) << 5) + p * 8; PIN(ra0); }
        wf[n % NWF][0] = trd(lds + ra0 + o); wf[n % NWF][1] = trd(lds + ra0 + o + 256); wf[n % NWF][2] = trd(lds + ra0 + o + 1024); wf[n % NWF][3] = trd(lds + ra0 + o + 1280);
      }
    };
    SBAR();
    dma_w(IC<1>{});
    asm volatile("s_waitcnt vmcnt(9)" : "+v"(srow) :: "memory");
    row_stats();
    SBAR();
    lbar();
    TS(1);
    UNROLL for (int n = 0; n < RA; ++n) rd_w(n);
    rd_x(0, 4);
    dma_x(IC<2>{});
    f32x16 A0 = Z, A1 = Z, B0 = Z, B1 = Z, C0 = Z;
    float cs0 = 0.f, cs1 = 0.f, rstd = 0.f, sig = 0.f, kg = 0.f; unsigned mm = 0; u32x2 sg = {0u, ~nonce};
    const int mt = mg * 4 + wm;
    const unsigned csel = ((wm & 1) ? 0x05040c0cu : 0x01000c0cu) + ((wm >> 1) ? 0x02020000u : 0u);
    auto colsum = [&](int n, int hh) AI {
      cs0 += U2F(__builtin_amdgcn_perm(wf[n % NWF][2 + hh][0], wf[n % NWF][hh][0], csel)); cs1 += U2F(__builtin_amdgcn_perm(wf[n % NWF][2 + hh][1], wf[n % NWF][hh][1], csel));
      PIN(cs0); PIN(cs1);
    };
    auto cs_out = [&]() AI {
      const float cs = cs0 + cs1, hi = rbf(cs); *(LAS unsigned*)(lds + L_CS + wn * 1024 + lane * 16 + ((wm & 1) * 2 + (wm >> 1)) * 4) = pk(hi, cs - hi); cs0 = 0.f; cs1 = 0.f; };
    auto fix2 = [&](f32x16& a0, f32x16& a1) AI {
      const u32x4 c = ld128(lds + L_CS + wn * 1024 + lane * 16);
      a0 = MFMA(c, ((u32x4){mm, mm, 0u, 0u}), a0); a1 = MFMA(c, ((u32x4){0u, 0u, mm, mm}), a1); PIN(a0); PIN(a1); };
    auto glu = [&](const f32x16& acc, int i) AI { return acc[i + 8] * __builtin_amdgcn_rcpf(__builtin_fmaf(EX(acc[i] * kg), sig, sig)); };
    auto glu8 = [&](const f32x16& acc) AI { return (u32x4){pk(glu(acc, 0), glu(acc, 1)), pk(glu(acc, 2), glu(acc, 3)), pk(glu(acc, 4), glu(acc, 5)), pk(glu(acc, 6), glu(acc, 7))}; };
    const int hs2 = WS_H + (mt * 112 + ng * 4 + wn * 2) * 1024;
    u32x4 tb0 = {0u, 0u, 0u, 0u}, tb1 = tb0;
    auto epi1 = [&](const f32x16& acc, int t) AI {
      if (bq) {
        const u32x4 e = t ? tb1 : tb0; const unsigned o[3] = {e[0] ^ (r << 4), e[1] ^ (r << 4), e[2] ^ (r << 4)};
        const unsigned m[2] = {(unsigned)__builtin_amdgcn_sbfe(e[3], 0, 1), (unsigned)__builtin_amdgcn_sbfe(e[3], 1, 1)};
        const float sc = 2 * ng + wn < 7 ? rstd * (0.18898223650461362f * 1.4426950408889634f) : rstd;
        u32x4 v[2];
        UNROLL for (int pp = 0; pp < 2; ++pp)
          v[pp] = widen((u32x2){pk(acc[8 * pp] * sc, acc[8 * pp + 1] * sc), pk(acc[8 * pp + 2] * sc, acc[8 * pp + 3] * sc)},
                        (u32x2){pk(acc[8 * pp + 4] * sc, acc[8 * pp + 5] * sc), pk(acc[8 * pp + 6] * sc, acc[8 * pp + 7] * sc)});
        u32x4 d0 = {v[0][0], v[0][1], bfi(m[0], PAD0, v[0][2]), ~m[0] & v[0][3]}, d1 = {v[1][0], v[1][1], bfi(m[1], PAD0, v[1][2]), ~m[1] & v[1][3]};
        u32x4 d2 = {PAD0, 0u, bfi(m[0], v[0][2], v[1][2]), bfi(m[0], v[0][3], v[1][3])};
        PIN(d0); PIN(d1); PIN(d2); SBAR();
        __builtin_amdgcn_raw_buffer_store_b128(d0, wsr, o[0], mt * QT, SC1); __builtin_amdgcn_raw_buffer_store_b128(d1, wsr, o[1], mt * QT, SC1);
        st128(d2, wsr, o[2], mt * QT, SC1);
      } else st128(glu8(acc), wsr, lane * 16 + t * 1024, hs2 + 12 * 4096, SC1);
    };
    u32x4 hp, hq; unsigned fl0 = ~nonce; f32x16 sa, sb; u32x4 cfix;
    int fqo = 0;
    auto fq_off = [&]() AI {
      const int c0 = (lane < 8 ? 448 + (wid & 1) * 448 : 0) + head * 28 - (head & 1) * 4 + (lane & 1) * 31, g = c0 >> 7, m2 = lane < 8 ? (wid >> 1) * 4 + (lane >> 1) : qg;
      fqo = WS_FQ + (m2 * 16 + g) * 128; };
    const int sgo = WS_ST + (mg * 128 + wm * 32 + r) * 8;

    sfor<0, 70>([&](auto nc) AI {
      constexpr int n = decltype(nc)::v, u = n & 7, ul = n > 26 && n <= 46 ? (n + 4) & 7 : u;
      constexpr int k0 = n < 28 ? n : n < 56 ? n - 28 : 2 * (n - 56), k1 = n < 56 ? k0 : k0 + 1;
      f32x16& D0 = n < 28 ? A0 : n < 56 ? B0 : C0; f32x16& D1 = n < 28 ? A1 : n < 56 ? B1 : C0;
      constexpr bool early = n == 26 || n == 27 || n == 54 || n == 55 || n >= 68;
      PIN(wf[n % NWF][3]);
      if (ul == 7) __builtin_amdgcn_s_setprio(3); if (ul == 1) __builtin_amdgcn_s_setprio(2); if (ul == 3) __builtin_amdgcn_s_setprio(1); if (ul == 5) __builtin_amdgcn_s_setprio(0);
      SBAR();
      D0 = MFMA(cat(wf[n % NWF][0], wf[n % NWF][1]), xf[k0], D0); PIN(D0);
      SBAR();
      constexpr bool bar = n == 2 || n == 6 || n == 14 || n == 22 || n == 26 || n == 34 || n == 42 || n == PUB || n == 54 || n == 62 || n == 68;
      if constexpr (bar) {
        if constexpr (n == 26 || n == 54 || n == 68) { UNROLL for (int e = 0; e < RA; ++e) { colsum(n + e, 0); colsum(n + e, 1); } cs_out(); }
        if (n == 2) wait_bar<7>(); else if (n <= 14) wait_bar<2>(); else if (n == 22) wait_bar<9>(); else if (n == 26) wait_bar<6>();
        else if (n == 34) { if (bq) wait_bar<12>(); else wait_bar<8>(); } else if (n == 42) { if (bq) wait_bar<6>(); else wait_bar<2>(); }
        else if (n == PUB) vwait_bar<0>(); else if (n == 54) wait_bar<4>();
        else if (n == 62) lbar(); else wait_bar<4>();
        if (n == PUB && wid == 0) flag_st(ws, WS_FQ + (mg * 16 + ng) * 128, nonce);
        if (n == 6) TS(2); if (n == 14) TS(3); if (n == 26) TS(4); if (n == 42) TS(5); if (n == 62) TS(6); if (n == 68) TS(7);
        SBAR();
      }
      constexpr bool d1f = bar;
      if constexpr (d1f) { D1 = MFMA(cat(wf[n % NWF][2], wf[n % NWF][3]), xf[k1], D1); PIN(D1); SBAR(); }
      if constexpr (n + RA < 70) rd_w(n + RA);
      if constexpr (n == 68) {
        const lptr qb = lds + L_Q + (wid & 1) * 4096 + ((wid >> 1) & 1) * 2048 + lane * 16, qa = lds + L_Q + (wid & 1) * 4096 + (((wid >> 1) & 1) ^ 1) * 2048 + lane * 16;
        kf0 = ld128(lds + lane * 16); QA0 = ld128(qa); QB0 = ld128(qb); kf1 = ld128(lds + 1024 + lane * 16); QA1 = ld128(qa + 1024); QB1 = ld128(qb + 1024);
        cfix = ld128(lds + L_CS + wn * 1024 + lane * 16);
      }
      if constexpr (n == 69) { sa = MFMA(kf1, QA1, sa); sb = MFMA(kf1, QB1, sb); PIN(sa); PIN(sb); }
      if constexpr (n == 2) rd_x(4, 8);
      if constexpr (u == 6 && n < 24) rd_x(n + 2, n + 6);
      if constexpr (u == 0 && n > 0 && n < 24) rd_x(n + 4, n + 8);
      if constexpr (!early) colsum(n, 0);
      SBAR();
      if constexpr (!d1f) { D1 = MFMA(cat(wf[n % NWF][2], wf[n % NWF][3]), xf[k1], D1); PIN(D1); }
      SBAR();
      if constexpr (!early) colsum(n, 1);
      if constexpr (n == 0) dma_w(IC<2>{}); if constexpr (n == 1) dma_x(IC<3>{}); if constexpr (n == 2) dma_w(IC<3>{}); if constexpr (n == 4) dma_w(IC<4>{});
      if constexpr (n == 6) dma_x(IC<4>{}); if constexpr (n == 7) dma_x(IC<5>{}); if constexpr (n == 8) dma_w(IC<5>{}); if constexpr (n == 10) dma_w(IC<6>{});
      if constexpr (n == 14) dma_x(IC<6>{}); if constexpr (n == 15) dma_w(IC<7>{}); if constexpr (n == 18) dma_w(IC<8>{}); if constexpr (n == 20) dma_w(IC<9>{});
      if constexpr (n == 23) dma_w(IC<10>{}); if constexpr (n == 25) dma_w(IC<11>{}); if constexpr (n == 26) dma_w(IC<12>{}); if constexpr (n == 27) dma_w(IC<13>{});
      if constexpr (n == 53) touch(SRD(b < 196 ? wd : wo, b < 196 ? 1792 * 896 : 448 * 896), lane < 8 ? (((b < 196 ? b : b - 196) * 8 + wid) * 8 + lane) * 128 : OOB, lds0 + L_PF + wid * 256);
      if constexpr (n == 17) asm volatile("buffer_load_dwordx2 %0, %1, %2, 0 offen sc1" : "+v"(sg) : "v"(sgo), "s"(wsr) : "memory");
      if constexpr (n == 16) asm volatile("global_load_dwordx4 %0, %2, %3\n\tglobal_load_dwordx4 %1, %2, %3 offset:32" : "+v"(tb0), "+v"(tb1) : "v"(h * 16), "s"(&qkv_tab.v[bq ? 4 * ng + 2 * wn : 0]) : "memory");
      if constexpr (n >= PUB && n < PUB + 7) dma_w(IC<14 + n - PUB>{});
      if constexpr (n == 26) {
        PIN(sg); PIN(tb0); PIN(tb1);
        _Pragma("nounroll") for (unsigned sp = 0; !ALL(sg[1] == nonce) && sp < SPIN_CAP; ++sp)
          asm volatile("buffer_load_dwordx2 %0, %1, %2, 0 offen sc1\n\ts_waitcnt vmcnt(0)" : "+v"(sg) : "v"(sgo), "s"(wsr) : "memory");
        sig = U2F(sg[0] & 0xffff0000u); rstd = __builtin_amdgcn_rcpf(sig); mm = __builtin_amdgcn_perm(sg[0], sg[0], 0x01000100u); kg = -1.4426950408889634f * rstd;
        PIN(sig); PIN(rstd); PIN(kg); PIN(mm);
      }
      if constexpr (n == 27) fix2(A0, A1);
      if constexpr (n == 28) epi1(A0, 0);
      if constexpr (n == 31) epi1(A1, 1);
      if constexpr (n == FQP) { fq_off(); asm volatile("buffer_load_dword %0, %1, %2, 0 offen sc1" : "+v"(fl0) : "v"(fqo), "s"(wsr) : "memory"); }
      if constexpr (n == 55) fix2(B0, B1);
      if constexpr (n == 57) { hp = glu8(B0); PIN(hp); }
      if constexpr (n == 64) { hq = glu8(B1); PIN(hq); }
      if constexpr (n == 69) { st128(hp, wsr, lane * 16, hs2, SC1); st128(hq, wsr, lane * 16 + 1024, hs2, SC1); }
      if constexpr (n == FQC) {
        asm volatile("s_waitcnt vmcnt(0)" : "+v"(fl0) :: "memory");
        _Pragma("nounroll") for (unsigned sp = 0; !ALL(fl0 == nonce) && sp < SPIN_CAP; ++sp)
          asm volatile("buffer_load_dword %0, %1, %2, 0 offen sc1\n\ts_waitcnt vmcnt(0)" : "+v"(fl0) : "v"(fqo), "s"(wsr) : "memory");
        kvv = (unsigned)(WS_QKV + head * QH + (wid >> 1) * 16 * QT + 2048 + (wid & 1) * 2048 + lane * 16);
        dma<0, L_Q, true>(wsr, WS_QKV + head * QH + (qg * 4 + (wid >> 1)) * QT + (wid & 1) * 1024 + lane * 16, lds0 + wid * 1024); dma_kv(IC<0>{}, kvv); }
      if constexpr (n > FQC && n < FQC + 8) dma_kv(IC<n - FQC>{}, kvv);
      if constexpr (n == 67) {
        const int vc = 2 * (grp ^ wn) + (p >> 1);
        kb = lds + (wid >> 1) * 4096 + lane * 16;
        vb0 = lds + (wid >> 1) * 4096 + 2048 + vc * 512 + ((((4 * h) ^ (4 * vc)) + q) << 4) + (p & 1) * 8;
        vb1 = lds + (wid >> 1) * 4096 + 2048 + vc * 512 + ((((4 * h + 8) ^ (4 * vc)) + q) << 4) + (p & 1) * 8;
        PIN(kb); PIN(vb0); PIN(vb1);
      }
      if constexpr (n == 68) { sa = MFMA(kf0, QA0, Z); sb = MFMA(kf0, QB0, Z); PIN(sa); PIN(sb); ka0 = ld128(kb); ka1 = ld128(kb + 1024); va0 = cat(trd(vb0), trd(vb1)); }
    });
    SBAR();
    C0 = MFMA(cfix, ((u32x4){mm, mm, mm, mm}), C0);
    float ma = sa[0], mb = sb[0];
    UNROLL for (int i = 1; i < 16; ++i) { ma = __builtin_fmaxf(ma, sa[i]); mb = __builtin_fmaxf(mb, sb[i]); }
    const unsigned pa = pk(1.f - max32(ma), 0.f), pb = pk(1.f - max32(mb), 0.f);
    const bool p0 = (head & 1) && h == 0, p1 = !(head & 1) && h == 1;
    QA0[0] = p0 ? pa : QA0[0]; QA1[2] = p1 ? pa : QA1[2]; QB0[0] = p0 ? pb : QB0[0]; QB1[2] = p1 ? pb : QB1[2];
    s0 = MFMA(ka0, QA0, Z); s0 = MFMA(ka1, QA1, s0);
    PIN(s0); PIN(QB0); PIN(QB1);
    SBAR();
    st128(glu8(C0), wsr, ng < 14 ? lane * 16 : OOB, WS_H + (mt * 112 + 64 + ng * 2 + wn) * 1024, SC1);
    SBAR();
    TS(8);
  }
  return phase23(x, wo, wd, ws, out, lds, nonce, QA0, QA1, QB0, QB1, ka0, ka1, va0, s0, kb, vb0, vb1, kvv, wid TSARG);
}

extern "C" void kernel_launch(void* const* d_in, const int* in_sizes, int n_in, void* d_out, int out_size, void* d_ws, size_t ws_size,
                              hipStream_t stream) {
  (void)hipFuncSetAttribute((const void*)fused_kernel, hipFuncAttributeMaxDynamicSharedMemorySize, LDS_BYTES);
  fused_kernel<<<256, 512, LDS_BYTES, stream>>>((const char*)d_in[0], (const char*)d_in[1], (const char*)d_in[2], (const char*)d_in[4], (const char*)d_in[5],
                                                (const char*)d_in[6], (char*)d_ws, (char*)d_out);
}
```

```cpp
#include <hip/hip_runtime.h>
#include <hip/hip_bf16.h>
#include <stdint.h>

typedef short bf16x8 __attribute__((ext_vector_type(8)));
typedef short s16x4 __attribute__((ext_vector_type(4)));
typedef float f32x16 __attribute__((ext_vector_type(16)));
typedef float f32x4 __attribute__((ext_vector_type(4)));
typedef float f32x2 __attribute__((ext_vector_type(2)));
typedef unsigned u32x4 __attribute__((ext_vector_type(4)));
typedef unsigned u32x2 __attribute__((ext_vector_type(2)));
typedef __bf16 bf2 __attribute__((ext_vector_type(2)));
typedef __amdgpu_buffer_rsrc_t srd_t;
#define LAS __attribute__((address_space(3)))
#define GAS __attribute__((address_space(1)))
typedef LAS char* lptr;
#define DI __device__ __forceinline__
#define AI __attribute__((always_inline))
#define MFMA(a, b, c) __builtin_amdgcn_mfma_f32_32x32x16_bf16(__builtin_bit_cast(bf16x8, (a)), __builtin_bit_cast(bf16x8, (b)), (c), 0, 0, 0)
#define PIN(x) asm volatile("" : "+v"(x))
#define SBAR() __builtin_amdgcn_sched_barrier(0)
#define RLX_AGENT __ATOMIC_RELAXED, __HIP_MEMORY_SCOPE_AGENT
#define EX(v) __builtin_amdgcn_exp2f(v)
#define F2U(x) __builtin_bit_cast(unsigned, (float)(x))
#define U2F(x) __builtin_bit_cast(float, (unsigned)(x))
#define ANY(p) (__builtin_amdgcn_ballot_w64(p) != 0)
#define ALL(p) (__builtin_amdgcn_ballot_w64(!(p)) == 0)
#define UNROLL _Pragma("unroll")
#define SRD(p, n) __builtin_amdgcn_make_buffer_rsrc((void*)(p), 0, (n), 0x00020000)

constexpr int WS_E = 0;
constexpr int WS_FQ = 4096;
constexpr int WS_FH = WS_FQ + 256 * 128;
constexpr int WS_FO = WS_FH + 256 * 128;
constexpr int WS_ST = WS_FO + 256 * 128;
constexpr int WS_CS = 1 << 17;
constexpr int WS_QKV = 1 << 20;
constexpr int QT = 6144, QH = 64 * QT;
constexpr int WS_O = WS_QKV + 16 * QH;
constexpr int WS_H = WS_O + (2 << 20);
constexpr int WS_END = WS_H + 64 * 112 * 1024;
constexpr int OOB = 1 << 30;
constexpr int SC1 = 16;
constexpr unsigned SPIN_CAP = 1u << 20;

template <int I> struct IC { static constexpr int v = I; };
template <int B, int E, class F> DI void sfor(F&& f) { if constexpr (B < E) { f(IC<B>{}); sfor<B + 1, E>(f); } }
template <int N> DI void wait_bar() { asm volatile("s_waitcnt vmcnt(%0) lgkmcnt(0)\n\ts_barrier" :: "n"(N) : "memory"); }
template <int N> DI void vwait_bar() { asm volatile("s_waitcnt vmcnt(%0)\n\ts_barrier" :: "n"(N) : "memory"); }
DI void lbar() { asm volatile("s_waitcnt lgkmcnt(0)\n\ts_barrier" ::: "memory"); }
DI unsigned pk(float a, float b) { f32x2 v = {a, b}; return __builtin_bit_cast(unsigned, __builtin_convertvector(v, bf2)); }
DI float rbf(float a) { return U2F(pk(a, a) & 0xffff0000u); }
DI float dot2(unsigned a, unsigned b, float c) { return __builtin_amdgcn_fdot2_f32_bf16(__builtin_bit_cast(bf2, a), __builtin_bit_cast(bf2, b), c, false); }
DI u32x2 trd(lptr p) { return __builtin_bit_cast(u32x2, __builtin_amdgcn_ds_read_tr16_b64_v4i16((LAS s16x4*)p)); }
DI u32x4 cat(u32x2 a, u32x2 b) { return (u32x4){a[0], a[1], b[0], b[1]}; }
DI u32x4 ld128(lptr p) { return *(LAS u32x4*)p; }
DI unsigned bfi(unsigned m, unsigned a, unsigned b) { return (m & a) | (~m & b); }
template <int G, int L, bool DEV = false> DI void dma(srd_t srd, unsigned voff, unsigned lbase) {
  unsigned t;
  if constexpr (DEV)
    asm volatile("s_add_u32 m0, %3, %5\n\ts_mov_b32 %0, %4\n\tbuffer_load_dwordx4 %1, %2, %0 offen sc1 lds" : "=&s"(t) : "v"(voff), "s"(srd), "s"(lbase), "n"(G), "n"(L) : "memory", "scc");
  else
    asm volatile("s_add_u32 m0, %3, %5\n\ts_mov_b32 %0, %4\n\tbuffer_load_dwordx4 %1, %2, %0 offen lds" : "=&s"(t) : "v"(voff), "s"(srd), "s"(lbase), "n"(G), "n"(L) : "memory", "scc");
}
template <int L> DI void dma2(srd_t srd, unsigned voff0, unsigned voff1, unsigned lbase, unsigned ga, unsigned gb) {
  unsigned t;
  asm volatile("s_add_u32 m0, %4, %5\n\ts_mul_i32 %0, %6, %7\n\tbuffer_load_dwordx4 %1, %3, %0 offen lds\n\ts_add_u32 m0, m0, 1024\n\ts_nop 0\n\tbuffer_load_dwordx4 %2, %3, %0 offen lds"
               : "=&s"(t) : "v"(voff0), "v"(voff1), "s"(srd), "s"(lbase), "n"(L), "s"(ga), "s"(gb) : "memory", "scc");
}
template <int L> DI void dma1(srd_t srd, unsigned voff, unsigned lbase, unsigned ga, unsigned gb) {
  unsigned t;
  asm volatile("s_add_u32 m0, %3, %4\n\ts_mul_i32 %0, %5, %6\n\tbuffer_load_dwordx4 %1, %2, %0 offen lds" : "=&s"(t) : "v"(voff), "s"(srd), "s"(lbase), "n"(L), "s"(ga), "s"(gb) : "memory", "scc");
}
DI void touch(srd_t srd, unsigned voff, unsigned ldst) { asm volatile("s_mov_b32 m0, %2\n\ts_nop 0\n\tbuffer_load_dword %0, %1, 0 offen lds" :: "v"(voff), "s"(srd), "s"(ldst) : "memory"); }
DI void st128(u32x4 v, srd_t srd, int voff, int soff, int) { __builtin_amdgcn_raw_buffer_store_b128(v, srd, voff, soff, SC1); SBAR(); asm volatile("s_nop 1"); SBAR(); }
DI float swap32(float v, int h) {
  auto r = __builtin_amdgcn_permlane32_swap(F2U(v), F2U(v), false, false);
  return U2F(h ? r[0] : r[1]);
}
DI float max32(float v) {
  auto r = __builtin_amdgcn_permlane32_swap(F2U(v), F2U(v), false, false);
  return __builtin_fmaxf(U2F(r[0]), U2F(r[1]));
}
DI u32x4 widen(u32x2 a, u32x2 b) {
  auto r0 = __builtin_amdgcn_permlane32_swap(a[0], b[0], false, false); auto r1 = __builtin_amdgcn_permlane32_swap(a[1], b[1], false, false);
  return (u32x4){r0[0], r1[0], r0[1], r1[1]};
}
DI float wave_sum(float v) {
  v += U2F(__builtin_amdgcn_update_dpp(0u, F2U(v), 0x111, 0xf, 0xf, true));
  v += U2F(__builtin_amdgcn_update_dpp(0u, F2U(v), 0x112, 0xf, 0xf, true));
  v += U2F(__builtin_amdgcn_update_dpp(0u, F2U(v), 0x114, 0xf, 0xf, true));
  v += U2F(__builtin_amdgcn_update_dpp(0u, F2U(v), 0x118, 0xf, 0xf, true));
  v += U2F(__builtin_amdgcn_update_dpp(0u, F2U(v), 0x142, 0xa, 0xf, true));
  v += U2F(__builtin_amdgcn_update_dpp(0u, F2U(v), 0x143, 0xc, 0xf, true));
  return v;
}
DI unsigned flag_ld(const char* ws, int off) { return __hip_atomic_load((GAS unsigned*)(ws + off), RLX_AGENT); }
DI void flag_st(char* ws, int off, unsigned v) {
  if (__builtin_amdgcn_mbcnt_hi(~0u, __builtin_amdgcn_mbcnt_lo(~0u, 0u)) == 0) __hip_atomic_store((GAS unsigned*)(ws + off), v, RLX_AGENT); }

constexpr int RA = 2, NWF = RA + 1;
constexpr int PART = 14336;
constexpr int L_W = 65536;
constexpr int L_CS = 9 * 16384;
constexpr int L_PF = L_CS + 8192;
constexpr int L_Q = 65536;
constexpr int wpos(int m) { return m < 9 ? L_W + (m % 5) * 16384 : m == 9 ? 49152 : m == 10 ? L_W : m < 14 ? (m - 11) * 16384 : L_W + 16384 + (m - 14) * 8192; }
constexpr int FQP = 52, FQC = 59;
constexpr int L_WD = 65536;
constexpr int LDS_BYTES = 163840;
constexpr unsigned ONES2 = 0x3f803f80u, PAD0 = 0x00003f80u;
static_assert(L_PF + 2048 <= LDS_BYTES && WS_ST + 2048 * 8 <= WS_CS && WS_CS + 96 * 8192 <= WS_QKV, "maps");

constexpr int DN0 = 7;
constexpr int kv_at(int s, int p) { return p == 0 && s > 0 && s + 3 < 16 ? (s + 3) * 2 + 1 : p == 7 && s + 4 < 16 ? (s + 4) * 2 : -1; }
constexpr int wd_at(int s, int p) {
  if (s >= 1 && s <= 3) return p == 1 ? s * 4 - 4 : p == 3 ? s * 4 - 3 : p == 4 ? s * 4 - 2 : p == 5 ? s * 4 - 1 : -1;
  for (int g = 0; g < 2; ++g) { if (s == 10 + 4 * g && p == 7) return 12 + 4 * g; if (s == 11 + 4 * g) return p == 0 ? 13 + 4 * g : p == 2 ? 14 + 4 * g : p == 5 ? 15 + 4 * g : -1; }
  return -1;
}
constexpr int h_at(int s, int p) { const int z = (s - 5) * 3 + (p == 1 ? 1 : p == 3 ? 2 : 3); if (s == 15) return p == 1 ? 24 : p == 3 ? 25 : p == 4 ? 26 : p == 7 ? 27 : -1;
  return s == 4 && p == 7 ? 0 : s >= 5 && (p == 1 || p == 3 || p == 4) && z < 24 ? z : -1; }
constexpr bool pf_at(int s, int p) { return s == 4 && p == 1; }
constexpr int n_at(int s, int p) { return (kv_at(s, p) >= 0) + (wd_at(s, p) >= 0) + (h_at(s, p) >= 0) + pf_at(s, p); }
constexpr int cnt_after(int s0, int p0, int s) { int n = 0; for (int z = s0 * 8 + p0 + 1; z < s * 8 + 6; ++z) n += n_at(z / 8, z % 8); return n; }
constexpr int dslot(int sub) { return L_WD + (sub % 3) * 32768; }

struct QkvTab { unsigned v[44][2][4]; };
constexpr QkvTab make_tab() {
  QkvTab t = {};
  for (int tile = 0; tile < 44; ++tile) for (int h = 0; h < 2; ++h) {
    const int col0 = tile * 32, sec = col0 / 448; unsigned* e = t.v[tile][h];
    e[0] = e[1] = e[2] = OOB; e[3] = 0;
    if (tile < 42) for (int pp = 0; pp < 2; ++pp) {
      const int cu = (col0 - sec * 448) / 8 + 2 * pp + h, j = cu / 7, w = cu % 7, hd = 2 * j + (w > 3), ch = w > 3 ? w - 3 : w;
      e[pp] = WS_QKV + sec * 2048 + hd * QH + ch * 512 + (sec == 2 ? ch * 64 : 0);
      if (w == 3) { e[2] = WS_QKV + sec * 2048 + (hd + 1) * QH; e[3] |= 1u << pp; }
    }
  }
  return t;
}
__device__ const QkvTab qkv_tab = make_tab();

#define LANE_IDS() int lane; asm volatile("v_mbcnt_lo_u32_b32 %0, -1, 0\n\tv_mbcnt_hi_u32_b32 %0, -1, %0" : "=v"(lane));        \
  const int r = lane & 31, h = lane >> 5, q = (lane & 15) >> 2, p = lane & 3, grp = (lane >> 4) & 1, rr = lane >> 3, cc = lane & 7
DI void phase23(const char* x, const char* wo, const char* wd, char* ws, char* out, lptr lds, unsigned nonce, u32x4 QA0, u32x4 QA1, u32x4 QB0, u32x4 QB1,
                u32x4 k0, u32x4 k1, u32x4 v0, f32x16 sa, lptr kb, lptr vb0, lptr vb1, unsigned kvv, int wid) {
  const unsigned lds0 = (unsigned)(uintptr_t)lds;
  const int b = __builtin_amdgcn_workgroup_id_x(), xcd = b & 7, idx = b >> 3;
  const int ng = (xcd & 3) * 4 + (idx & 3), mg = (xcd >> 2) * 8 + (idx >> 2);
  const srd_t wsr = SRD(ws, WS_END);
  const f32x16 Z = {};
  const int head = 2 * xcd + (idx & 1), qg = idx >> 1, odd = head & 1;
  const int idx3 = idx < 28 ? idx : idx - 28, mq3 = (idx3 * 37) >> 8, mg3 = xcd * 4 + mq3, ng3 = idx3 - mq3 * 7;
  LANE_IDS();
  const unsigned lw2k = lds0 + wid * 2048;
  auto dma_kv = [&](auto zc) AI { constexpr int z = decltype(zc)::v; dma<(z >> 1) * QT + (z & 1) * 1024, ((z >> 1) & 3) * 16384 + (z & 1) * 1024, true>(wsr, kvv, lw2k); };
  const int w3m = wid & 1, w3k = wid >> 1;
  u32x4 hf[28];
  const int ft = w3k & 1, fs = w3k >> 1;
  unsigned vo_d = 0; const unsigned ld_d = lds0 + wid * 4096;
  auto set_vo_d = [&]() AI { vo_d = (unsigned)(((idx < 28 ? w3k * 448 + w3m * 32 : OOB / 896) + rr) * 896 + ((cc ^ (grp << 2)) << 4) + ng3 * 128); PIN(vo_d); };
  const srd_t sD = SRD(wd, 1792 * 896);
  auto dma_d = [&](auto zc) AI { constexpr int z = decltype(zc)::v; dma<((z >> 2) * 64 + (z & 3) * 8) * 896, dslot(z >> 2) + (z & 3) * 1024>(sD, vo_d, ld_d); };
  int db0 = 0, db1 = 0, dbo = L_WD;
  auto set_db = [&]() AI { db0 = L_WD + w3k * 8192 + (4 * h + q) * 128 + p * 8 + ((grp ^ ((q >> 1) << 1)) << 5); db1 = db0 ^ 64; PIN(db0); PIN(db1); };
  auto rebase = [&](int o) AI { db0 += o - dbo; db1 += o - dbo; dbo = o; PIN(db0); PIN(db1); };
  f32x16 F0 = Z, F1 = Z;
  u32x4 dw0, dw1;
  auto rd_d0 = [&](int d) AI { const int o = dslot(d >> 2) + (d & 3) * 2048 - dbo; dw0 = cat(trd(lds + db0 + o), trd(lds + db0 + o + 1024)); };
  auto rd_d1 = [&](int d) AI { const int o = dslot(d >> 2) + (d & 3) * 2048 - dbo; dw1 = cat(trd(lds + db1 + o), trd(lds + db1 + o + 1024)); };
  {
    const int wq = wid & 1, kq = wid >> 1;
    u32x4 v1, pa, pb;
    float e0, e1, e2, e3;
    f32x16 OA = Z, OB = Z, sb;
    unsigned junk, fh = ~nonce; const int fho = WS_FH + ((mg3 >> 1) * 16 + (lane & 15)) * 128;
    const int hld = WS_H + ((mg3 * 2 + w3m) * 112 + w3k * 28) * 1024, hlv = idx < 28 ? lane * 16 : OOB;
    auto send = [&](const f32x16& a, int hh, int j) AI {
      const lptr d = kb + (((wid ^ (2 * j)) * 6144 + (j - 1) * 2048) - kq * 4096);
      *(LAS f32x4*)d = (f32x4){a[8 * hh], a[8 * hh + 1], a[8 * hh + 2], a[8 * hh + 3]}; *(LAS f32x4*)(d + 1024) = (f32x4){a[8 * hh + 4], a[8 * hh + 5], a[8 * hh + 6], a[8 * hh + 7]}; };
#define EX2(S, i, a, c) do { a = EX(S[i]); c = EX(S[(i) + 1]); PIN(a); PIN(c); } while (0)
#define STEP(FIRST, SC, SN, QN0, QN1, OP, OC, L0, L1, L2, L3, X0, X1) do {                                                       \
      SBAR(); SN = MFMA(k0, QN0, Z); SBAR(); L0;                                                                          \
      if (!(FIRST)) { pb[2] = pk(e0, e1); pb[3] = pk(e2, e3); PIN(pb); } SBAR(); EX2(SC, 0, e0, e1); SBAR(); X0; SBAR(); EX2(SC, 2, e2, e3); \
      SBAR(); if (!(FIRST)) OP = MFMA(v1, pb, OP); SBAR(); L1;                                                                          \
      pa[0] = pk(e0, e1); pa[1] = pk(e2, e3); PIN(pa); SBAR(); EX2(SC, 4, e0, e1); EX2(SC, 6, e2, e3);                    \
      SBAR(); SN = MFMA(k1, QN1, SN); SBAR(); L2;                                                                         \
      pa[2] = pk(e0, e1); pa[3] = pk(e2, e3); PIN(pa); SBAR(); EX2(SC, 8, e0, e1); SBAR(); X1; SBAR(); EX2(SC, 10, e2, e3); \
      SBAR(); OC = MFMA(v0, pa, OC); SBAR(); L3;                                                                          \
      pb[0] = pk(e0, e1); pb[1] = pk(e2, e3); PIN(pb); SBAR(); EX2(SC, 12, e0, e1); EX2(SC, 14, e2, e3); SBAR(); } while (0)
    sfor<0, 16>([&](auto sc_) AI {
      constexpr int s = decltype(sc_)::v, so = (s & 3) * 16384, sn = ((s + 1) & 3) * 16384;
      auto side = [&](auto pc) AI {
        constexpr int P = decltype(pc)::v, kv = kv_at(s, P), wz = wd_at(s, P), hz = h_at(s, P);
        if constexpr (s == 0 && P == 3) set_vo_d();
        if constexpr (s == 0 && P == 5) set_db();
        if constexpr (kv >= 0) dma_kv(IC<kv>{});
        if constexpr (wz >= 0) dma_d(IC<wz>{});
        if constexpr (pf_at(s, P)) {
          const int row = wid < 2 ? mq3 * 448 + 320 + wid * 64 + lane : mq3 * 112 + (wid - 2) * 56 + (lane < 56 ? lane : 55);
          asm volatile("buffer_load_dword %0, %1, %2, 0 offen" : "=&v"(junk) : "v"(wid < 4 && idx < 28 ? row * 896 + ng3 * 128 : OOB), "s"(SRD(wid < 2 ? wd : wo, wid < 2 ? 1792 * 896 : 448 * 896)) : "memory");
        }
        if constexpr (hz >= 0) hf[hz] = __builtin_amdgcn_raw_buffer_load_b128(wsr, hlv + (hz & 3) * 1024, hld + (hz >> 2) * 4096, SC1);
      };
      auto mid = [&]() AI {
        if (s == 4 && wid == 0) {
          asm volatile("s_waitcnt vmcnt(%1)" : "+v"(fh) : "n"(cnt_after(3, 6, 4)) : "memory");
          _Pragma("nounroll") for (unsigned sp = 0; !ALL(fh == nonce) && sp < SPIN_CAP; ++sp) { fh = flag_ld(ws, fho); PIN(fh); }
        }
        if constexpr (s == 0) vwait_bar<5 + cnt_after(0, -1, 0)>();
        else if constexpr (s == 1) vwait_bar<cnt_after(0, -1, 1)>();
        else if constexpr (s == 14) vwait_bar<0>(); else if constexpr (s < 14) vwait_bar<cnt_after(s - 1, 0, s)>();
        if (s == 1 && wid == 0) flag_st(ws, WS_FH + (mg * 16 + ng) * 128, nonce);
        if (s == 3 && wid == 0) asm volatile("buffer_load_dword %0, %1, %2, 0 offen sc1" : "+v"(fh) : "v"(fho), "s"(wsr) : "memory");
        if constexpr (s == 6) asm volatile("" :: "v"(junk));
        if constexpr (s == 13) rebase(L_WD + 65536);
        if constexpr (s == 14) { UNROLL for (int k = 23; k >= 8; --k) PIN(hf[k]); }
        __builtin_amdgcn_s_setprio(3);
      };
      auto dn0 = [&](int d) AI { if (d >= 0) { F0 = MFMA(dw0, hf[d], F0); PIN(F0); } if (d >= -1) rd_d0(d + 1); };
      auto dn1 = [&](int d) AI { if (d >= 0) { F1 = MFMA(dw1, hf[d], F1); PIN(F1); } if (d >= -1) rd_d1(d + 1); };
      STEP(s == 0, sa, sb, QB0, QB1, OB, OA,
           k0 = ld128(kb + sn); side(IC<0>{}); __builtin_amdgcn_s_setprio(2),
           v1 = cat(trd(vb0 + so + 256), trd(vb1 + so + 256)); side(IC<1>{}),
           k1 = ld128(kb + sn + 1024); side(IC<2>{}); __builtin_amdgcn_s_setprio(1),
           side(IC<3>{}), dn0(s - DN0), if (s == 15) dn1(8));
      STEP(false, sb, sa, QA0, QA1, OA, OB,
           __builtin_amdgcn_s_setprio(0); side(IC<4>{}),
           side(IC<5>{}),
           mid(); if (s == 15) send(OA, 0, 1),
           v0 = cat(trd(vb0 + sn), trd(vb1 + sn)); side(IC<7>{}); if (s == 15) send(OA, 1, 3), if (s < 15) dn1(s - DN0); else dn0(9), if (s == 15) dn1(9));
    });
    pb[2] = pk(e0, e1); pb[3] = pk(e2, e3);
    OB = MFMA(v1, pb, OB);
    __builtin_amdgcn_s_setprio(0);
    send(OB, 1, 2);
    *(LAS float*)(kb + (49152 + wq * 1024)) = odd ? OB[0] : OB[4];
    {
    LANE_IDS();
    const bool padl = h != odd;
    const int qt = qg * 4 + wq * 2 + ft, lw = (wid & 3) | (odd ? 0 : 4);
    const lptr pp = lds + wid * 6144 + lane * 16, lp = lds + lw * 6144 + (odd ? 0 : 1536) + r * 16;
    const lptr lq = lds + 49152 + (lw >> 1) * 4096 + wq * 1024 + (odd ? 0 : 512) + r * 16;
    f32x4 o0 = {OB[0], OB[1], OB[2], OB[3]}, o1 = {OB[4], OB[5], OB[6], OB[7]};
    lbar();
    const float l0 = *(LAS float*)lq, l1 = *(LAS float*)lp, l2 = *(LAS float*)(lp + 2048), l3 = *(LAS float*)(lp + 4096);
    f32x4 t[6];
    UNROLL for (int k = 0; k < 6; ++k) t[k] = *(LAS f32x4*)(pp + k * 1024);
    SBAR();
    float l = (l0 + l1) + (l2 + l3);
    UNROLL for (int k = 0; k < 3; ++k) { o0 += t[2 * k]; o1 += t[2 * k + 1]; }
    if (__builtin_expect(ANY(!(l <= 1.8446744e19f && l >= 5.4210109e-20f)), 0)) {
      const int qo = WS_QKV + head * QH + qt * QT + lane * 16;
      u32x4 q0 = __builtin_amdgcn_raw_buffer_load_b128(wsr, qo, 0, SC1), q1 = __builtin_amdgcn_raw_buffer_load_b128(wsr, qo + 1024, 0, SC1);
      f32x16 O = Z; float m = -__builtin_inff();
      _Pragma("nounroll") for (int t = 0; t < 64; ++t) {
        const int ko = WS_QKV + head * QH + t * QT + 2048;
        f32x16 s = MFMA(__builtin_amdgcn_raw_buffer_load_b128(wsr, ko + lane * 16, 0, SC1), q0, Z);
        s = MFMA(__builtin_amdgcn_raw_buffer_load_b128(wsr, ko + 1024 + lane * 16, 0, SC1), q1, s);
        float mn = m;
        UNROLL for (int i = 0; i < 16; ++i) mn = __builtin_fmaxf(mn, s[i]);
        mn = max32(mn);
        const float f = EX(m - mn); m = mn;
        UNROLL for (int i = 0; i < 16; ++i) O[i] *= f;
        UNROLL for (int st = 0; st < 2; ++st) {
          unsigned vv[8];
          UNROLL for (int j = 0; j < 8; ++j) vv[j] = __builtin_amdgcn_raw_buffer_load_b16(wsr, ko + 2048 + (r >> 3) * 512 + (((16 * st + 8 * (j >> 2) + 4 * h + (j & 3)) ^ (4 * (r >> 3))) << 4) + (r & 7) * 2, 0, SC1) & 0xffffu;
          O = MFMA(((u32x4){vv[0] | vv[1] << 16, vv[2] | vv[3] << 16, vv[4] | vv[5] << 16, vv[6] | vv[7] << 16}),
                   ((u32x4){pk(EX(s[8 * st] - m), EX(s[8 * st + 1] - m)), pk(EX(s[8 * st + 2] - m), EX(s[8 * st + 3] - m)),
                            pk(EX(s[8 * st + 4] - m), EX(s[8 * st + 5] - m)), pk(EX(s[8 * st + 6] - m), EX(s[8 * st + 7] - m))}), O);
        }
      }
      const float lc = odd ? O[0] : O[12], lx = swap32(lc, h); l = padl ? lc : lx;
      o0 = fs ? (f32x4){O[8], O[9], O[10], O[11]} : (f32x4){O[0], O[1], O[2], O[3]};
      o1 = fs ? (f32x4){O[12], O[13], O[14], O[15]} : (f32x4){O[4], O[5], O[6], O[7]};
    }
    const float ri = __builtin_amdgcn_rcpf(l);
    const float r0 = padl && odd && fs == 0 ? 0.f : ri, r1 = padl && !odd && fs == 1 ? 0.f : ri;
    st128((u32x4){pk(o0[0] * r0, o0[1] * r0), pk(o0[2] * r0, o0[3] * r0), pk(o1[0] * r1, o1[1] * r1), pk(o1[2] * r1, o1[3] * r1)},
                                           wsr, lane * 16, WS_O + (qt * 16 + head) * 2048 + fs * 1024, SC1);
    }
  }
  if (idx >= 28) { wait_bar<0>();
    if (wid == 0) {
      flag_st(ws, WS_FO + (qg * 16 + head) * 128, nonce);
      if (b == 255) {
        LANE_IDS();
        _Pragma("nounroll") for (unsigned sp = 0; sp < SPIN_CAP; ++sp) {
          unsigned d = 0;
          UNROLL for (int i = 0; i < 4; ++i) d |= flag_ld(ws, WS_FQ + lane * 512 + i * 128) ^ nonce;
          if (ALL(d == 0)) break;
        }
        flag_st(ws, WS_E, nonce);
      }
    }
    return;
  }
  {
    lbar();
    const srd_t sO = SRD(wo, 448 * 896); const unsigned wov = vo_d + (wid * 56 - w3k * 448 - w3m * 32) * 896, wol = lds0 + wid * 7168;
    auto dma_o = [&](auto ic) AI { constexpr int i = decltype(ic)::v; dma<i * 8 * 896, i * 1024>(sO, wov, wol); };
    sfor<0, 4>(dma_o);
    u32x4 ew0, ew1;
    auto down2 = [&](int d, bool more) AI {
      const int o = dslot((d + 1) >> 2) + ((d + 1) & 3) * 2048 - dbo;
      ew0 = cat(trd(lds + db0 + o), trd(lds + db0 + o + 1024)); ew1 = cat(trd(lds + db1 + o), trd(lds + db1 + o + 1024));
      if (d == 10 || d == 22) rebase(L_WD);
      SBAR(); F0 = MFMA(dw0, hf[d], F0); F1 = MFMA(dw1, hf[d], F1); PIN(F0); PIN(F1); SBAR();
      if (more) { rd_d0(d + 2); rd_d1(d + 2); }
      SBAR(); F0 = MFMA(ew0, hf[d + 1], F0); F1 = MFMA(ew1, hf[d + 1], F1); PIN(F0); PIN(F1); SBAR();
    };
    static_assert(DN0 == 7, "units 0..9 ran beside the attention");
    down2(10, true); sfor<4, 7>(dma_o);
    vwait_bar<7>();
    if (wid == 0) flag_st(ws, WS_FO + (qg * 16 + head) * 128, nonce);
    sfor<20, 22>(dma_d); down2(12, true); sfor<22, 24>(dma_d); down2(14, true);
    vwait_bar<4>();
    LANE_IDS();
    const int mt = mg3 * 2 + w3m, orot = ng3 & 1;
    unsigned fo = ~nonce; const int foo = WS_FO + ((mg3 >> 1) * 16 + (lane & 15)) * 128;
    sfor<24, 26>(dma_d); down2(16, true); sfor<26, 28>(dma_d);
    if (wid == 0) asm volatile("buffer_load_dword %0, %1, %2, 0 offen sc1" : "+v"(fo) : "v"(foo), "s"(wsr) : "memory");
    down2(18, false);
    u32x4 wo0[8], wo1[8];
    UNROLL for (int k = 0; k < 8; ++k) {
      const int wbo = w3k * (PART - 8192) - L_WD + ((k >> 2) ^ orot) * 7168, o = (((k >> 1) & 1) * 24 + (k & 1) * 16) * 128;
      const lptr wb0 = lds + db0 + wbo, wb1 = lds + db1 + wbo;
      wo0[k] = cat(trd(wb0 + o), trd(wb0 + o + 1024)); wo1[k] = cat(trd(wb1 + o), trd(wb1 + o + 1024));
    }
    rebase(L_WD + 65536);
    if (wid == 0) {
      asm volatile("s_waitcnt vmcnt(0)" : "+v"(fo) :: "memory");
      _Pragma("nounroll") for (unsigned sp = 0; !ALL(fo == nonce) && sp < SPIN_CAP; ++sp) { fo = flag_ld(ws, foo); PIN(fo); }
    }
    wait_bar<0>();
    UNROLL for (int k = 27; k >= 24; --k) PIN(hf[k]);
    u32x4 of[8];
    const int oso = WS_O + (mt * 16 + w3k * 4) * 2048 + orot * 4096;
    UNROLL for (int k = 0; k < 4; ++k) of[k] = __builtin_amdgcn_raw_buffer_load_b128(wsr, lane * 16 + k * 1024, oso, SC1);
    SBAR();
    rd_d0(20); rd_d1(20); down2(20, true); down2(22, true);
    UNROLL for (int k = 4; k < 8; ++k) of[k] = __builtin_amdgcn_raw_buffer_load_b128(wsr, lane * 16 + (k & 3) * 1024, oso ^ 4096, SC1);
    const unsigned xo = (unsigned)((mt * 32 + w3k * 8 + rr) * 896 + ng3 * 128 + cc * 16);
    const u32x4 xv = *(const u32x4*)(x + xo);
    SBAR();
    down2(24, true); down2(26, false);
    auto xch_send = [&](const f32x16& a0, const f32x16& a1, int base) AI {
      UNROLL for (int c = 0; c < 8; ++c) { const f32x16& a = c < 4 ? a0 : a1; const int o = (c & 3) * 4;
        *(LAS f32x4*)(lds + (base + wid * 8192 + c * 1024) + ((lane ^ (2 * c)) << 4)) = (f32x4){a[o], a[o + 1], a[o + 2], a[o + 3]}; } };
    int xro = w3m * 8192 + cc * 1024 + (((w3k * 8 + rr) ^ (2 * cc)) << 4); PIN(xro);
    xch_send(F0, F1, 0);
    f32x16 S0 = Z, S1 = Z;
    UNROLL for (int k = 0; k < 4; ++k) { S0 = MFMA(wo0[k], of[k], S0); S1 = MFMA(wo1[k], of[k], S1); }
    PIN(S0); PIN(S1);
    lbar();
    f32x4 g0 = *(LAS f32x4*)(lds + xro), g1 = *(LAS f32x4*)(lds + xro + 512);
    UNROLL for (int k = 1; k < 4; ++k) { g0 += *(LAS f32x4*)(lds + xro + k * 16384); g1 += *(LAS f32x4*)(lds + xro + k * 16384 + 512); }
    UNROLL for (int k = 4; k < 8; ++k) { S0 = MFMA(wo0[k], of[k], S0); S1 = MFMA(wo1[k], of[k], S1); }
    UNROLL for (int i = 0; i < 4; ++i) {
      g0[i] = g0[i] * __builtin_amdgcn_rcpf(1.f + EX(g0[i] * -1.4426950408889634f)); g1[i] = g1[i] * __builtin_amdgcn_rcpf(1.f + EX(g1[i] * -1.4426950408889634f)); }
    xch_send(S0, S1, L_WD);
    f32x4 y0 = {U2F(xv[0] << 16), U2F(xv[0] & 0xffff0000u), U2F(xv[1] << 16), U2F(xv[1] & 0xffff0000u)}, y1 = {U2F(xv[2] << 16), U2F(xv[2] & 0xffff0000u), U2F(xv[3] << 16), U2F(xv[3] & 0xffff0000u)};
    xro += L_WD;
    PIN(g0); PIN(g1); PIN(y0); PIN(y1); PIN(xro);
    lbar();
    f32x4 t[8];
    UNROLL for (int k = 0; k < 8; ++k) t[k] = *(LAS f32x4*)(lds + xro + (k >> 1) * 16384 + (k & 1) * 512);
    SBAR();
    UNROLL for (int k = 0; k < 4; ++k) UNROLL for (int i = 0; i < 4; ++i) { y0[i] = __builtin_fmaf(t[2 * k][i], g0[i], y0[i]); y1[i] = __builtin_fmaf(t[2 * k + 1][i], g1[i], y1[i]); }
    __builtin_amdgcn_raw_buffer_store_b128((u32x4){pk(y0[0], y0[1]), pk(y0[2], y0[3]), pk(y1[0], y1[1]), pk(y1[2], y1[3])}, SRD(out, 2048 * 896), xo, 0, SC1);
  }
}

#if defined(__HIP_DEVICE_COMPILE__)
#define NOPK __attribute__((target("no-packed-fp32-ops")))
#else
#define NOPK
#endif
__global__ __launch_bounds__(512, 2) NOPK void fused_kernel(const char* x, const char* wqkv, const char* wo, const char* wg, const char* wu, const char* wd,
                                                       char* ws, char* out) {
  extern __shared__ __attribute__((aligned(16))) char smem[];
  const lptr lds = (lptr)smem;
  const unsigned lds0 = (unsigned)(uintptr_t)lds;
  const int tid = __builtin_amdgcn_workitem_id_x(), lane = tid & 63, wid = __builtin_amdgcn_readfirstlane(tid >> 6);
  const int rr = lane >> 3, cc = lane & 7;
  const int b = __builtin_amdgcn_workgroup_id_x(), xcd = b & 7, idx = b >> 3;
  const int ng = (xcd & 3) * 4 + (idx & 3), mg = (xcd >> 2) * 8 + (idx >> 2);
  {
    unsigned long long pc; asm volatile("s_getpc_b64 %0" : "=s"(pc));
    touch(SRD(pc & ~127ull, 28672), (idx * 8 + (tid >> 6)) * 128, lds0 + L_PF);
  }
  const int rot = (mg & 7) == 7 ? 3 : mg & 7;
  unsigned kc[7];
  UNROLL for (int c = 0; c < 7; ++c) kc[c] = (unsigned)((c + rot >= 7 ? c + rot - 7 : c + rot) * 64);
  const srd_t xr = SRD(x, 2048 * 896);
  const unsigned xv0 = (unsigned)((mg * 128 + wid * 16 + rr) * 896 + ((cc ^ rr) << 4));
  const unsigned xv1 = (unsigned)((mg * 128 + wid * 16 + 8 + (rr ^ 1)) * 896 + ((cc ^ rr ^ 1) << 4));
  const unsigned lw2k = lds0 + wid * 2048;
  auto dma_x = [&](auto cc_) AI { constexpr int c = decltype(cc_)::v; dma2<(c & 3) * 16384>(xr, xv0, xv1, lw2k, kc[c], 2); };
  const srd_t wsr = SRD(ws, WS_END);
  u32x4 srow; unsigned nonce;
  const int srw = mg * 128 + ng * 8 + wid;
  asm volatile("buffer_load_dwordx4 %0, %1, %2, %3 offen" : "=&v"(srow) : "v"(lane * 16), "s"(SRD(x, srw * 896 + 896)), "s"(srw * 896) : "memory");
  dma_x(IC<0>{});
  SBAR();
  const int r = lane & 31, h = lane >> 5, q = (lane & 15) >> 2, p = lane & 3, grp = (lane >> 4) & 1;
  const f32x16 Z = {};
  const int head = 2 * xcd + (idx & 1), qg = idx >> 1;
  auto dma_kv = [&](auto zc, unsigned kvv) AI { constexpr int z = decltype(zc)::v; dma<(z >> 1) * QT + (z & 1) * 1024, ((z >> 1) & 3) * 16384 + (z & 1) * 1024, true>(wsr, kvv, lw2k); };
  u32x4 QA0, QA1, QB0, QB1, kf0, kf1, ka0, ka1, va0; f32x16 s0; lptr kb, vb0, vb1; unsigned kvv = 0;
  {
    const int wm = wid & 3, wn = wid >> 2;
    const bool bq = ng <= 10;
    const unsigned st1 = bq ? 2688 : 3584;
    unsigned va = (16 * wm + rr) * st1 + (((cc << 4) ^ (grp << (bq ? 6 : 5))) ^ (bq ? 0 : wn << 6)) + (bq ? 2 * ng + wn : ng + 12) * 128, vb = va + 8 * st1;
    const char* wgu = wn ? wu : wg;
    const srd_t s2 = SRD(wgu, 448 * 3584), s1 = SRD(bq ? wqkv : wgu, 448 * st1);
    auto dma_w = [&](auto mc) AI {
      constexpr int m = decltype(mc)::v;
      if constexpr (m == 7) { va = (unsigned)((16 * wm + rr) * 3584 + ((cc ^ (grp << 1) ^ (wn << 2)) << 4)) + ng * 128; vb = va + 8 * 3584; PIN(va); PIN(vb); }
      if constexpr (m == 14) {
        va = (unsigned)((16 * wm + (lane >> 2)) * 3584 + (((lane & 3) ^ (wn << 1)) << 4)) + 2048 + ng * 64;
        if (ng >= 14) va = OOB;
        PIN(va);
      }
      if constexpr (m < 7) dma2<wpos(m)>(s1, va, vb, lw2k, kc[m], st1);
      else if constexpr (m < 14) dma2<wpos(m)>(s2, va, vb, lw2k, kc[m - 7], 3584);
      else dma1<wpos(m)>(s2, va, lds0 + wid * 1024, kc[m - 14], 3584);
    };
    dma_w(IC<0>{});
    unsigned epoch;
    asm volatile("buffer_load_dword %0, off, %1, 0 sc1" : "=&v"(epoch) : "s"(wsr) : "memory");
    dma_x(IC<1>{});
    SBAR();
    const int rowb = (8 * h + q) * 128 + p * 8;
    int b20 = grp * 8192 + rowb + (((2 * wn) ^ (q >> 1) ^ (grp << 1)) << 5);
    int b10 = bq ? wn * 8192 + rowb + ((grp ^ ((q >> 1) << 1)) << 5) : b20, b11 = b10 ^ (bq ? 64 : 32);
    int xb[4];
    UNROLL for (int i = 0; i < 4; ++i) { xb[i] = (4 * wm + (r >> 3)) * 1024 + ((r & 7) ^ ((r >> 3) & 1)) * 128 + (((2 * i + h) ^ (r & 7)) << 4); PIN(xb[i]); }
    PIN(b10); PIN(b11); PIN(b20);
    UNROLL for (int c = 2; c < 7; ++c) asm volatile("" : "+s"(kc[c]));
    SBAR();
    u32x4 xf[28];
    auto row_stats = [&]() AI {
      float a = dot2(srow[0], ONES2, 0.f), c = dot2(srow[0], srow[0], 0.f);
      UNROLL for (int i = 1; i < 4; ++i) { a = dot2(srow[i], ONES2, a); c = dot2(srow[i], srow[i], c); }
      a = wave_sum(a); c = wave_sum(c);
      const float mean = a * (1.f / 448.f), var = __builtin_fmaxf(c * (1.f / 448.f) - mean * mean, 0.f);
      unsigned g = pk(-mean, __builtin_amdgcn_sqrtf(rbf(rbf(var) + 1e-5f))); PIN(g); SBAR();
      asm volatile("s_waitcnt vmcnt(4)" : "+v"(epoch) :: "memory");
      nonce = __builtin_amdgcn_readfirstlane(epoch) * 0x9E3779B1u + 0x7F4A7C15u;
      if (lane == 63) __hip_atomic_store((GAS unsigned long long*)(ws + WS_ST + srw * 8), (unsigned long long)nonce << 32 | g, RLX_AGENT);
    };
    auto rd_x = [&](int k0, int k1) AI { UNROLL for (int ks = k0; ks < k1; ++ks) xf[ks] = ld128(lds + xb[ks & 3] + ((ks >> 2) & 3) * 16384); };
    u32x2 wf[NWF][4];
    int ra0 = 0, ra1 = 0;
    auto rd_w = [&](int n) AI {
      if (n < 56) {
        const int m = n >> 2, o = (n & 3) * 2048;
        if ((n & 3) == 0) { ra0 = (m < 7 ? b10 : b20) + wpos(m); ra1 = (m < 7 ? b11 : b20 ^ 32) + wpos(m); PIN(ra0); PIN(ra1); }
        wf[n % NWF][0] = trd(lds + ra0 + o); wf[n % NWF][1] = trd(lds + ra0 + o + 512); wf[n % NWF][2] = trd(lds + ra1 + o); wf[n % NWF][3] = trd(lds + ra1 + o + 512);
      } else {
        const int o = ((n - 56) >> 1) * 8192 + (n & 1) * 2048;
        if (n == 56) { ra0 = wpos(14) + grp * 4096 + (8 * h + q) * 64 + ((wn ^ grp) << 5) + p * 8; PIN(ra0); }
        wf[n % NWF][0] = trd(lds + ra0 + o); wf[n % NWF][1] = trd(lds + ra0 + o + 256); wf[n % NWF][2] = trd(lds + ra0 + o + 1024); wf[n % NWF][3] = trd(lds + ra0 + o + 1280);
      }
    };
    SBAR();
    dma_w(IC<1>{});
    asm volatile("s_waitcnt vmcnt(9)" : "+v"(srow) :: "memory");
    row_stats();
    SBAR();
    lbar();
    UNROLL for (int n = 0; n < RA; ++n) rd_w(n);
    rd_x(0, 4);
    dma_x(IC<2>{});
    f32x16 A0 = Z, A1 = Z, B0 = Z, B1 = Z, C0 = Z;
    float rstd = 0.f, sig = 0.f, kg = 0.f; unsigned mm = 0; u32x2 sg = {0u, ~nonce};
    const int mt = mg * 4 + wm;
    const int cso = WS_CS + ((mg >> 3) * 48 + ng) * 8192;
    auto cs_make = [&](int pass) AI {
      float v = 0.f;
      const bool prod = (wm >> 1) == wn;
      if (prod && (pass < 2 || !(wm & 1))) {
        const int base = pass == 0 ? ((wm & 1) ? b11 : b10) + wpos(0) : pass == 1 ? (b20 ^ ((wm & 1) << 5)) + wpos(7) : wpos(14) + grp * 4096 + (8 * h + q) * 64 + ((wn ^ grp) << 5) + p * 8;
        f32x16 acc = Z;
        UNROLL for (int k = 0; k < 4; ++k) acc = MFMA(((u32x4){ONES2, ONES2, ONES2, ONES2}), cat(trd(lds + base + k * (pass < 2 ? 2048 : 1024)), trd(lds + base + k * (pass < 2 ? 2048 : 1024) + (pass < 2 ? 512 : 256))), acc);
        if ((mg & 7) != 7) v = acc[0];
      }
      const float hi = rbf(v);
      __builtin_amdgcn_raw_buffer_store_b64((u32x2){pk(hi, v - hi), nonce}, wsr, prod && h == 0 ? (wm * 32 + r) * 8 : OOB, cso + pass * 131072 + (mg & 7) * 1024, SC1);
    };
    auto cs_fetch = [&](auto pc) AI { dma<decltype(pc)::v * 131072, L_CS, true>(wsr, cso + tid * 16, lds0 + wid * 1024); };
    u32x4 cf[2], ct[2];
    auto cs_read = [&](int nt) AI {
      const lptr o = lds + L_CS + h * 4096 + (wn * 64 + r) * 8;
      UNROLL for (int t = 0; t < nt; ++t) UNROLL for (int i = 0; i < 4; ++i) { cf[t][i] = *(LAS unsigned*)(o + t * 256 + i * 1024); ct[t][i] = *(LAS unsigned*)(o + t * 256 + i * 1024 + 4); }
    };
    auto cs_check = [&](int pass, int nt) AI {
      unsigned d = 0;
      UNROLL for (int t = 0; t < nt; ++t) d |= ((ct[t][0] ^ ct[t][1]) | (ct[t][2] ^ ct[t][3])) | ((ct[t][0] ^ nonce) | (ct[t][2] ^ nonce));
      _Pragma("nounroll") for (unsigned sp = 0; __builtin_expect(ANY(d != 0), 0) && sp < SPIN_CAP; ++sp) {
        LANE_IDS();
        d = 0;
        UNROLL for (int t = 0; t < nt; ++t) UNROLL for (int i = 0; i < 4; ++i) {
          const u32x2 g = __builtin_amdgcn_raw_buffer_load_b64(wsr, h * 4096 + (wn * 64 + t * 32 + r) * 8 + i * 1024, cso + pass * 131072, SC1); cf[t][i] = g[0]; d |= g[1] ^ nonce; }
        PIN(d);
      }
    };
    auto fix = [&](f32x16& a, u32x4 c) AI { a = MFMA(c, ((u32x4){mm, mm, mm, mm}), a); PIN(a); };
    auto glu = [&](const f32x16& acc, int i) AI { return acc[i + 8] * __builtin_amdgcn_rcpf(__builtin_fmaf(EX(acc[i] * kg), sig, sig)); };
    auto glu8 = [&](const f32x16& acc) AI { return (u32x4){pk(glu(acc, 0), glu(acc, 1)), pk(glu(acc, 2), glu(acc, 3)), pk(glu(acc, 4), glu(acc, 5)), pk(glu(acc, 6), glu(acc, 7))}; };
    const int hs2 = WS_H + (mt * 112 + ng * 4 + wn * 2) * 1024;
    u32x4 tb0 = {0u, 0u, 0u, 0u}, tb1 = tb0;
    auto epi1 = [&](const f32x16& acc, int t) AI {
      if (bq) {
        const u32x4 e = t ? tb1 : tb0; const unsigned o[3] = {e[0] ^ (r << 4), e[1] ^ (r << 4), e[2] ^ (r << 4)};
        const unsigned m[2] = {(unsigned)__builtin_amdgcn_sbfe(e[3], 0, 1), (unsigned)__builtin_amdgcn_sbfe(e[3], 1, 1)};
        const float sc = 2 * ng + wn < 7 ? rstd * (0.18898223650461362f * 1.4426950408889634f) : rstd;
        u32x4 v[2];
        UNROLL for (int pp = 0; pp < 2; ++pp)
          v[pp] = widen((u32x2){pk(acc[8 * pp] * sc, acc[8 * pp + 1] * sc), pk(acc[8 * pp + 2] * sc, acc[8 * pp + 3] * sc)},
                        (u32x2){pk(acc[8 * pp + 4] * sc, acc[8 * pp + 5] * sc), pk(acc[8 * pp + 6] * sc, acc[8 * pp + 7] * sc)});
        u32x4 d0 = {v[0][0], v[0][1], bfi(m[0], PAD0, v[0][2]), ~m[0] & v[0][3]}, d1 = {v[1][0], v[1][1], bfi(m[1], PAD0, v[1][2]), ~m[1] & v[1][3]};
        u32x4 d2 = {PAD0, 0u, bfi(m[0], v[0][2], v[1][2]), bfi(m[0], v[0][3], v[1][3])};
        PIN(d0); PIN(d1); PIN(d2); SBAR();
        __builtin_amdgcn_raw_buffer_store_b128(d0, wsr, o[0], mt * QT, SC1); __builtin_amdgcn_raw_buffer_store_b128(d1, wsr, o[1], mt * QT, SC1);
        st128(d2, wsr, o[2], mt * QT, SC1);
      } else st128(glu8(acc), wsr, lane * 16 + t * 1024, hs2 + 12 * 4096, SC1);
    };
    u32x4 hp, hq; unsigned fl0 = ~nonce; f32x16 sa, sb;
    int fqo = 0;
    auto fq_off = [&]() AI {
      const int c0 = (lane < 8 ? 448 + (wid & 1) * 448 : 0) + head * 28 - (head & 1) * 4 + (lane & 1) * 31, g = c0 >> 7, m2 = lane < 8 ? (wid >> 1) * 4 + (lane >> 1) : qg;
      fqo = WS_FQ + (m2 * 16 + g) * 128; };
    const int sgo = WS_ST + (mg * 128 + wm * 32 + r) * 8;

    sfor<0, 70>([&](auto nc) AI {
      constexpr int n = decltype(nc)::v, u = n & 7;
      constexpr int k0 = n < 28 ? n : n < 56 ? n - 28 : 2 * (n - 56), k1 = n < 56 ? k0 : k0 + 1;
      f32x16& D0 = n < 28 ? A0 : n < 56 ? B0 : C0; f32x16& D1 = n < 28 ? A1 : n < 56 ? B1 : C0;
      PIN(wf[n % NWF][3]);
      if (u == 7) __builtin_amdgcn_s_setprio(3); if (u == 1) __builtin_amdgcn_s_setprio(2); if (u == 3) __builtin_amdgcn_s_setprio(1); if (u == 5) __builtin_amdgcn_s_setprio(0);
      SBAR();
      D0 = MFMA(cat(wf[n % NWF][0], wf[n % NWF][1]), xf[k0], D0); PIN(D0);
      SBAR();
      if constexpr (u == 6 || n == 68 || n == 2) {
        if (n == 2) wait_bar<8>(); else if (n <= 14) wait_bar<2>(); else if (n == 22) wait_bar<4>(); else if (n == 30) { if (bq) wait_bar<13>(); else wait_bar<9>(); }
        else if (n == 38) wait_bar<2>(); else if (n == 46) wait_bar<3>(); else if (n == 54) wait_bar<6>();
        else if (n == 62) lbar(); else wait_bar<4>();
        if (n == 38 && wid == 0) flag_st(ws, WS_FQ + (mg * 16 + ng) * 128, nonce);
        SBAR();
      }
      constexpr bool d1f = u == 6 || n == 68 || n == 2;
      if constexpr (d1f) { D1 = MFMA(cat(wf[n % NWF][2], wf[n % NWF][3]), xf[k1], D1); PIN(D1); SBAR(); }
      if constexpr (n + RA < 70) rd_w(n + RA);
      if constexpr (n == 68) {
        const lptr qb = lds + L_Q + (wid & 1) * 4096 + ((wid >> 1) & 1) * 2048 + lane * 16, qa = lds + L_Q + (wid & 1) * 4096 + (((wid >> 1) & 1) ^ 1) * 2048 + lane * 16;
        kf0 = ld128(lds + lane * 16); QA0 = ld128(qa); QB0 = ld128(qb); kf1 = ld128(lds + 1024 + lane * 16); QA1 = ld128(qa + 1024); QB1 = ld128(qb + 1024);
        cs_read(1);
      }
      if constexpr (n == 69) { sa = MFMA(kf1, QA1, sa); sb = MFMA(kf1, QB1, sb); PIN(sa); PIN(sb); }
      if constexpr (n == 2) rd_x(4, 8);
      if constexpr (u == 6 && n < 24) rd_x(n + 2, n + 6);
      if constexpr (u == 0 && n > 0 && n < 24) rd_x(n + 4, n + 8);
      SBAR();
      if constexpr (!d1f) { D1 = MFMA(cat(wf[n % NWF][2], wf[n % NWF][3]), xf[k1], D1); PIN(D1); }
      SBAR();
      if constexpr (n == 0) dma_w(IC<2>{}); if constexpr (n == 1) dma_x(IC<3>{}); if constexpr (n == 2) dma_w(IC<3>{}); if constexpr (n == 4) dma_w(IC<4>{});
      if constexpr (n == 6) dma_x(IC<4>{}); if constexpr (n == 7) dma_x(IC<5>{}); if constexpr (n == 8) dma_w(IC<5>{}); if constexpr (n == 10) dma_w(IC<6>{});
      if constexpr (n == 14) dma_x(IC<6>{}); if constexpr (n == 15) dma_w(IC<7>{}); if constexpr (n == 18) dma_w(IC<8>{}); if constexpr (n == 20) dma_w(IC<9>{});
      if constexpr (n == 23) dma_w(IC<10>{}); if constexpr (n == 26) dma_w(IC<11>{}); if constexpr (n == 29) dma_w(IC<12>{}); if constexpr (n == 32) dma_w(IC<13>{});
      if constexpr (n == 48) touch(SRD(b < 196 ? wd : wo, b < 196 ? 1792 * 896 : 448 * 896), lane < 8 ? (((b < 196 ? b : b - 196) * 8 + wid) * 8 + lane) * 128 : OOB, lds0 + L_PF + wid * 256);
      if constexpr (n == 17) asm volatile("buffer_load_dwordx2 %0, %1, %2, 0 offen sc1" : "+v"(sg) : "v"(sgo), "s"(wsr) : "memory");
      if constexpr (n == 16) asm volatile("global_load_dwordx4 %0, %2, %3\n\tglobal_load_dwordx4 %1, %2, %3 offset:32" : "+v"(tb0), "+v"(tb1) : "v"(h * 16), "s"(&qkv_tab.v[bq ? 4 * ng + 2 * wn : 0]) : "memory");
      if constexpr (n >= 39 && n < 53 && (n & 1)) dma_w(IC<14 + (n - 39) / 2>{});
      if constexpr (n == 0) cs_make(0); if constexpr (n == 24) cs_make(1); if constexpr (n == 46) cs_make(2);
      if constexpr (n == 17) cs_fetch(IC<0>{}); if constexpr (n == 38) cs_fetch(IC<1>{}); if constexpr (n == 62) cs_fetch(IC<2>{});
      if constexpr (n == 23) {
        PIN(sg); PIN(tb0); PIN(tb1);
        _Pragma("nounroll") for (unsigned sp = 0; !ALL(sg[1] == nonce) && sp < SPIN_CAP; ++sp)
          asm volatile("buffer_load_dwordx2 %0, %1, %2, 0 offen sc1\n\ts_waitcnt vmcnt(0)" : "+v"(sg) : "v"(sgo), "s"(wsr) : "memory");
        sig = U2F(sg[0] & 0xffff0000u); rstd = __builtin_amdgcn_rcpf(sig); mm = __builtin_amdgcn_perm(sg[0], sg[0], 0x01000100u); kg = -1.4426950408889634f * rstd;
        PIN(sig); PIN(rstd); PIN(kg); PIN(mm);
      }
      if constexpr (n == 25 || n == 50) cs_read(2);
      if constexpr (n == 27) { cs_check(0, 2); fix(A0, cf[0]); fix(A1, cf[1]); }
      if constexpr (n == 28) epi1(A0, 0);
      if constexpr (n == 29) epi1(A1, 1);
      if constexpr (n == FQP) { fq_off(); asm volatile("buffer_load_dword %0, %1, %2, 0 offen sc1" : "+v"(fl0) : "v"(fqo), "s"(wsr) : "memory"); }
      if constexpr (n == 53) { cs_check(1, 2); fix(B0, cf[0]); fix(B1, cf[1]); }
      if constexpr (n == 57) { hp = glu8(B0); PIN(hp); }
      if constexpr (n == 64) { hq = glu8(B1); PIN(hq); }
      if constexpr (n == 69) { st128(hp, wsr, lane * 16, hs2, SC1); st128(hq, wsr, lane * 16 + 1024, hs2, SC1); }
      if constexpr (n == FQC) {
        asm volatile("s_waitcnt vmcnt(0)" : "+v"(fl0) :: "memory");
        _Pragma("nounroll") for (unsigned sp = 0; !ALL(fl0 == nonce) && sp < SPIN_CAP; ++sp)
          asm volatile("buffer_load_dword %0, %1, %2, 0 offen sc1\n\ts_waitcnt vmcnt(0)" : "+v"(fl0) : "v"(fqo), "s"(wsr) : "memory");
        kvv = (unsigned)(WS_QKV + head * QH + (wid >> 1) * 16 * QT + 2048 + (wid & 1) * 2048 + lane * 16);
        dma<0, L_Q, true>(wsr, WS_QKV + head * QH + (qg * 4 + (wid >> 1)) * QT + (wid & 1) * 1024 + lane * 16, lds0 + wid * 1024); dma_kv(IC<0>{}, kvv); }
      if constexpr (n > FQC && n < FQC + 8) dma_kv(IC<n - FQC>{}, kvv);
      if constexpr (n == 67) {
        const int vc = 2 * (grp ^ wn) + (p >> 1);
        kb = lds + (wid >> 1) * 4096 + lane * 16;
        vb0 = lds + (wid >> 1) * 4096 + 2048 + vc * 512 + ((((4 * h) ^ (4 * vc)) + q) << 4) + (p & 1) * 8;
        vb1 = lds + (wid >> 1) * 4096 + 2048 + vc * 512 + ((((4 * h + 8) ^ (4 * vc)) + q) << 4) + (p & 1) * 8;
        PIN(kb); PIN(vb0); PIN(vb1);
      }
      if constexpr (n == 68) { sa = MFMA(kf0, QA0, Z); sb = MFMA(kf0, QB0, Z); PIN(sa); PIN(sb); ka0 = ld128(kb); ka1 = ld128(kb + 1024); va0 = cat(trd(vb0), trd(vb1)); }
    });
    SBAR();
    cs_check(2, 1); fix(C0, cf[0]);
    float ma = sa[0], mb = sb[0];
    UNROLL for (int i = 1; i < 16; ++i) { ma = __builtin_fmaxf(ma, sa[i]); mb = __builtin_fmaxf(mb, sb[i]); }
    const unsigned pa = pk(1.f - max32(ma), 0.f), pb = pk(1.f - max32(mb), 0.f);
    const bool p0 = (head & 1) && h == 0, p1 = !(head & 1) && h == 1;
    QA0[0] = p0 ? pa : QA0[0]; QA1[2] = p1 ? pa : QA1[2]; QB0[0] = p0 ? pb : QB0[0]; QB1[2] = p1 ? pb : QB1[2];
    s0 = MFMA(ka0, QA0, Z); s0 = MFMA(ka1, QA1, s0);
    PIN(s0); PIN(QB0); PIN(QB1);
    SBAR();
    st128(glu8(C0), wsr, ng < 14 ? lane * 16 : OOB, WS_H + (mt * 112 + 64 + ng * 2 + wn) * 1024, SC1);
    SBAR();
  }
  return phase23(x, wo, wd, ws, out, lds, nonce, QA0, QA1, QB0, QB1, ka0, ka1, va0, s0, kb, vb0, vb1, kvv, wid);
}

extern "C" void kernel_launch(void* const* d_in, const int* in_sizes, int n_in, void* d_out, int out_size, void* d_ws, size_t ws_size,
                              hipStream_t stream) {
  (void)hipFuncSetAttribute((const void*)fused_kernel, hipFuncAttributeMaxDynamicSharedMemorySize, LDS_BYTES);
  fused_kernel<<<256, 512, LDS_BYTES, stream>>>((const char*)d_in[0], (const char*)d_in[1], (const char*)d_in[2], (const char*)d_in[4], (const char*)d_in[5],
                                                (const char*)d_in[6], (char*)d_ws, (char*)d_out);
}
```

```cpp
#include <hip/hip_runtime.h>
#include <hip/hip_bf16.h>
#include <stdint.h>

typedef short bf16x8 __attribute__((ext_vector_type(8)));
typedef short s16x4 __attribute__((ext_vector_type(4)));
typedef float f32x16 __attribute__((ext_vector_type(16)));
typedef float f32x4 __attribute__((ext_vector_type(4)));
typedef float f32x2 __attribute__((ext_vector_type(2)));
typedef unsigned u32x4 __attribute__((ext_vector_type(4)));
typedef unsigned u32x2 __attribute__((ext_vector_type(2)));
typedef __bf16 bf2 __attribute__((ext_vector_type(2)));
typedef __amdgpu_buffer_rsrc_t srd_t;
#define LAS __attribute__((address_space(3)))
#define GAS __attribute__((address_space(1)))
typedef LAS char* lptr;
#define DI __device__ __forceinline__
#define AI __attribute__((always_inline))
#define MFMA(a, b, c) __builtin_amdgcn_mfma_f32_32x32x16_bf16(__builtin_bit_cast(bf16x8, (a)), __builtin_bit_cast(bf16x8, (b)), (c), 0, 0, 0)
#define PIN(x) asm volatile("" : "+v"(x))
#define SBAR() __builtin_amdgcn_sched_barrier(0)
#define RLX_AGENT __ATOMIC_RELAXED, __HIP_MEMORY_SCOPE_AGENT
#define EX(v) __builtin_amdgcn_exp2f(v)
#define F2U(x) __builtin_bit_cast(unsigned, (float)(x))
#define U2F(x) __builtin_bit_cast(float, (unsigned)(x))
#define ANY(p) (__builtin_amdgcn_ballot_w64(p) != 0)
#define ALL(p) (__builtin_amdgcn_ballot_w64(!(p)) == 0)
#define UNROLL _Pragma("unroll")
#define SRD(p, n) __builtin_amdgcn_make_buffer_rsrc((void*)(p), 0, (n), 0x00020000)

constexpr int WS_E = 0;
constexpr int WS_FQ = 4096;
constexpr int WS_FH = WS_FQ + 256 * 128;
constexpr int WS_FO = WS_FH + 256 * 128;
constexpr int WS_ST = WS_FO + 256 * 128;
constexpr int WS_QKV = 1 << 20;
constexpr int QT = 6144, QH = 64 * QT;
constexpr int WS_O = WS_QKV + 16 * QH;
constexpr int WS_H = WS_O + (2 << 20);
constexpr int WS_END = WS_H + 64 * 112 * 1024;
constexpr int OOB = 1 << 30;
constexpr int SC1 = 16;
constexpr unsigned SPIN_CAP = 1u << 17;

template <int I> struct IC { static constexpr int v = I; };
template <int B, int E, class F> DI void sfor(F&& f) { if constexpr (B < E) { f(IC<B>{}); sfor<B + 1, E>(f); } }
template <int N> DI void wait_bar() { asm volatile("s_waitcnt vmcnt(%0) lgkmcnt(0)\n\ts_barrier" :: "n"(N) : "memory"); }
template <int N> DI void vwait_bar() { asm volatile("s_waitcnt vmcnt(%0)\n\ts_barrier" :: "n"(N) : "memory"); }
DI void lbar() { asm volatile("s_waitcnt lgkmcnt(0)\n\ts_barrier" ::: "memory"); }
DI unsigned pk(float a, float b) { f32x2 v = {a, b}; return __builtin_bit_cast(unsigned, __builtin_convertvector(v, bf2)); }
DI float rbf(float a) { return U2F(pk(a, a) & 0xffff0000u); }
DI float dot2(unsigned a, unsigned b, float c) { return __builtin_amdgcn_fdot2_f32_bf16(__builtin_bit_cast(bf2, a), __builtin_bit_cast(bf2, b), c, false); }
DI u32x2 trd(lptr p) { return __builtin_bit_cast(u32x2, __builtin_amdgcn_ds_read_tr16_b64_v4i16((LAS s16x4*)p)); }
DI u32x4 cat(u32x2 a, u32x2 b) { return (u32x4){a[0], a[1], b[0], b[1]}; }
DI u32x4 ld128(lptr p) { return *(LAS u32x4*)p; }
DI unsigned bfi(unsigned m, unsigned a, unsigned b) { return (m & a) | (~m & b); }
template <int G, int L, bool DEV = false> DI void dma(srd_t srd, unsigned voff, unsigned lbase) {
  unsigned t;
  if constexpr (DEV)
    asm volatile("s_add_u32 m0, %3, %5\n\ts_mov_b32 %0, %4\n\tbuffer_load_dwordx4 %1, %2, %0 offen sc1 lds" : "=&s"(t) : "v"(voff), "s"(srd), "s"(lbase), "n"(G), "n"(L) : "memory", "scc");
  else
    asm volatile("s_add_u32 m0, %3, %5\n\ts_mov_b32 %0, %4\n\tbuffer_load_dwordx4 %1, %2, %0 offen lds" : "=&s"(t) : "v"(voff), "s"(srd), "s"(lbase), "n"(G), "n"(L) : "memory", "scc");
}
template <int L> DI void dma2(srd_t srd, unsigned voff0, unsigned voff1, unsigned lbase, unsigned ga, unsigned gb) {
  unsigned t;
  asm volatile("s_add_u32 m0, %4, %5\n\ts_mul_i32 %0, %6, %7\n\tbuffer_load_dwordx4 %1, %3, %0 offen lds\n\ts_add_u32 m0, m0, 1024\n\ts_nop 0\n\tbuffer_load_dwordx4 %2, %3, %0 offen lds"
               : "=&s"(t) : "v"(voff0), "v"(voff1), "s"(srd), "s"(lbase), "n"(L), "s"(ga), "s"(gb) : "memory", "scc");
}
template <int L> DI void dma1(srd_t srd, unsigned voff, unsigned lbase, unsigned ga, unsigned gb) {
  unsigned t;
  asm volatile("s_add_u32 m0, %3, %4\n\ts_mul_i32 %0, %5, %6\n\tbuffer_load_dwordx4 %1, %2, %0 offen lds" : "=&s"(t) : "v"(voff), "s"(srd), "s"(lbase), "n"(L), "s"(ga), "s"(gb) : "memory", "scc");
}
DI void touch(srd_t srd, unsigned voff, unsigned ldst) { asm volatile("s_mov_b32 m0, %2\n\ts_nop 0\n\tbuffer_load_dword %0, %1, 0 offen lds" :: "v"(voff), "s"(srd), "s"(ldst) : "memory"); }
DI void st128(u32x4 v, srd_t srd, int voff, int soff, int) { __builtin_amdgcn_raw_buffer_store_b128(v, srd, voff, soff, SC1); SBAR(); asm volatile("s_nop 1"); SBAR(); }
DI float swap32(float v, int h) {
  auto r = __builtin_amdgcn_permlane32_swap(F2U(v), F2U(v), false, false);
  return U2F(h ? r[0] : r[1]);
}
DI float max32(float v) {
  auto r = __builtin_amdgcn_permlane32_swap(F2U(v), F2U(v), false, false);
  return __builtin_fmaxf(U2F(r[0]), U2F(r[1]));
}
DI u32x4 widen(u32x2 a, u32x2 b) {
  auto r0 = __builtin_amdgcn_permlane32_swap(a[0], b[0], false, false); auto r1 = __builtin_amdgcn_permlane32_swap(a[1], b[1], false, false);
  return (u32x4){r0[0], r1[0], r0[1], r1[1]};
}
DI float wave_sum(float v) {
  v += U2F(__builtin_amdgcn_update_dpp(0u, F2U(v), 0x111, 0xf, 0xf, true));
  v += U2F(__builtin_amdgcn_update_dpp(0u, F2U(v), 0x112, 0xf, 0xf, true));
  v += U2F(__builtin_amdgcn_update_dpp(0u, F2U(v), 0x114, 0xf, 0xf, true));
  v += U2F(__builtin_amdgcn_update_dpp(0u, F2U(v), 0x118, 0xf, 0xf, true));
  v += U2F(__builtin_amdgcn_update_dpp(0u, F2U(v), 0x142, 0xa, 0xf, true));
  v += U2F(__builtin_amdgcn_update_dpp(0u, F2U(v), 0x143, 0xc, 0xf, true));
  return v;
}
DI unsigned flag_ld(const char* ws, int off) { return __hip_atomic_load((GAS unsigned*)(ws + off), RLX_AGENT); }
DI void flag_st(char* ws, int off, unsigned v) {
  if (__builtin_amdgcn_mbcnt_hi(~0u, __builtin_amdgcn_mbcnt_lo(~0u, 0u)) == 0) __hip_atomic_store((GAS unsigned*)(ws + off), v, RLX_AGENT); }

constexpr int RA = 2, NWF = RA + 1;
constexpr int PART = 14336;
constexpr int L_W = 65536;
constexpr int L_CS = 9 * 16384;
constexpr int L_PF = L_CS + 2048;
constexpr int L_Q = 65536;
constexpr int wpos(int m) { return m < 9 ? L_W + (m % 5) * 16384 : m == 9 ? 49152 : m == 10 ? L_W : m < 14 ? (m - 11) * 16384 : L_W + 16384 + (m - 14) * 8192; }
constexpr int FQP = 52, FQC = 59;
constexpr int L_WD = 65536;
constexpr int LDS_BYTES = 163840;
constexpr unsigned ONES2 = 0x3f803f80u, PAD0 = 0x00003f80u;
static_assert(L_PF + 2048 <= LDS_BYTES, "LDS map");

constexpr int DN0 = 7;
constexpr int kv_at(int s, int p) { return p == 0 && s > 0 && s + 3 < 16 ? (s + 3) * 2 + 1 : p == 7 && s + 4 < 16 ? (s + 4) * 2 : -1; }
constexpr int wd_at(int s, int p) {
  if (s >= 1 && s <= 3) return p == 1 ? s * 4 - 4 : p == 3 ? s * 4 - 3 : p == 4 ? s * 4 - 2 : p == 5 ? s * 4 - 1 : -1;
  for (int g = 0; g < 2; ++g) { if (s == 10 + 4 * g && p == 7) return 12 + 4 * g; if (s == 11 + 4 * g) return p == 0 ? 13 + 4 * g : p == 2 ? 14 + 4 * g : p == 5 ? 15 + 4 * g : -1; }
  return -1;
}
constexpr int h_at(int s, int p) { const int z = (s - 5) * 3 + (p == 1 ? 1 : p == 3 ? 2 : 3); if (s == 15) return p == 1 ? 24 : p == 3 ? 25 : p == 4 ? 26 : p == 7 ? 27 : -1;
  return s == 4 && p == 7 ? 0 : s >= 5 && (p == 1 || p == 3 || p == 4) && z < 24 ? z : -1; }
constexpr bool pf_at(int s, int p) { return s == 4 && p == 1; }
constexpr int n_at(int s, int p) { return (kv_at(s, p) >= 0) + (wd_at(s, p) >= 0) + (h_at(s, p) >= 0) + pf_at(s, p); }
constexpr int cnt_after(int s0, int p0, int s) { int n = 0; for (int z = s0 * 8 + p0 + 1; z < s * 8 + 6; ++z) n += n_at(z / 8, z % 8); return n; }
constexpr int dslot(int sub) { return L_WD + (sub % 3) * 32768; }

struct QkvTab { unsigned v[44][2][4]; };
constexpr QkvTab make_tab() {
  QkvTab t = {};
  for (int tile = 0; tile < 44; ++tile) for (int h = 0; h < 2; ++h) {
    const int col0 = tile * 32, sec = col0 / 448; unsigned* e = t.v[tile][h];
    e[0] = e[1] = e[2] = OOB; e[3] = 0;
    if (tile < 42) for (int pp = 0; pp < 2; ++pp) {
      const int cu = (col0 - sec * 448) / 8 + 2 * pp + h, j = cu / 7, w = cu % 7, hd = 2 * j + (w > 3), ch = w > 3 ? w - 3 : w;
      e[pp] = WS_QKV + sec * 2048 + hd * QH + ch * 512 + (sec == 2 ? ch * 64 : 0);
      if (w == 3) { e[2] = WS_QKV + sec * 2048 + (hd + 1) * QH; e[3] |= 1u << pp; }
    }
  }
  return t;
}
__device__ const QkvTab qkv_tab = make_tab();

#define LANE_IDS() int lane; asm volatile("v_mbcnt_lo_u32_b32 %0, -1, 0\n\tv_mbcnt_hi_u32_b32 %0, -1, %0" : "=v"(lane));        \
  const int r = lane & 31, h = lane >> 5, q = (lane & 15) >> 2, p = lane & 3, grp = (lane >> 4) & 1, rr = lane >> 3, cc = lane & 7
DI void phase23(const char* x, const char* wo, const char* wd, char* ws, char* out, lptr lds, unsigned nonce, u32x4 QA0, u32x4 QA1, u32x4 QB0, u32x4 QB1,
                u32x4 k0, u32x4 k1, u32x4 v0, f32x16 sa, lptr kb, lptr vb0, lptr vb1, unsigned kvv, int wid) {
  const unsigned lds0 = (unsigned)(uintptr_t)lds;
  const int b = __builtin_amdgcn_workgroup_id_x(), xcd = b & 7, idx = b >> 3;
  const int ng = (xcd & 3) * 4 + (idx & 3), mg = (xcd >> 2) * 8 + (idx >> 2);
  const srd_t wsr = SRD(ws, WS_END);
  const f32x16 Z = {};
  const int head = 2 * xcd + (idx & 1), qg = idx >> 1, odd = head & 1;
  const int idx3 = idx < 28 ? idx : idx - 28, mq3 = (idx3 * 37) >> 8, mg3 = xcd * 4 + mq3, ng3 = idx3 - mq3 * 7;
  LANE_IDS();
  const unsigned lw2k = lds0 + wid * 2048;
  auto dma_kv = [&](auto zc) AI { constexpr int z = decltype(zc)::v; dma<(z >> 1) * QT + (z & 1) * 1024, ((z >> 1) & 3) * 16384 + (z & 1) * 1024, true>(wsr, kvv, lw2k); };
  const int w3m = wid & 1, w3k = wid >> 1;
  u32x4 hf[28];
  const int ft = w3k & 1, fs = w3k >> 1;
  unsigned vo_d = 0; const unsigned ld_d = lds0 + wid * 4096;
  auto set_vo_d = [&]() AI { vo_d = (unsigned)(((idx < 28 ? w3k * 448 + w3m * 32 : OOB / 896) + rr) * 896 + ((cc ^ (grp << 2)) << 4) + ng3 * 128); PIN(vo_d); };
  const srd_t sD = SRD(wd, 1792 * 896);
  auto dma_d = [&](auto zc) AI { constexpr int z = decltype(zc)::v; dma<((z >> 2) * 64 + (z & 3) * 8) * 896, dslot(z >> 2) + (z & 3) * 1024>(sD, vo_d, ld_d); };
  int db0 = 0, db1 = 0, dbo = L_WD;
  auto set_db = [&]() AI { db0 = L_WD + w3k * 8192 + (4 * h + q) * 128 + p * 8 + ((grp ^ ((q >> 1) << 1)) << 5); db1 = db0 ^ 64; PIN(db0); PIN(db1); };
  auto rebase = [&](int o) AI { db0 += o - dbo; db1 += o - dbo; dbo = o; PIN(db0); PIN(db1); };
  f32x16 F0 = Z, F1 = Z;
  u32x4 dw0, dw1;
  auto rd_d0 = [&](int d) AI { const int o = dslot(d >> 2) + (d & 3) * 2048 - dbo; dw0 = cat(trd(lds + db0 + o), trd(lds + db0 + o + 1024)); };
  auto rd_d1 = [&](int d) AI { const int o = dslot(d >> 2) + (d & 3) * 2048 - dbo; dw1 = cat(trd(lds + db1 + o), trd(lds + db1 + o + 1024)); };
  {
    const int wq = wid & 1, kq = wid >> 1;
    u32x4 v1, pa, pb;
    float e0, e1, e2, e3;
    f32x16 OA = Z, OB = Z, sb;
    unsigned junk, fh = ~nonce; const int fho = WS_FH + ((mg3 >> 1) * 16 + (lane & 15)) * 128;
    const int hld = WS_H + ((mg3 * 2 + w3m) * 112 + w3k * 28) * 1024, hlv = idx < 28 ? lane * 16 : OOB;
    auto send = [&](const f32x16& a, int hh, int j) AI {
      const lptr d = kb + (((wid ^ (2 * j)) * 6144 + (j - 1) * 2048) - kq * 4096);
      *(LAS f32x4*)d = (f32x4){a[8 * hh], a[8 * hh + 1], a[8 * hh + 2], a[8 * hh + 3]}; *(LAS f32x4*)(d + 1024) = (f32x4){a[8 * hh + 4], a[8 * hh + 5], a[8 * hh + 6], a[8 * hh + 7]}; };
#define EX2(S, i, a, c) do { a = EX(S[i]); c = EX(S[(i) + 1]); PIN(a); PIN(c); } while (0)
#define STEP(FIRST, SC, SN, QN0, QN1, OP, OC, L0, L1, L2, L3, X0, X1) do {                                                       \
      SBAR(); SN = MFMA(k0, QN0, Z); SBAR(); L0;                                                                          \
      if (!(FIRST)) { pb[2] = pk(e0, e1); pb[3] = pk(e2, e3); PIN(pb); } SBAR(); EX2(SC, 0, e0, e1); SBAR(); X0; SBAR(); EX2(SC, 2, e2, e3); \
      SBAR(); if (!(FIRST)) OP = MFMA(v1, pb, OP); SBAR(); L1;                                                                          \
      pa[0] = pk(e0, e1); pa[1] = pk(e2, e3); PIN(pa); SBAR(); EX2(SC, 4, e0, e1); EX2(SC, 6, e2, e3);                    \
      SBAR(); SN = MFMA(k1, QN1, SN); SBAR(); L2;                                                                         \
      pa[2] = pk(e0, e1); pa[3] = pk(e2, e3); PIN(pa); SBAR(); EX2(SC, 8, e0, e1); SBAR(); X1; SBAR(); EX2(SC, 10, e2, e3); \
      SBAR(); OC = MFMA(v0, pa, OC); SBAR(); L3;                                                                          \
      pb[0] = pk(e0, e1); pb[1] = pk(e2, e3); PIN(pb); SBAR(); EX2(SC, 12, e0, e1); EX2(SC, 14, e2, e3); SBAR(); } while (0)
    sfor<0, 16>([&](auto sc_) AI {
      constexpr int s = decltype(sc_)::v, so = (s & 3) * 16384, sn = ((s + 1) & 3) * 16384;
      auto side = [&](auto pc) AI {
        constexpr int P = decltype(pc)::v, kv = kv_at(s, P), wz = wd_at(s, P), hz = h_at(s, P);
        if constexpr (s == 0 && P == 3) set_vo_d();
        if constexpr (s == 0 && P == 5) set_db();
        if constexpr (kv >= 0) dma_kv(IC<kv>{});
        if constexpr (wz >= 0) dma_d(IC<wz>{});
        if constexpr (pf_at(s, P)) {
          const int row = wid < 2 ? mq3 * 448 + 320 + wid * 64 + lane : mq3 * 112 + (wid - 2) * 56 + (lane < 56 ? lane : 55);
          asm volatile("buffer_load_dword %0, %1, %2, 0 offen" : "=&v"(junk) : "v"(wid < 4 && idx < 28 ? row * 896 + ng3 * 128 : OOB), "s"(SRD(wid < 2 ? wd : wo, wid < 2 ? 1792 * 896 : 448 * 896)) : "memory");
        }
        if constexpr (hz >= 0) hf[hz] = __builtin_amdgcn_raw_buffer_load_b128(wsr, hlv + (hz & 3) * 1024, hld + (hz >> 2) * 4096, SC1);
      };
      auto mid = [&]() AI {
        if (s == 4 && wid == 0) {
          asm volatile("s_waitcnt vmcnt(%1)" : "+v"(fh) : "n"(cnt_after(3, 6, 4)) : "memory");
          _Pragma("nounroll") for (unsigned sp = 0; !ALL(fh == nonce) && sp < SPIN_CAP; ++sp) { fh = flag_ld(ws, fho); PIN(fh); }
        }
        if constexpr (s == 0) vwait_bar<5 + cnt_after(0, -1, 0)>();
        else if constexpr (s == 1) vwait_bar<cnt_after(0, -1, 1)>();
        else if constexpr (s == 14) vwait_bar<0>(); else if constexpr (s < 14) vwait_bar<cnt_after(s - 1, 0, s)>();
        if (s == 1 && wid == 0) flag_st(ws, WS_FH + (mg * 16 + ng) * 128, nonce);
        if (s == 3 && wid == 0) asm volatile("buffer_load_dword %0, %1, %2, 0 offen sc1" : "+v"(fh) : "v"(fho), "s"(wsr) : "memory");
        if constexpr (s == 6) asm volatile("" :: "v"(junk));
        if constexpr (s == 13) rebase(L_WD + 65536);
        if constexpr (s == 14) { UNROLL for (int k = 23; k >= 8; --k) PIN(hf[k]); }
        __builtin_amdgcn_s_setprio(3);
      };
      auto dn0 = [&](int d) AI { if (d >= 0) { F0 = MFMA(dw0, hf[d], F0); PIN(F0); } if (d >= -1) rd_d0(d + 1); };
      auto dn1 = [&](int d) AI { if (d >= 0) { F1 = MFMA(dw1, hf[d], F1); PIN(F1); } if (d >= -1) rd_d1(d + 1); };
      STEP(s == 0, sa, sb, QB0, QB1, OB, OA,
           k0 = ld128(kb + sn); side(IC<0>{}); __builtin_amdgcn_s_setprio(2),
           v1 = cat(trd(vb0 + so + 256), trd(vb1 + so + 256)); side(IC<1>{}),
           k1 = ld128(kb + sn + 1024); side(IC<2>{}); __builtin_amdgcn_s_setprio(1),
           side(IC<3>{}), dn0(s - DN0), if (s == 15) dn1(8));
      STEP(false, sb, sa, QA0, QA1, OA, OB,
           __builtin_amdgcn_s_setprio(0); side(IC<4>{}),
           side(IC<5>{}),
           mid(); if (s == 15) send(OA, 0, 1),
           v0 = cat(trd(vb0 + sn), trd(vb1 + sn)); side(IC<7>{}); if (s == 15) send(OA, 1, 3), if (s < 15) dn1(s - DN0); else dn0(9), if (s == 15) dn1(9));
    });
    pb[2] = pk(e0, e1); pb[3] = pk(e2, e3);
    OB = MFMA(v1, pb, OB);
    __builtin_amdgcn_s_setprio(0);
    send(OB, 1, 2);
    *(LAS float*)(kb + (49152 + wq * 1024)) = odd ? OB[0] : OB[4];
    {
    LANE_IDS();
    const bool padl = h != odd;
    const int qt = qg * 4 + wq * 2 + ft, lw = (wid & 3) | (odd ? 0 : 4);
    const lptr pp = lds + wid * 6144 + lane * 16, lp = lds + lw * 6144 + (odd ? 0 : 1536) + r * 16;
    const lptr lq = lds + 49152 + (lw >> 1) * 4096 + wq * 1024 + (odd ? 0 : 512) + r * 16;
    f32x4 o0 = {OB[0], OB[1], OB[2], OB[3]}, o1 = {OB[4], OB[5], OB[6], OB[7]};
    lbar();
    const float l0 = *(LAS float*)lq, l1 = *(LAS float*)lp, l2 = *(LAS float*)(lp + 2048), l3 = *(LAS float*)(lp + 4096);
    f32x4 t[6];
    UNROLL for (int k = 0; k < 6; ++k) t[k] = *(LAS f32x4*)(pp + k * 1024);
    SBAR();
    float l = (l0 + l1) + (l2 + l3);
    UNROLL for (int k = 0; k < 3; ++k) { o0 += t[2 * k]; o1 += t[2 * k + 1]; }
    if (__builtin_expect(ANY(!(l <= 1.8446744e19f && l >= 5.4210109e-20f)), 0)) {
      const int qo = WS_QKV + head * QH + qt * QT + lane * 16;
      u32x4 q0 = __builtin_amdgcn_raw_buffer_load_b128(wsr, qo, 0, SC1), q1 = __builtin_amdgcn_raw_buffer_load_b128(wsr, qo + 1024, 0, SC1);
      f32x16 O = Z; float m = -__builtin_inff();
      _Pragma("nounroll") for (int t = 0; t < 64; ++t) {
        const int ko = WS_QKV + head * QH + t * QT + 2048;
        f32x16 s = MFMA(__builtin_amdgcn_raw_buffer_load_b128(wsr, ko + lane * 16, 0, SC1), q0, Z);
        s = MFMA(__builtin_amdgcn_raw_buffer_load_b128(wsr, ko + 1024 + lane * 16, 0, SC1), q1, s);
        float mn = m;
        UNROLL for (int i = 0; i < 16; ++i) mn = __builtin_fmaxf(mn, s[i]);
        mn = max32(mn);
        const float f = EX(m - mn); m = mn;
        UNROLL for (int i = 0; i < 16; ++i) O[i] *= f;
        UNROLL for (int st = 0; st < 2; ++st) {
          unsigned vv[8];
          UNROLL for (int j = 0; j < 8; ++j) vv[j] = __builtin_amdgcn_raw_buffer_load_b16(wsr, ko + 2048 + (r >> 3) * 512 + (((16 * st + 8 * (j >> 2) + 4 * h + (j & 3)) ^ (4 * (r >> 3))) << 4) + (r & 7) * 2, 0, SC1) & 0xffffu;
          O = MFMA(((u32x4){vv[0] | vv[1] << 16, vv[2] | vv[3] << 16, vv[4] | vv[5] << 16, vv[6] | vv[7] << 16}),
                   ((u32x4){pk(EX(s[8 * st] - m), EX(s[8 * st + 1] - m)), pk(EX(s[8 * st + 2] - m), EX(s[8 * st + 3] - m)),
                            pk(EX(s[8 * st + 4] - m), EX(s[8 * st + 5] - m)), pk(EX(s[8 * st + 6] - m), EX(s[8 * st + 7] - m))}), O);
        }
      }
      const float lc = odd ? O[0] : O[12], lx = swap32(lc, h); l = padl ? lc : lx;
      o0 = fs ? (f32x4){O[8], O[9], O[10], O[11]} : (f32x4){O[0], O[1], O[2], O[3]};
      o1 = fs ? (f32x4){O[12], O[13], O[14], O[15]} : (f32x4){O[4], O[5], O[6], O[7]};
    }
    const float ri = __builtin_amdgcn_rcpf(l);
    const float r0 = padl && odd && fs == 0 ? 0.f : ri, r1 = padl && !odd && fs == 1 ? 0.f : ri;
    st128((u32x4){pk(o0[0] * r0, o0[1] * r0), pk(o0[2] * r0, o0[3] * r0), pk(o1[0] * r1, o1[1] * r1), pk(o1[2] * r1, o1[3] * r1)},
                                           wsr, lane * 16, WS_O + (qt * 16 + head) * 2048 + fs * 1024, SC1);
    }
  }
  if (idx >= 28) { wait_bar<0>();
    if (wid == 0) {
      flag_st(ws, WS_FO + (qg * 16 + head) * 128, nonce);
      if (b == 255) {
        LANE_IDS();
        _Pragma("nounroll") for (unsigned sp = 0; sp < SPIN_CAP; ++sp) {
          unsigned d = 0;
          UNROLL for (int i = 0; i < 4; ++i) d |= flag_ld(ws, WS_FQ + lane * 512 + i * 128) ^ nonce;
          if (ALL(d == 0)) break;
        }
        flag_st(ws, WS_E, nonce);
      }
    }
    return;
  }
  {
    lbar();
    const srd_t sO = SRD(wo, 448 * 896); const unsigned wov = vo_d + (wid * 56 - w3k * 448 - w3m * 32) * 896, wol = lds0 + wid * 7168;
    auto dma_o = [&](auto ic) AI { constexpr int i = decltype(ic)::v; dma<i * 8 * 896, i * 1024>(sO, wov, wol); };
    sfor<0, 4>(dma_o);
    u32x4 ew0, ew1;
    auto down2 = [&](int d, bool more) AI {
      const int o = dslot((d + 1) >> 2) + ((d + 1) & 3) * 2048 - dbo;
      ew0 = cat(trd(lds + db0 + o), trd(lds + db0 + o + 1024)); ew1 = cat(trd(lds + db1 + o), trd(lds + db1 + o + 1024));
      if (d == 10 || d == 22) rebase(L_WD);
      SBAR(); F0 = MFMA(dw0, hf[d], F0); F1 = MFMA(dw1, hf[d], F1); PIN(F0); PIN(F1); SBAR();
      if (more) { rd_d0(d + 2); rd_d1(d + 2); }
      SBAR(); F0 = MFMA(ew0, hf[d + 1], F0); F1 = MFMA(ew1, hf[d + 1], F1); PIN(F0); PIN(F1); SBAR();
    };
    static_assert(DN0 == 7, "units 0..9 ran beside the attention");
    down2(10, true); sfor<4, 7>(dma_o);
    vwait_bar<7>();
    if (wid == 0) flag_st(ws, WS_FO + (qg * 16 + head) * 128, nonce);
    sfor<20, 22>(dma_d); down2(12, true); sfor<22, 24>(dma_d); down2(14, true);
    vwait_bar<4>();
    LANE_IDS();
    const int mt = mg3 * 2 + w3m, orot = ng3 & 1;
    unsigned fo = ~nonce; const int foo = WS_FO + ((mg3 >> 1) * 16 + (lane & 15)) * 128;
    sfor<24, 26>(dma_d); down2(16, true); sfor<26, 28>(dma_d);
    if (wid == 0) asm volatile("buffer_load_dword %0, %1, %2, 0 offen sc1" : "+v"(fo) : "v"(foo), "s"(wsr) : "memory");
    down2(18, false);
    u32x4 wo0[8], wo1[8];
    UNROLL for (int k = 0; k < 8; ++k) {
      const int wbo = w3k * (PART - 8192) - L_WD + ((k >> 2) ^ orot) * 7168, o = (((k >> 1) & 1) * 24 + (k & 1) * 16) * 128;
      const lptr wb0 = lds + db0 + wbo, wb1 = lds + db1 + wbo;
      wo0[k] = cat(trd(wb0 + o), trd(wb0 + o + 1024)); wo1[k] = cat(trd(wb1 + o), trd(wb1 + o + 1024));
    }
    rebase(L_WD + 65536);
    if (wid == 0) {
      asm volatile("s_waitcnt vmcnt(0)" : "+v"(fo) :: "memory");
      _Pragma("nounroll") for (unsigned sp = 0; !ALL(fo == nonce) && sp < SPIN_CAP; ++sp) { fo = flag_ld(ws, foo); PIN(fo); }
    }
    wait_bar<0>();
    UNROLL for (int k = 27; k >= 24; --k) PIN(hf[k]);
    u32x4 of[8];
    const int oso = WS_O + (mt * 16 + w3k * 4) * 2048 + orot * 4096;
    UNROLL for (int k = 0; k < 4; ++k) of[k] = __builtin_amdgcn_raw_buffer_load_b128(wsr, lane * 16 + k * 1024, oso, SC1);
    SBAR();
    rd_d0(20); rd_d1(20); down2(20, true); down2(22, true);
    UNROLL for (int k = 4; k < 8; ++k) of[k] = __builtin_amdgcn_raw_buffer_load_b128(wsr, lane * 16 + (k & 3) * 1024, oso ^ 4096, SC1);
    const unsigned xo = (unsigned)((mt * 32 + w3k * 8 + rr) * 896 + ng3 * 128 + cc * 16);
    const u32x4 xv = *(const u32x4*)(x + xo);
    SBAR();
    down2(24, true); down2(26, false);
    auto xch_send = [&](const f32x16& a0, const f32x16& a1, int base) AI {
      UNROLL for (int c = 0; c < 8; ++c) { const f32x16& a = c < 4 ? a0 : a1; const int o = (c & 3) * 4;
        *(LAS f32x4*)(lds + (base + wid * 8192 + c * 1024) + ((lane ^ (2 * c)) << 4)) = (f32x4){a[o], a[o + 1], a[o + 2], a[o + 3]}; } };
    int xro = w3m * 8192 + cc * 1024 + (((w3k * 8 + rr) ^ (2 * cc)) << 4); PIN(xro);
    xch_send(F0, F1, 0);
    f32x16 S0 = Z, S1 = Z;
    UNROLL for (int k = 0; k < 4; ++k) { S0 = MFMA(wo0[k], of[k], S0); S1 = MFMA(wo1[k], of[k], S1); }
    PIN(S0); PIN(S1);
    lbar();
    f32x4 g0 = *(LAS f32x4*)(lds + xro), g1 = *(LAS f32x4*)(lds + xro + 512);
    UNROLL for (int k = 1; k < 4; ++k) { g0 += *(LAS f32x4*)(lds + xro + k * 16384); g1 += *(LAS f32x4*)(lds + xro + k * 16384 + 512); }
    UNROLL for (int k = 4; k < 8; ++k) { S0 = MFMA(wo0[k], of[k], S0); S1 = MFMA(wo1[k], of[k], S1); }
    UNROLL for (int i = 0; i < 4; ++i) {
      g0[i] = g0[i] * __builtin_amdgcn_rcpf(1.f + EX(g0[i] * -1.4426950408889634f)); g1[i] = g1[i] * __builtin_amdgcn_rcpf(1.f + EX(g1[i] * -1.4426950408889634f)); }
    xch_send(S0, S1, L_WD);
    f32x4 y0 = {U2F(xv[0] << 16), U2F(xv[0] & 0xffff0000u), U2F(xv[1] << 16), U2F(xv[1] & 0xffff0000u)}, y1 = {U2F(xv[2] << 16), U2F(xv[2] & 0xffff0000u), U2F(xv[3] << 16), U2F(xv[3] & 0xffff0000u)};
    xro += L_WD;
    PIN(g0); PIN(g1); PIN(y0); PIN(y1); PIN(xro);
    lbar();
    f32x4 t[8];
    UNROLL for (int k = 0; k < 8; ++k) t[k] = *(LAS f32x4*)(lds + xro + (k >> 1) * 16384 + (k & 1) * 512);
    SBAR();
    UNROLL for (int k = 0; k < 4; ++k) UNROLL for (int i = 0; i < 4; ++i) { y0[i] = __builtin_fmaf(t[2 * k][i], g0[i], y0[i]); y1[i] = __builtin_fmaf(t[2 * k + 1][i], g1[i], y1[i]); }
    __builtin_amdgcn_raw_buffer_store_b128((u32x4){pk(y0[0], y0[1]), pk(y0[2], y0[3]), pk(y1[0], y1[1]), pk(y1[2], y1[3])}, SRD(out, 2048 * 896), xo, 0, SC1);
  }
}

#if defined(__HIP_DEVICE_COMPILE__)
#define NOPK __attribute__((target("no-packed-fp32-ops")))
#else
#define NOPK
#endif
__global__ __launch_bounds__(512, 2) NOPK void fused_kernel(const char* x, const char* wqkv, const char* wo, const char* wg, const char* wu, const char* wd,
                                                       char* ws, char* out) {
  extern __shared__ __attribute__((aligned(16))) char smem[];
  const lptr lds = (lptr)smem;
  const unsigned lds0 = (unsigned)(uintptr_t)lds;
  const int tid = __builtin_amdgcn_workitem_id_x(), lane = tid & 63, wid = __builtin_amdgcn_readfirstlane(tid >> 6);
  const int rr = lane >> 3, cc = lane & 7;
  const int b = __builtin_amdgcn_workgroup_id_x(), xcd = b & 7, idx = b >> 3;
  const int ng = (xcd & 3) * 4 + (idx & 3), mg = (xcd >> 2) * 8 + (idx >> 2);
  {
    unsigned long long pc; asm volatile("s_getpc_b64 %0" : "=s"(pc));
    touch(SRD(pc & ~127ull, 28672), (idx * 8 + (tid >> 6)) * 128, lds0 + L_PF);
  }
  const int rot = (mg & 7) == 7 ? 3 : mg & 7;
  unsigned kc[7];
  UNROLL for (int c = 0; c < 7; ++c) kc[c] = (unsigned)((c + rot >= 7 ? c + rot - 7 : c + rot) * 64);
  const srd_t xr = SRD(x, 2048 * 896);
  const unsigned xv0 = (unsigned)((mg * 128 + wid * 16 + rr) * 896 + ((cc ^ rr) << 4));
  const unsigned xv1 = (unsigned)((mg * 128 + wid * 16 + 8 + (rr ^ 1)) * 896 + ((cc ^ rr ^ 1) << 4));
  const unsigned lw2k = lds0 + wid * 2048;
  auto dma_x = [&](auto cc_) AI { constexpr int c = decltype(cc_)::v; dma2<(c & 3) * 16384>(xr, xv0, xv1, lw2k, kc[c], 2); };
  const srd_t wsr = SRD(ws, WS_END);
  u32x4 srow; unsigned nonce;
  const int srw = mg * 128 + ng * 8 + wid;
  asm volatile("buffer_load_dwordx4 %0, %1, %2, %3 offen" : "=&v"(srow) : "v"(lane * 16), "s"(SRD(x, srw * 896 + 896)), "s"(srw * 896) : "memory");
  dma_x(IC<0>{});
  SBAR();
  const int r = lane & 31, h = lane >> 5, q = (lane & 15) >> 2, p = lane & 3, grp = (lane >> 4) & 1;
  const f32x16 Z = {};
  const int head = 2 * xcd + (idx & 1), qg = idx >> 1;
  auto dma_kv = [&](auto zc, unsigned kvv) AI { constexpr int z = decltype(zc)::v; dma<(z >> 1) * QT + (z & 1) * 1024, ((z >> 1) & 3) * 16384 + (z & 1) * 1024, true>(wsr, kvv, lw2k); };
  u32x4 QA0, QA1, QB0, QB1, kf0, kf1, ka0, ka1, va0; f32x16 s0; lptr kb, vb0, vb1; unsigned kvv = 0;
  {
    const int wm = wid & 3, wn = wid >> 2;
    const bool bq = ng <= 10;
    const unsigned st1 = bq ? 2688 : 3584;
    unsigned va = (16 * wm + rr) * st1 + (((cc << 4) ^ (grp << (bq ? 6 : 5))) ^ (bq ? 0 : wn << 6)) + (bq ? 2 * ng + wn : ng + 12) * 128, vb = va + 8 * st1;
    const char* wgu = wn ? wu : wg;
    const srd_t s2 = SRD(wgu, 448 * 3584), s1 = SRD(bq ? wqkv : wgu, 448 * st1);
    auto dma_w = [&](auto mc) AI {
      constexpr int m = decltype(mc)::v;
      if constexpr (m == 7) { va = (unsigned)((16 * wm + rr) * 3584 + ((cc ^ (grp << 1) ^ (wn << 2)) << 4)) + ng * 128; vb = va + 8 * 3584; PIN(va); PIN(vb); }
      if constexpr (m == 14) {
        va = (unsigned)((16 * wm + (lane >> 2)) * 3584 + (((lane & 3) ^ (wn << 1)) << 4)) + 2048 + ng * 64;
        if (ng >= 14) va = OOB;
        PIN(va);
      }
      if constexpr (m < 7) dma2<wpos(m)>(s1, va, vb, lw2k, kc[m], st1);
      else if constexpr (m < 14) dma2<wpos(m)>(s2, va, vb, lw2k, kc[m - 7], 3584);
      else dma1<wpos(m)>(s2, va, lds0 + wid * 1024, kc[m - 14], 3584);
    };
    dma_w(IC<0>{});
    unsigned epoch;
    asm volatile("buffer_load_dword %0, off, %1, 0 sc1" : "=&v"(epoch) : "s"(wsr) : "memory");
    dma_x(IC<1>{});
    SBAR();
    const int rowb = (8 * h + q) * 128 + p * 8;
    int b20 = grp * 8192 + rowb + (((2 * wn) ^ (q >> 1) ^ (grp << 1)) << 5);
    int b10 = bq ? wn * 8192 + rowb + ((grp ^ ((q >> 1) << 1)) << 5) : b20, b11 = b10 ^ (bq ? 64 : 32);
    int xb[4];
    UNROLL for (int i = 0; i < 4; ++i) { xb[i] = (4 * wm + (r >> 3)) * 1024 + ((r & 7) ^ ((r >> 3) & 1)) * 128 + (((2 * i + h) ^ (r & 7)) << 4); PIN(xb[i]); }
    PIN(b10); PIN(b11); PIN(b20);
    UNROLL for (int c = 2; c < 7; ++c) asm volatile("" : "+s"(kc[c]));
    SBAR();
    u32x4 xf[28];
    auto row_stats = [&]() AI {
      float a = dot2(srow[0], ONES2, 0.f), c = dot2(srow[0], srow[0], 0.f);
      UNROLL for (int i = 1; i < 4; ++i) { a = dot2(srow[i], ONES2, a); c = dot2(srow[i], srow[i], c); }
      a = wave_sum(a); c = wave_sum(c);
      const float mean = a * (1.f / 448.f), var = __builtin_fmaxf(c * (1.f / 448.f) - mean * mean, 0.f);
      unsigned g = pk(-mean, __builtin_amdgcn_sqrtf(rbf(rbf(var) + 1e-5f))); PIN(g); SBAR();
      asm volatile("s_waitcnt vmcnt(4)" : "+v"(epoch) :: "memory");
      nonce = __builtin_amdgcn_readfirstlane(epoch) * 0x9E3779B1u + 0x7F4A7C15u;
      if (lane == 63) __hip_atomic_store((GAS unsigned long long*)(ws + WS_ST + srw * 8), (unsigned long long)nonce << 32 | g, RLX_AGENT);
    };
    auto rd_x = [&](int k0, int k1) AI { UNROLL for (int ks = k0; ks < k1; ++ks) xf[ks] = ld128(lds + xb[ks & 3] + ((ks >> 2) & 3) * 16384); };
    u32x2 wf[NWF][4];
    int ra0 = 0, ra1 = 0;
    auto rd_w = [&](int n) AI {
      if (n < 56) {
        const int m = n >> 2, o = (n & 3) * 2048;
        if ((n & 3) == 0) { ra0 = (m < 7 ? b10 : b20) + wpos(m); ra1 = (m < 7 ? b11 : b20 ^ 32) + wpos(m); PIN(ra0); PIN(ra1); }
        wf[n % NWF][0] = trd(lds + ra0 + o); wf[n % NWF][1] = trd(lds + ra0 + o + 512); wf[n % NWF][2] = trd(lds + ra1 + o); wf[n % NWF][3] = trd(lds + ra1 + o + 512);
      } else {
        const int o = ((n - 56) >> 1) * 8192 + (n & 1) * 2048;
        if (n == 56) { ra0 = wpos(14) + grp * 4096 + (8 * h + q) * 64 + ((wn ^ grp) << 5) + p * 8; PIN(ra0); }
        wf[n % NWF][0] = trd(lds + ra0 + o); wf[n % NWF][1] = trd(lds + ra0 + o + 256); wf[n % NWF][2] = trd(lds + ra0 + o + 1024); wf[n % NWF][3] = trd(lds + ra0 + o + 1280);
      }
    };
    SBAR();
    dma_w(IC<1>{});
    asm volatile("s_waitcnt vmcnt(9)" : "+v"(srow) :: "memory");
    row_stats();
    SBAR();
    lbar();
    UNROLL for (int n = 0; n < RA; ++n) rd_w(n);
    rd_x(0, 4);
    dma_x(IC<2>{});
    f32x16 A0 = Z, A1 = Z, B0 = Z, B1 = Z, C0 = Z;
    float cs0 = 0.f, cs1 = 0.f, rstd = 0.f, sig = 0.f, kg = 0.f; unsigned mm = 0; u32x2 sg = {0u, ~nonce};
    const int mt = mg * 4 + wm;
    const unsigned csel = ((wm & 1) ? 0x05040c0cu : 0x01000c0cu) + ((wm >> 1) ? 0x02020000u : 0u);
    auto colsum = [&](int n, int hh) AI {
      cs0 += U2F(__builtin_amdgcn_perm(wf[n % NWF][2 + hh][0], wf[n % NWF][hh][0], csel)); cs1 += U2F(__builtin_amdgcn_perm(wf[n % NWF][2 + hh][1], wf[n % NWF][hh][1], csel));
      PIN(cs0); PIN(cs1);
    };
    auto cs_out = [&]() AI {
      const float cs = cs0 + cs1, hi = rbf(cs); *(LAS unsigned*)(lds + L_CS + wn * 1024 + lane * 16 + ((wm & 1) * 2 + (wm >> 1)) * 4) = pk(hi, cs - hi); cs0 = 0.f; cs1 = 0.f; };
    auto fix2 = [&](f32x16& a0, f32x16& a1) AI {
      const u32x4 c = ld128(lds + L_CS + wn * 1024 + lane * 16);
      a0 = MFMA(c, ((u32x4){mm, mm, 0u, 0u}), a0); a1 = MFMA(c, ((u32x4){0u, 0u, mm, mm}), a1); PIN(a0); PIN(a1); };
    auto glu = [&](const f32x16& acc, int i) AI { return acc[i + 8] * __builtin_amdgcn_rcpf(__builtin_fmaf(EX(acc[i] * kg), sig, sig)); };
    auto glu8 = [&](const f32x16& acc) AI { return (u32x4){pk(glu(acc, 0), glu(acc, 1)), pk(glu(acc, 2), glu(acc, 3)), pk(glu(acc, 4), glu(acc, 5)), pk(glu(acc, 6), glu(acc, 7))}; };
    const int hs2 = WS_H + (mt * 112 + ng * 4 + wn * 2) * 1024;
    u32x4 tb0 = {0u, 0u, 0u, 0u}, tb1 = tb0;
    auto epi1 = [&](const f32x16& acc, int t) AI {
      if (bq) {
        const u32x4 e = t ? tb1 : tb0; const unsigned o[3] = {e[0] ^ (r << 4), e[1] ^ (r << 4), e[2] ^ (r << 4)};
        const unsigned m[2] = {(unsigned)__builtin_amdgcn_sbfe(e[3], 0, 1), (unsigned)__builtin_amdgcn_sbfe(e[3], 1, 1)};
        const float sc = 2 * ng + wn < 7 ? rstd * (0.18898223650461362f * 1.4426950408889634f) : rstd;
        u32x4 v[2];
        UNROLL for (int pp = 0; pp < 2; ++pp)
          v[pp] = widen((u32x2){pk(acc[8 * pp] * sc, acc[8 * pp + 1] * sc), pk(acc[8 * pp + 2] * sc, acc[8 * pp + 3] * sc)},
                        (u32x2){pk(acc[8 * pp + 4] * sc, acc[8 * pp + 5] * sc), pk(acc[8 * pp + 6] * sc, acc[8 * pp + 7] * sc)});
        u32x4 d0 = {v[0][0], v[0][1], bfi(m[0], PAD0, v[0][2]), ~m[0] & v[0][3]}, d1 = {v[1][0], v[1][1], bfi(m[1], PAD0, v[1][2]), ~m[1] & v[1][3]};
        u32x4 d2 = {PAD0, 0u, bfi(m[0], v[0][2], v[1][2]), bfi(m[0], v[0][3], v[1][3])};
        PIN(d0); PIN(d1); PIN(d2); SBAR();
        __builtin_amdgcn_raw_buffer_store_b128(d0, wsr, o[0], mt * QT, SC1); __builtin_amdgcn_raw_buffer_store_b128(d1, wsr, o[1], mt * QT, SC1);
        st128(d2, wsr, o[2], mt * QT, SC1);
      } else st128(glu8(acc), wsr, lane * 16 + t * 1024, hs2 + 12 * 4096, SC1);
    };
    u32x4 hp, hq; unsigned fl0 = ~nonce; f32x16 sa, sb; u32x4 cfix;
    int fqo = 0;
    auto fq_off = [&]() AI {
      const int c0 = (lane < 8 ? 448 + (wid & 1) * 448 : 0) + head * 28 - (head & 1) * 4 + (lane & 1) * 31, g = c0 >> 7, m2 = lane < 8 ? (wid >> 1) * 4 + (lane >> 1) : qg;
      fqo = WS_FQ + (m2 * 16 + g) * 128; };
    const int sgo = WS_ST + (mg * 128 + wm * 32 + r) * 8;

    sfor<0, 70>([&](auto nc) AI {
      constexpr int n = decltype(nc)::v, u = n & 7;
      constexpr int k0 = n < 28 ? n : n < 56 ? n - 28 : 2 * (n - 56), k1 = n < 56 ? k0 : k0 + 1;
      f32x16& D0 = n < 28 ? A0 : n < 56 ? B0 : C0; f32x16& D1 = n < 28 ? A1 : n < 56 ? B1 : C0;
      constexpr bool early = n == 54 || n == 55 || n >= 68;
      PIN(wf[n % NWF][3]);
      if (u == 7) __builtin_amdgcn_s_setprio(3); if (u == 1) __builtin_amdgcn_s_setprio(2); if (u == 3) __builtin_amdgcn_s_setprio(1); if (u == 5) __builtin_amdgcn_s_setprio(0);
      SBAR();
      D0 = MFMA(cat(wf[n % NWF][0], wf[n % NWF][1]), xf[k0], D0); PIN(D0);
      SBAR();
      if constexpr (u == 6 || n == 68 || n == 2) {
        if constexpr (n == 54 || n == 68) { UNROLL for (int e = 0; e < RA; ++e) { colsum(n + e, 0); colsum(n + e, 1); } cs_out(); }
        if (n == 2) wait_bar<7>(); else if (n <= 14) wait_bar<2>(); else if (n == 22) wait_bar<4>(); else if (n == 30) wait_bar<6>();
        else if (n == 38) { if (bq) wait_bar<10>(); else wait_bar<6>(); } else if (n == 46) wait_bar<4>(); else if (n == 54) wait_bar<5>();
        else if (n == 62) lbar(); else wait_bar<4>();
        if (n == 46 && wid == 0) flag_st(ws, WS_FQ + (mg * 16 + ng) * 128, nonce);
        SBAR();
      }
      constexpr bool d1f = u == 6 || n == 68 || n == 2;
      if constexpr (d1f) { D1 = MFMA(cat(wf[n % NWF][2], wf[n % NWF][3]), xf[k1], D1); PIN(D1); SBAR(); }
      if constexpr (n + RA < 70) rd_w(n + RA);
      if constexpr (n == 68) {
        const lptr qb = lds + L_Q + (wid & 1) * 4096 + ((wid >> 1) & 1) * 2048 + lane * 16, qa = lds + L_Q + (wid & 1) * 4096 + (((wid >> 1) & 1) ^ 1) * 2048 + lane * 16;
        kf0 = ld128(lds + lane * 16); QA0 = ld128(qa); QB0 = ld128(qb); kf1 = ld128(lds + 1024 + lane * 16); QA1 = ld128(qa + 1024); QB1 = ld128(qb + 1024);
        cfix = ld128(lds + L_CS + wn * 1024 + lane * 16);
      }
      if constexpr (n == 69) { sa = MFMA(kf1, QA1, sa); sb = MFMA(kf1, QB1, sb); PIN(sa); PIN(sb); }
      if constexpr (n == 2) rd_x(4, 8);
      if constexpr (u == 6 && n < 24) rd_x(n + 2, n + 6);
      if constexpr (u == 0 && n > 0 && n < 24) rd_x(n + 4, n + 8);
      if constexpr (!early) colsum(n, 0);
      SBAR();
      if constexpr (!d1f) { D1 = MFMA(cat(wf[n % NWF][2], wf[n % NWF][3]), xf[k1], D1); PIN(D1); }
      SBAR();
      if constexpr (!early) colsum(n, 1);
      if constexpr (n == 0) dma_w(IC<2>{}); if constexpr (n == 1) dma_x(IC<3>{}); if constexpr (n == 2) dma_w(IC<3>{}); if constexpr (n == 4) dma_w(IC<4>{});
      if constexpr (n == 6) dma_x(IC<4>{}); if constexpr (n == 7) dma_x(IC<5>{}); if constexpr (n == 8) dma_w(IC<5>{}); if constexpr (n == 10) dma_w(IC<6>{});
      if constexpr (n == 14) dma_x(IC<6>{}); if constexpr (n == 15) dma_w(IC<7>{}); if constexpr (n == 18) dma_w(IC<8>{}); if constexpr (n == 20) dma_w(IC<9>{});
      if constexpr (n == 23) dma_w(IC<10>{}); if constexpr (n == 26) dma_w(IC<11>{}); if constexpr (n == 29) dma_w(IC<12>{}); if constexpr (n == 32) dma_w(IC<13>{});
      if constexpr (n == 48) touch(SRD(b < 196 ? wd : wo, b < 196 ? 1792 * 896 : 448 * 896), lane < 8 ? (((b < 196 ? b : b - 196) * 8 + wid) * 8 + lane) * 128 : OOB, lds0 + L_PF + wid * 256);
      if constexpr (n == 17) asm volatile("buffer_load_dwordx2 %0, %1, %2, 0 offen sc1" : "+v"(sg) : "v"(sgo), "s"(wsr) : "memory");
      if constexpr (n == 16) asm volatile("global_load_dwordx4 %0, %2, %3\n\tglobal_load_dwordx4 %1, %2, %3 offset:32" : "+v"(tb0), "+v"(tb1) : "v"(h * 16), "s"(&qkv_tab.v[bq ? 4 * ng + 2 * wn : 0]) : "memory");
      if constexpr (n >= 39 && n < 53 && (n & 1)) dma_w(IC<14 + (n - 39) / 2>{});
      if constexpr (n == 27) cs_out();
      if constexpr (n == 23) {
        PIN(sg); PIN(tb0); PIN(tb1);
        _Pragma("nounroll") for (unsigned sp = 0; !ALL(sg[1] == nonce) && sp < SPIN_CAP; ++sp)
          asm volatile("buffer_load_dwordx2 %0, %1, %2, 0 offen sc1\n\ts_waitcnt vmcnt(0)" : "+v"(sg) : "v"(sgo), "s"(wsr) : "memory");
        sig = U2F(sg[0] & 0xffff0000u); rstd = __builtin_amdgcn_rcpf(sig); mm = __builtin_amdgcn_perm(sg[0], sg[0], 0x01000100u); kg = -1.4426950408889634f * rstd;
        PIN(sig); PIN(rstd); PIN(kg); PIN(mm);
      }
      if constexpr (n == 30) fix2(A0, A1);
      if constexpr (n == 31) epi1(A0, 0);
      if constexpr (n == 34) epi1(A1, 1);
      if constexpr (n == FQP) { fq_off(); asm volatile("buffer_load_dword %0, %1, %2, 0 offen sc1" : "+v"(fl0) : "v"(fqo), "s"(wsr) : "memory"); }
      if constexpr (n == 55) fix2(B0, B1);
      if constexpr (n == 57) { hp = glu8(B0); PIN(hp); }
      if constexpr (n == 64) { hq = glu8(B1); PIN(hq); }
      if constexpr (n == 69) { st128(hp, wsr, lane * 16, hs2, SC1); st128(hq, wsr, lane * 16 + 1024, hs2, SC1); }
      if constexpr (n == FQC) {
        asm volatile("s_waitcnt vmcnt(0)" : "+v"(fl0) :: "memory");
        _Pragma("nounroll") for (unsigned sp = 0; !ALL(fl0 == nonce) && sp < SPIN_CAP; ++sp)
          asm volatile("buffer_load_dword %0, %1, %2, 0 offen sc1\n\ts_waitcnt vmcnt(0)" : "+v"(fl0) : "v"(fqo), "s"(wsr) : "memory");
        kvv = (unsigned)(WS_QKV + head * QH + (wid >> 1) * 16 * QT + 2048 + (wid & 1) * 2048 + lane * 16);
        dma<0, L_Q, true>(wsr, WS_QKV + head * QH + (qg * 4 + (wid >> 1)) * QT + (wid & 1) * 1024 + lane * 16, lds0 + wid * 1024); dma_kv(IC<0>{}, kvv); }
      if constexpr (n > FQC && n < FQC + 8) dma_kv(IC<n - FQC>{}, kvv);
      if constexpr (n == 67) {
        const int vc = 2 * (grp ^ wn) + (p >> 1);
        kb = lds + (wid >> 1) * 4096 + lane * 16;
        vb0 = lds + (wid >> 1) * 4096 + 2048 + vc * 512 + ((((4 * h) ^ (4 * vc)) + q) << 4) + (p & 1) * 8;
        vb1 = lds + (wid >> 1) * 4096 + 2048 + vc * 512 + ((((4 * h + 8) ^ (4 * vc)) + q) << 4) + (p & 1) * 8;
        PIN(kb); PIN(vb0); PIN(vb1);
      }
      if constexpr (n == 68) { sa = MFMA(kf0, QA0, Z); sb = MFMA(kf0, QB0, Z); PIN(sa); PIN(sb); ka0 = ld128(kb); ka1 = ld128(kb + 1024); va0 = cat(trd(vb0), trd(vb1)); }
    });
    SBAR();
    C0 = MFMA(cfix, ((u32x4){mm, mm, mm, mm}), C0);
    float ma = sa[0], mb = sb[0];
    UNROLL for (int i = 1; i < 16; ++i) { ma = __builtin_fmaxf(ma, sa[i]); mb = __builtin_fmaxf(mb, sb[i]); }
    const unsigned pa = pk(1.f - max32(ma), 0.f), pb = pk(1.f - max32(mb), 0.f);
    const bool p0 = (head & 1) && h == 0, p1 = !(head & 1) && h == 1;
    QA0[0] = p0 ? pa : QA0[0]; QA1[2] = p1 ? pa : QA1[2]; QB0[0] = p0 ? pb : QB0[0]; QB1[2] = p1 ? pb : QB1[2];
    s0 = MFMA(ka0, QA0, Z); s0 = MFMA(ka1, QA1, s0);
    PIN(s0); PIN(QB0); PIN(QB1);
    SBAR();
    st128(glu8(C0), wsr, ng < 14 ? lane * 16 : OOB, WS_H + (mt * 112 + 64 + ng * 2 + wn) * 1024, SC1);
    SBAR();
  }
  return phase23(x, wo, wd, ws, out, lds, nonce, QA0, QA1, QB0, QB1, ka0, ka1, va0, s0, kb, vb0, vb1, kvv, wid);
}

extern "C" void kernel_launch(void* const* d_in, const int* in_sizes, int n_in, void* d_out, int out_size, void* d_ws, size_t ws_size,
                              hipStream_t stream) {
  (void)hipFuncSetAttribute((const void*)fused_kernel, hipFuncAttributeMaxDynamicSharedMemorySize, LDS_BYTES);
  fused_kernel<<<256, 512, LDS_BYTES, stream>>>((const char*)d_in[0], (const char*)d_in[1], (const char*)d_in[2], (const char*)d_in[4], (const char*)d_in[5],
                                                (const char*)d_in[6], (char*)d_ws, (char*)d_out);
}
```
